```python
import torch

def _softmax(x, axis=-1):
    return torch.softmax(x, dim=axis)
def _gelu(x):
    return torch.nn.functional.gelu(x, approximate="tanh")
def _silu(x):
    return torch.nn.functional.silu(x)
def _sigmoid(x):
    return torch.sigmoid(x)
def _layernorm(x, axis=-1, eps=1e-5):
    m = x.mean(dim=axis, keepdim=True)
    v = x.var(dim=axis, keepdim=True, unbiased=False)
    return (x - m) / torch.sqrt(v + eps)
def _scatter_add(tgt, idx, src, axis=0):
    return tgt.clone().index_add_(axis, idx, src)
def _segment_sum(src, seg, n):
    out = torch.zeros((n,) + src.shape[1:], dtype=src.dtype)
    return out.index_add_(0, seg, src)
def _pad(x, pw):
    flat = tuple(v for pair in reversed(pw) for v in pair)
    return torch.nn.functional.pad(x, flat)

kernel_name = 'standalone__gqa_softmax__near_ridge'

def setup_inputs():

    _w = (lambda a: (lambda t: t.to(torch.bfloat16) if t.is_floating_point() else t)(torch.as_tensor(a)))
    B, Hq, Hkv, Sq, Sk, D = 8, 32, 16, 336, 1024, 128
    return {
        "q": _w((lambda *s: torch.randn(*s, dtype=torch.bfloat16) * 0.8)(B, Hq, Sq, D)),
        "k": _w((lambda *s: torch.randn(*s, dtype=torch.bfloat16) * 0.8)(B, Hkv, Sk, D)),
        "v": _w((lambda *s: torch.randn(*s, dtype=torch.bfloat16) * 0.05)(B, Hkv, Sk, D)),
    }

def reference(q, k, v):
    B, Hq, Sq, D = q.shape
    Hkv = k.shape[1]
    rep = Hq // Hkv
    k = (lambda x, n, axis: torch.repeat_interleave(x, n, dim=axis))(k, rep, axis=1)
    v = (lambda x, n, axis: torch.repeat_interleave(x, n, dim=axis))(v, rep, axis=1)
    scale = D ** -0.5
    scores = torch.einsum("bhqd,bhkd->bhqk", q, k) * scale
    p = _softmax(scores, axis=-1)
    return torch.einsum("bhqk,bhkd->bhqd", p, v)
```

```cpp
#include <hip/hip_runtime.h>
#include <hip/hip_bf16.h>
#include <cstdio>

using bf16 = __hip_bfloat16;
constexpr int   D = 128, NW = 8, QBLK = 32, KVBLK = 64;
constexpr float SCALE = 0.088388347648318440f;
#define ATTN_THR 8.f
constexpr float THR = ATTN_THR;
#define ATTN_LAYOUT 0
constexpr int LAYOUT = ATTN_LAYOUT;
#define ATTN_SDEPTH 2
constexpr int SDEPTH = ATTN_SDEPTH;
static_assert(SDEPTH == 1 || SDEPTH == 2, "SDEPTH is 1 or 2");
#define ATTN_B 8
#define ATTN_HQ 32
#define ATTN_HKV 16
#define ATTN_S 512
#define ATTN_SKV 1024
constexpr int B = ATTN_B, HQ = ATTN_HQ, HKV = ATTN_HKV, S = ATTN_S, SKV = ATTN_SKV;
constexpr int LDQ = LAYOUT ? HQ * D : D, LDK = LAYOUT ? HKV * D : D, LDO = LDQ;
static_assert(S % (QBLK * NW) == 0 && SKV % (2 * KVBLK) == 0 && HQ % HKV == 0, "S % 256 == 0, SKV % 128 == 0 (even tile count), HQ % HKV == 0");
constexpr size_t SHM_V = KVBLK * D * 2, SHM_K = KVBLK * D * 2, SHM_ATTN = 2 * SHM_V + 2 * SHM_K + NW * 64 * 4;

using bf16x8 = __attribute__((ext_vector_type(8))) short;
using s16x4  = __attribute__((ext_vector_type(4))) short;
using f32x16 = __attribute__((ext_vector_type(16))) float;
using f32x8  = __attribute__((ext_vector_type(8))) float;
using u32x4  = __attribute__((ext_vector_type(4))) unsigned;
#define KSWZ(row, colB) ((row) * 256 + ((colB) ^ (((row) & 7) << 4)))
#define SBAR() __builtin_amdgcn_sched_barrier(0)
__device__ __forceinline__ int crow(int r, int hi) { return (r & 3) + 8 * (r >> 2) + 4 * hi; }
__device__ __forceinline__ unsigned cvtpk(float lo, float hi) {
  unsigned r; asm volatile("v_cvt_pk_bf16_f32 %0, %1, %2" : "=v"(r) : "v"(lo), "v"(hi)); return r;
}
template <typename TIn> struct Stage;
template <> struct Stage<bf16>  { using T = bf16x8;
  __device__ static __forceinline__ T ld8(const bf16* p) { return *reinterpret_cast<const bf16x8*>(p); }
  __device__ static __forceinline__ bf16x8 tobf(T x) { return x; } };
template <> struct Stage<float> { using T = f32x8;
  __device__ static __forceinline__ T ld8(const float* p) { return *reinterpret_cast<const f32x8*>(p); }
  __device__ static __forceinline__ bf16x8 tobf(T x) {
    u32x4 w = {cvtpk(x[0], x[1]), cvtpk(x[2], x[3]), cvtpk(x[4], x[5]), cvtpk(x[6], x[7])}; return *reinterpret_cast<bf16x8*>(&w); } };

__device__ __forceinline__ void partialSM(f32x16& p0, f32x16& p1, float& m_reg, float& mn, float& alpha) {
  constexpr float C = SCALE * 1.4426950408889634f;
  float pmax = p0[0]; for (int r = 1; r < 16; ++r) pmax = fmaxf(pmax, p0[r]); for (int r = 0; r < 16; ++r) pmax = fmaxf(pmax, p1[r]);
  { auto rr = __builtin_amdgcn_permlane32_swap(__float_as_uint(pmax), __float_as_uint(pmax), false, false);
    pmax = fmaxf(__uint_as_float(rr[0]), __uint_as_float(rr[1])); }
  if (__builtin_expect(__all(pmax - m_reg <= THR / SCALE), 1)) { mn = m_reg; alpha = 1.f; }
  else { mn = fmaxf(m_reg, pmax); alpha = __builtin_amdgcn_exp2f((m_reg - mn) * C); m_reg = mn; }
  float mnC = -mn * C;
  for (int r = 0; r < 16; ++r) p0[r] = fmaf(p0[r], C, mnC); for (int r = 0; r < 16; ++r) p1[r] = fmaf(p1[r], C, mnC);
  for (int r = 0; r < 16; ++r) p0[r] = __builtin_amdgcn_exp2f(p0[r]);
}
__device__ __forceinline__ void finishSM(f32x16& p0, f32x16& p1, float alpha, float& l_reg, bf16x8& pa0, bf16x8& pa1, bf16x8& pa2, bf16x8& pa3) {
  for (int r = 0; r < 16; ++r) p1[r] = __builtin_amdgcn_exp2f(p1[r]);
  float ps = 0; for (int r = 0; r < 16; ++r) ps += p0[r]; for (int r = 0; r < 16; ++r) ps += p1[r];
  { auto rr = __builtin_amdgcn_permlane32_swap(__float_as_uint(ps), __float_as_uint(ps), false, false);
    ps = __uint_as_float(rr[0]) + __uint_as_float(rr[1]); }
  l_reg = l_reg * alpha + ps;
#define PK4(P, BASE, OUT) do { unsigned a0 = cvtpk(P[BASE + 0], P[BASE + 1]), a1 = cvtpk(P[BASE + 2], P[BASE + 3]);   \
    unsigned b0 = cvtpk(P[BASE + 4], P[BASE + 5]), b1 = cvtpk(P[BASE + 6], P[BASE + 7]);                              \
    auto r0 = __builtin_amdgcn_permlane32_swap(a0, b0, false, false); auto r1 = __builtin_amdgcn_permlane32_swap(a1, b1, false, false); \
    u32x4 w = {r0[0], r1[0], r0[1], r1[1]}; OUT = *reinterpret_cast<bf16x8*>(&w); } while (0)
  PK4(p0, 0, pa0); PK4(p0, 8, pa1); PK4(p1, 0, pa2); PK4(p1, 8, pa3);
#undef PK4
}
__device__ __forceinline__ void qkt(f32x16& p0, f32x16& p1, const bf16* Ks, const bf16x8* qr, int r32, int hi) {
  p0 = f32x16{}; p1 = f32x16{};
  for (int d0 = 0; d0 < 8; ++d0) { int cb = (d0 * 16 + hi * 8) * 2;
    bf16x8 b0 = *reinterpret_cast<const bf16x8*>((const char*)Ks + KSWZ(r32, cb));
    bf16x8 b1 = *reinterpret_cast<const bf16x8*>((const char*)Ks + KSWZ(32 + r32, cb));
    p0 = __builtin_amdgcn_mfma_f32_32x32x16_bf16(b0, qr[d0], p0, 0, 0, 0);
    p1 = __builtin_amdgcn_mfma_f32_32x32x16_bf16(b1, qr[d0], p1, 0, 0, 0); }
}
__device__ __forceinline__ int v_st(int k, int c) { const int kk = (k & ~0xC) | ((k & 4) << 1) | ((k & 8) >> 1); return ((kk >> 3) * 4 + (c >> 5)) * 512 + ((kk & 7) * 32 + (c & 31)) * 2; }
__device__ __forceinline__ int v_rd_base(int lane) { return ((lane & 3) << 3) | (((lane >> 2) & 3) << 6) | (((lane >> 4) & 1) << 5) | (((lane >> 5) & 1) << 8); }
constexpr int v_rd_off(int d0, int ks, int half) { return d0 * 512 + ks * 4096 + half * 2048; }
template <int OFF> __device__ __forceinline__ s16x4 tr_read(int vb) {
  s16x4 r; asm volatile("ds_read_b64_tr_b16 %0, %1 offset:%2" : "=&v"(r) : "v"(vb), "i"(OFF) : "memory"); return r;
}
template <int D0> __device__ __forceinline__ void pv_one(f32x16& od, int vb, bf16x8 pa0, bf16x8 pa1, bf16x8 pa2, bf16x8 pa3) {
  const s16x4 l0 = tr_read<v_rd_off(D0, 0, 0)>(vb), h0 = tr_read<v_rd_off(D0, 0, 1)>(vb), l1 = tr_read<v_rd_off(D0, 1, 0)>(vb), h1 = tr_read<v_rd_off(D0, 1, 1)>(vb);
  const s16x4 l2 = tr_read<v_rd_off(D0, 2, 0)>(vb), h2 = tr_read<v_rd_off(D0, 2, 1)>(vb), l3 = tr_read<v_rd_off(D0, 3, 0)>(vb), h3 = tr_read<v_rd_off(D0, 3, 1)>(vb);
  asm volatile("s_waitcnt lgkmcnt(0)" ::: "memory"); SBAR();
#define PK(L, H) (bf16x8){L[0], L[1], L[2], L[3], H[0], H[1], H[2], H[3]}
  od = __builtin_amdgcn_mfma_f32_32x32x16_bf16(pa0, PK(l0, h0), od, 0, 0, 0);
  od = __builtin_amdgcn_mfma_f32_32x32x16_bf16(pa1, PK(l1, h1), od, 0, 0, 0);
  od = __builtin_amdgcn_mfma_f32_32x32x16_bf16(pa2, PK(l2, h2), od, 0, 0, 0);
  od = __builtin_amdgcn_mfma_f32_32x32x16_bf16(pa3, PK(l3, h3), od, 0, 0, 0);
#undef PK
}
__device__ __forceinline__ void pv_d0(f32x16* o, int vb, bf16x8 pa0, bf16x8 pa1, bf16x8 pa2, bf16x8 pa3) {
  pv_one<0>(o[0], vb, pa0, pa1, pa2, pa3); pv_one<1>(o[1], vb, pa0, pa1, pa2, pa3); pv_one<2>(o[2], vb, pa0, pa1, pa2, pa3); pv_one<3>(o[3], vb, pa0, pa1, pa2, pa3);
}

template <typename TQ>
__device__ __forceinline__ void attn_dense_body(const TQ* __restrict__ Qb, const bf16* __restrict__ Kh, const bf16* __restrict__ Vh,
                                                bf16* __restrict__ Ob, int seq, char* lds) {
  using St = Stage<bf16>; using SQ = Stage<TQ>;
  const int tid = threadIdx.x, wid = tid >> 6, lane = tid & 63, r32 = lane & 31, hi = lane >> 5;
  bf16* V_lds = (bf16*)lds; bf16* K_lds = (bf16*)(lds + 2 * SHM_V);
  float* ws = (float*)(lds + 2 * SHM_V + 2 * SHM_K) + wid * 64; float* li_l = ws; float* al_l = ws + 32;
  float m_reg = -1e30f, l_reg = 0; f32x16 o[4] = {}; bf16x8 qr[8];
  const TQ* Qw = Qb + (long)(wid * QBLK + r32) * LDQ + hi * 8;
#pragma unroll
  for (int d0 = 0; d0 < 8; ++d0) qr[d0] = SQ::tobf(SQ::ld8(Qw + d0 * 16));
  const int sr = tid >> 4, sc = (tid & 15) * 8, vst0 = v_st(sr, sc), vst1 = v_st(32 + sr, sc);
  const int vb0 = (int)(uintptr_t)V_lds + v_rd_base(lane);
  struct { typename St::T vs0, vs1, ks0, ks1; } sr_[SDEPTH];
#define SLOAD(i, k0) do { sr_[i].vs0 = St::ld8(&Vh[(long)((k0) + sr) * LDK + sc]); sr_[i].vs1 = St::ld8(&Vh[(long)((k0) + 32 + sr) * LDK + sc]); \
    sr_[i].ks0 = St::ld8(&Kh[(long)((k0) + sr) * LDK + sc]); sr_[i].ks1 = St::ld8(&Kh[(long)((k0) + 32 + sr) * LDK + sc]); } while (0)
#define SWRITE(b, i) do { *(bf16x8*)((char*)V_lds + (b) * SHM_V + vst0) = St::tobf(sr_[i].vs0);          \
    *(bf16x8*)((char*)V_lds + (b) * SHM_V + vst1) = St::tobf(sr_[i].vs1); int kc = sc * 2;               \
    *(bf16x8*)((char*)K_lds + (b) * SHM_K + KSWZ(sr, kc)) = St::tobf(sr_[i].ks0);                       \
    *(bf16x8*)((char*)K_lds + (b) * SHM_K + KSWZ(32 + sr, kc)) = St::tobf(sr_[i].ks1); } while (0)
#define SWAIT() do { if constexpr (SDEPTH == 2) asm volatile("s_waitcnt vmcnt(4)" ::: "memory"); else asm volatile("s_waitcnt vmcnt(0)" ::: "memory"); } while (0)
#define RESC(a) do { if (__any((a) < 1.f)) { if (hi == 0) al_l[r32] = (a); asm volatile("s_waitcnt lgkmcnt(0)" ::: "memory"); \
    for (int d = 0; d < 4; ++d) for (int r = 0; r < 16; ++r) o[d][r] *= al_l[crow(r, hi)]; } } while (0)
  f32x16 pA0, pA1, pB0, pB1; float mnA, mnB, alA, alB; bf16x8 pa0, pa1, pa2, pa3; const int NT = seq / KVBLK;
  constexpr int SE = 0, SO = SDEPTH - 1;
  SLOAD(SE, 0); asm volatile("s_waitcnt vmcnt(0)" ::: "memory"); SWRITE(0, SE); __syncthreads();
  qkt(pA0, pA1, K_lds, qr, r32, hi); partialSM(pA0, pA1, m_reg, mnA, alA);
  SLOAD(SO, KVBLK); if constexpr (SDEPTH == 2) { if (2 < NT) SLOAD(SE, 2 * KVBLK); }
  SWAIT(); SWRITE(1, SO); __syncthreads();
  for (int j = 1; j + 1 < NT; j += 2) {
    SBAR(); qkt(pB0, pB1, (bf16*)((char*)K_lds + SHM_K), qr, r32, hi);
    finishSM(pA0, pA1, alA, l_reg, pa0, pa1, pa2, pa3); SBAR();
    SLOAD(SO, (j + SDEPTH) * KVBLK); SBAR();
    pv_d0(o, vb0, pa0, pa1, pa2, pa3); partialSM(pB0, pB1, m_reg, mnB, alB);
    __syncthreads(); SWAIT(); SWRITE(0, SE);
    RESC(alB); __syncthreads();
    SBAR(); qkt(pA0, pA1, K_lds, qr, r32, hi);
    finishSM(pB0, pB1, alB, l_reg, pa0, pa1, pa2, pa3); SBAR();
    if (SDEPTH == 1 || j + 3 < NT) SLOAD(SE, (j + 1 + SDEPTH) * KVBLK); SBAR();
    pv_d0(o, vb0 + (int)SHM_V, pa0, pa1, pa2, pa3); partialSM(pA0, pA1, m_reg, mnA, alA);
    __syncthreads(); SWAIT(); SWRITE(1, SO);
    RESC(alA); __syncthreads();
  }
  SBAR(); qkt(pB0, pB1, (bf16*)((char*)K_lds + SHM_K), qr, r32, hi);
  finishSM(pA0, pA1, alA, l_reg, pa0, pa1, pa2, pa3); SBAR();
  pv_d0(o, vb0, pa0, pa1, pa2, pa3); partialSM(pB0, pB1, m_reg, mnB, alB);
  __syncthreads(); RESC(alB);
  finishSM(pB0, pB1, alB, l_reg, pa0, pa1, pa2, pa3); SBAR();
  pv_d0(o, vb0 + (int)SHM_V, pa0, pa1, pa2, pa3);
  if (hi == 0) li_l[r32] = l_reg; asm volatile("s_waitcnt lgkmcnt(0)" ::: "memory");
  float rli[16];
#pragma unroll
  for (int r = 0; r < 16; ++r) rli[r] = __builtin_amdgcn_rcpf(li_l[crow(r, hi)]);
  bf16* Ow = Ob + (long)(wid * QBLK) * LDO;
#pragma unroll
  for (int r = 0; r < 16; ++r) { int orow = crow(r, hi);
    for (int d0 = 0; d0 < 4; ++d0) Ow[(long)orow * LDO + d0 * 32 + r32] = __float2bfloat16(o[d0][r] * rli[r]); }
#undef SLOAD
#undef SWRITE
#undef SWAIT
#undef RESC
}

__global__ __launch_bounds__(64 * NW, 1)
void attn_fwd_dense(const bf16* __restrict__ Q, const bf16* __restrict__ K, const bf16* __restrict__ V, bf16* __restrict__ O) {
  extern __shared__ __attribute__((aligned(16))) char shm[];
  const long b = blockIdx.z, h = blockIdx.y, kvh = h / 2, i0 = blockIdx.x ? 80 : 0;
  const long q0 = ((b * 32 + h) * 336 + i0) * D, k0 = (b * 16 + kvh) * 1024 * (long)D;
  attn_dense_body<bf16>(Q + q0, K + k0, V + k0, O + q0, 1024, shm);
}
extern "C" void kernel_launch(void* const* d_in, const int* in_sizes, int n_in, void* d_out, int out_size, void* d_ws, size_t ws_size, hipStream_t stream) {
  hipFuncSetAttribute((const void*)attn_fwd_dense, hipFuncAttributeMaxDynamicSharedMemorySize, (int)SHM_ATTN);
  hipLaunchKernelGGL(attn_fwd_dense, dim3(2, 32, 8), dim3(64 * NW), SHM_ATTN, stream,
                     (const bf16*)d_in[0], (const bf16*)d_in[1], (const bf16*)d_in[2], (bf16*)d_out);
}
```

```cpp
#include <hip/hip_runtime.h>
#define GQA_STAMPS 0

__global__ __launch_bounds__(256, 1) void gqa_attn_fwd(const char* __restrict__ q, const char* __restrict__ k, const char* __restrict__ v,
                                                       char* __restrict__ o, char* __restrict__ dbg) {
  __shared__ __attribute__((aligned(1024))) char lds[163840];
  asm volatile(
    "s_getpc_b64 s[76:77]\n"
    ".Lgqa_PC0:\n"
    "v_lshlrev_b32 v5, 7, %2\n"
    "v_min_u32 v5, .Lgqa_HOTEND-.Lgqa_PC0, v5\n"
    "global_load_dword v9, v5, s[76:77]\n"
    "s_load_dwordx8 s[8:15], %0, 0x0\n"
    "v_readfirstlane_b32 s56, %2\n"
    "v_and_b32 v1, 63, %2\n"
    "s_and_b32 s72, %1, 7\n"
    "s_lshr_b32 s73, %1, 3\n"
    "s_lshl_b32 s74, s72, 5\n"
    "s_add_u32 s74, s74, s73\n"
    "s_mul_i32 s74, s74, 86016\n"
    "s_lshl_b32 s75, s72, 4\n"
    "s_lshr_b32 s73, s73, 1\n"
    "s_add_u32 s75, s75, s73\n"
    "s_lshl_b32 s75, s75, 18\n"
    "s_lshr_b32 s56, s56, 6\n"
    "v_bfe_u32 v2, v1, 2, 3\n"
    "v_lshlrev_b32 v2, 8, v2\n"
    "v_lshrrev_b32 v3, 5, v1\n"
    "v_lshl_add_u32 v2, v3, 6, v2\n"
    "v_and_b32 v4, 3, v1\n"
    "v_lshl_add_u32 v211, v4, 4, v2\n"
    "v_xor_b32_e64 v212, 32, v211\n"
    "s_lshl_b32 s63, s56, 13\n"
    "s_mov_b32 s57, s63\n"
    "s_mov_b32 s60, s63\n"
    "s_add_u32 s61, s63, 0x1000\n"
    "s_mov_b32 s42, 0x40000\n"
    "s_mov_b32 s43, 0x20000\n"
    "s_mov_b32 s50, 0x15000\n"
    "s_mov_b32 s51, 0x20000\n"
    "s_mul_i32 s76, s56, 20480\n"
    "s_and_b32 s79, s56, 1\n"
    "s_lshl_b32 s80, s79, 5\n"
    "v_xor_b32 v44, s80, v211\n"
    "s_lshl_b32 s79, s79, 11\n"
    "s_lshr_b32 s80, s56, 1\n"
    "s_lshl_b32 s81, s80, 10\n"
    "s_add_u32 s81, s81, s79\n"
    "s_lshl_b32 s80, s80, 7\n"
    "s_add_u32 s80, s80, s79\n"
    "s_add_u32 s80, s80, 81920\n"
    "s_getreg_b32 s18, hwreg(HW_REG_XCC_ID, 0, 3)\n"
    "s_bitcmp0_b32 0x55, s18\n"
    "s_cbranch_scc1 .Lgqa_XD\n"
    "s_sleep 20\n"
    ".Lgqa_XD:\n"
    "s_waitcnt lgkmcnt(0)\n"
    "s_add_u32 s48, s8, s74\n"
    "s_addc_u32 s49, s9, 0\n"
    "s_and_b32 s49, s49, 0xffff\n"
    "s_add_u32 m0, s63, 0x8000\n"
    "s_add_u32 s77, s76, 0\n"
    "buffer_load_dwordx4 v211, s[48:51], s77 offen offset:0 nt lds\n"
    "buffer_load_dwordx4 v212, s[48:51], s77 offen offset:2048 nt lds\n"
    "s_add_u32 m0, s63, 0x8380\n"
    "s_nop 0\n"
    "buffer_load_dwordx4 v211, s[48:51], s77 offen offset:128 nt lds\n"
    "buffer_load_dwordx4 v212, s[48:51], s77 offen offset:2176 nt lds\n"
    "s_add_u32 m0, s81, 0x21000\n"
    "s_nop 0\n"
    "buffer_load_dwordx4 v44, s[48:51], s80 offen offset:0 nt lds\n"
    "s_add_u32 s40, s10, s75\n"
    "s_addc_u32 s41, s11, 0\n"
    "s_and_b32 s41, s41, 0xffff\n"
    "s_add_u32 m0, s57, 0x0\n"
    "s_nop 0\n"
    "buffer_load_dwordx4 v211, s[40:43], s60 offen offset:0 nt lds\n"
    "buffer_load_dwordx4 v212, s[40:43], s60 offen offset:2048 nt lds\n"
    "s_add_u32 m0, s57, 0x380\n"
    "s_nop 0\n"
    "buffer_load_dwordx4 v211, s[40:43], s60 offen offset:128 nt lds\n"
    "buffer_load_dwordx4 v212, s[40:43], s60 offen offset:2176 nt lds\n"
    "s_add_u32 m0, s57, 0x1000\n"
    "s_nop 0\n"
    "buffer_load_dwordx4 v211, s[40:43], s61 offen offset:0 nt lds\n"
    "buffer_load_dwordx4 v212, s[40:43], s61 offen offset:2048 nt lds\n"
    "s_add_u32 m0, s57, 0x1380\n"
    "s_nop 0\n"
    "buffer_load_dwordx4 v211, s[40:43], s61 offen offset:128 nt lds\n"
    "buffer_load_dwordx4 v212, s[40:43], s61 offen offset:2176 nt lds\n"
    "s_add_u32 s44, s12, s75\n"
    "s_addc_u32 s45, s13, 0\n"
    "s_and_b32 s45, s45, 0xffff\n"
    "s_mov_b32 s46, 0x40000\n"
    "s_mov_b32 s47, 0x20000\n"
    "s_add_u32 s52, s14, s74\n"
    "s_addc_u32 s53, s15, 0\n"
    "s_and_b32 s53, s53, 0xffff\n"
    "s_mov_b32 s54, 0x15000\n"
    "s_mov_b32 s55, 0x20000\n"
    "v_bfe_u32 v5, v1, 4, 1\n"
    "v_lshlrev_b32 v6, 5, v5\n"
    "v_xor_b32 v213, v211, v6\n"
    "v_bfe_u32 v2, v1, 3, 1\n"
    "v_lshlrev_b32 v7, 1, v2\n"
    "v_lshlrev_b32 v2, 11, v2\n"
    "v_lshrrev_b32 v6, 4, v1\n"
    "v_xor_b32 v6, v6, v7\n"
    "v_lshl_add_u32 v2, v6, 4, v2\n"
    "v_and_b32 v7, 7, v1\n"
    "v_lshl_add_u32 v2, v7, 6, v2\n"
    "v_add_u32_e64 v208, s63, v2\n"
    "v_add_u32 v217, 0x18000, v208\n"
    "v_add_u32 v218, 0x8000, v208\n"
    "v_add_u32 v219, 0x21000, v2\n"
    "s_add_u32 s78, s63, 0x10000\n"
    "s_xor_b32 s62, s63, 0x8000\n"
    "v_lshlrev_b32_e64 v2, 4, v4\n"
    "v_bfe_u32 v6, v1, 2, 2\n"
    "v_lshl_add_u32 v2, v6, 6, v2\n"
    "v_mul_u32_u24 v5, 288, v5\n"
    "v_xor_b32 v2, v2, v5\n"
    "v_lshl_add_u32 v2, v3, 11, v2\n"
    "v_add_u32 v209, s78, v2\n"
    "v_xor_b32 v210, 0x2000, v209\n"
    "v_and_b32 v2, 15, v1\n"
    "v_lshlrev_b32 v2, 8, v2\n"
    "v_lshrrev_b32 v6, 4, v1\n"
    "v_lshl_add_u32 v215, v6, 4, v2\n"
    "v_mov_b32 v252, 0x3f803f80\n"
    "v_mov_b32 v253, v252\n"
    "v_mov_b32 v254, v252\n"
    "v_mov_b32 v255, v252\n"
    "s_mov_b32 s67, 0x3e0293ee\n"
    "s_mov_b32 s64, 0x47800000\n"
    "s_mov_b32 s66, 0x3e0293ee\n"
    "v_bfe_u32 v175, v211, 4, 1\n"
    "v_mul_u32_u24 v175, 0xaaaa, v175\n"
    "v_add_u32 v175, 0x4444, v175\n"
    "s_mov_b64 exec, 0\n"
    "s_cmp_eq_u32 s56, 0\n"
    "s_cbranch_scc1 .Lgqa_T0\n"
    "s_cmp_eq_u32 s56, 1\n"
    "s_cbranch_scc1 .Lgqa_T1\n"
    "s_cmp_eq_u32 s56, 2\n"
    "s_cbranch_scc1 .Lgqa_T2\n"
    "s_branch .Lgqa_T3\n"
    ".Lgqa_TBACK:\n"
    "s_mov_b64 exec, -1\n"
    "s_waitcnt vmcnt(9)\n"
    "ds_read_b128 v[176:179], v218 offset:0\n"
    "ds_read_b128 v[180:183], v218 offset:512\n"
    "ds_read_b128 v[184:187], v218 offset:1024\n"
    "ds_read_b128 v[188:191], v218 offset:1536\n"
    "s_waitcnt lgkmcnt(3)\n"
    "v_lshlrev_b32 v192, 16, v176\n"
    "v_and_b32 v193, 0xffff0000, v176\n"
    "v_lshlrev_b32 v194, 16, v177\n"
    "v_and_b32 v195, 0xffff0000, v177\n"
    "v_lshlrev_b32 v196, 16, v178\n"
    "v_and_b32 v197, 0xffff0000, v178\n"
    "v_lshlrev_b32_e64 v198, 16, v179\n"
    "v_and_b32 v199, 0xffff0000, v179\n"
    "v_pk_mul_f32 v[192:193], v[192:193], s[66:67]\n"
    "v_pk_mul_f32 v[194:195], v[194:195], s[66:67]\n"
    "v_pk_mul_f32 v[196:197], v[196:197], s[66:67]\n"
    "v_pk_mul_f32 v[198:199], v[198:199], s[66:67]\n"
    "v_cvt_pk_bf16_f32 v192, v192, v193\n"
    "v_cvt_pk_bf16_f32 v194, v194, v195\n"
    "v_cvt_pk_bf16_f32 v196, v196, v197\n"
    "v_cvt_pk_bf16_f32 v198, v198, v199\n"
    "v_accvgpr_write_b32 a160, v192\n"
    "v_accvgpr_write_b32 a161, v194\n"
    "v_accvgpr_write_b32 a162, v196\n"
    "v_accvgpr_write_b32 a163, v198\n"
    "s_waitcnt lgkmcnt(2)\n"
    "v_lshlrev_b32 v200, 16, v180\n"
    "v_and_b32 v201, 0xffff0000, v180\n"
    "v_lshlrev_b32 v202, 16, v181\n"
    "v_and_b32 v203, 0xffff0000, v181\n"
    "v_lshlrev_b32 v204, 16, v182\n"
    "v_and_b32 v205, 0xffff0000, v182\n"
    "v_lshlrev_b32_e64 v206, 16, v183\n"
    "v_and_b32 v207, 0xffff0000, v183\n"
    "v_pk_mul_f32 v[200:201], v[200:201], s[66:67]\n"
    "v_pk_mul_f32 v[202:203], v[202:203], s[66:67]\n"
    "v_pk_mul_f32 v[204:205], v[204:205], s[66:67]\n"
    "v_pk_mul_f32 v[206:207], v[206:207], s[66:67]\n"
    "v_cvt_pk_bf16_f32 v200, v200, v201\n"
    "v_cvt_pk_bf16_f32 v202, v202, v203\n"
    "v_cvt_pk_bf16_f32 v204, v204, v205\n"
    "v_cvt_pk_bf16_f32 v206, v206, v207\n"
    "v_accvgpr_write_b32 a164, v200\n"
    "v_accvgpr_write_b32 a165, v202\n"
    "v_accvgpr_write_b32 a166, v204\n"
    "v_accvgpr_write_b32 a167, v206\n"
    "s_waitcnt lgkmcnt(1)\n"
    "v_lshlrev_b32 v192, 16, v184\n"
    "v_and_b32 v193, 0xffff0000, v184\n"
    "v_lshlrev_b32 v194, 16, v185\n"
    "v_and_b32 v195, 0xffff0000, v185\n"
    "v_lshlrev_b32 v196, 16, v186\n"
    "v_and_b32 v197, 0xffff0000, v186\n"
    "v_lshlrev_b32_e64 v198, 16, v187\n"
    "v_and_b32 v199, 0xffff0000, v187\n"
    "v_pk_mul_f32 v[192:193], v[192:193], s[66:67]\n"
    "v_pk_mul_f32 v[194:195], v[194:195], s[66:67]\n"
    "v_pk_mul_f32 v[196:197], v[196:197], s[66:67]\n"
    "v_pk_mul_f32 v[198:199], v[198:199], s[66:67]\n"
    "v_cvt_pk_bf16_f32 v192, v192, v193\n"
    "v_cvt_pk_bf16_f32 v194, v194, v195\n"
    "v_cvt_pk_bf16_f32 v196, v196, v197\n"
    "v_cvt_pk_bf16_f32 v198, v198, v199\n"
    "v_accvgpr_write_b32 a168, v192\n"
    "v_accvgpr_write_b32 a169, v194\n"
    "v_accvgpr_write_b32 a170, v196\n"
    "v_accvgpr_write_b32 a171, v198\n"
    "s_waitcnt vmcnt(8)\n"
    "s_barrier\n"
    "ds_read_b128 v[48:51], v219 offset:0\n"
    "s_waitcnt lgkmcnt(1)\n"
    "v_lshlrev_b32 v200, 16, v188\n"
    "v_and_b32 v201, 0xffff0000, v188\n"
    "v_lshlrev_b32 v202, 16, v189\n"
    "ds_read_b128 v[52:55], v219 offset:512\n"
    "v_and_b32 v203, 0xffff0000, v189\n"
    "v_lshlrev_b32 v204, 16, v190\n"
    "v_and_b32 v205, 0xffff0000, v190\n"
    "ds_read_b128 v[56:59], v219 offset:1024\n"
    "v_lshlrev_b32_e64 v206, 16, v191\n"
    "v_and_b32 v207, 0xffff0000, v191\n"
    "v_pk_mul_f32 v[200:201], v[200:201], s[66:67]\n"
    "ds_read_b128 v[60:63], v219 offset:1536\n"
    "v_pk_mul_f32 v[202:203], v[202:203], s[66:67]\n"
    "v_pk_mul_f32 v[204:205], v[204:205], s[66:67]\n"
    "v_pk_mul_f32 v[206:207], v[206:207], s[66:67]\n"
    "v_cvt_pk_bf16_f32 v200, v200, v201\n"
    "v_cvt_pk_bf16_f32 v202, v202, v203\n"
    "v_cvt_pk_bf16_f32 v204, v204, v205\n"
    "v_cvt_pk_bf16_f32 v206, v206, v207\n"
    "v_accvgpr_write_b32 a172, v200\n"
    "v_accvgpr_write_b32 a173, v202\n"
    "v_accvgpr_write_b32 a174, v204\n"
    "v_accvgpr_write_b32 a175, v206\n"
    "s_add_u32 m0, s63, 0x9000\n"
    "s_add_u32 s77, s76, 4096\n"
    "buffer_load_dwordx4 v211, s[48:51], s77 offen offset:0 nt lds\n"
    "s_waitcnt lgkmcnt(3)\n"
    "v_lshlrev_b32 v192, 16, v48\n"
    "v_and_b32 v193, 0xffff0000, v48\n"
    "v_lshlrev_b32 v194, 16, v49\n"
    "v_and_b32 v195, 0xffff0000, v49\n"
    "v_lshlrev_b32 v196, 16, v50\n"
    "v_and_b32 v197, 0xffff0000, v50\n"
    "v_lshlrev_b32_e64 v198, 16, v51\n"
    "v_and_b32 v199, 0xffff0000, v51\n"
    "v_pk_mul_f32 v[192:193], v[192:193], s[66:67]\n"
    "v_pk_mul_f32 v[194:195], v[194:195], s[66:67]\n"
    "v_pk_mul_f32 v[196:197], v[196:197], s[66:67]\n"
    "v_pk_mul_f32 v[198:199], v[198:199], s[66:67]\n"
    "v_cvt_pk_bf16_f32 v192, v192, v193\n"
    "v_cvt_pk_bf16_f32 v194, v194, v195\n"
    "v_cvt_pk_bf16_f32 v196, v196, v197\n"
    "v_cvt_pk_bf16_f32 v198, v198, v199\n"
    "v_accvgpr_write_b32 a240, v192\n"
    "v_accvgpr_write_b32 a241, v194\n"
    "v_accvgpr_write_b32 a242, v196\n"
    "v_accvgpr_write_b32 a243, v198\n"
    "s_waitcnt lgkmcnt(2)\n"
    "v_lshlrev_b32 v200, 16, v52\n"
    "v_and_b32 v201, 0xffff0000, v52\n"
    "v_lshlrev_b32 v202, 16, v53\n"
    "v_and_b32 v203, 0xffff0000, v53\n"
    "v_lshlrev_b32 v204, 16, v54\n"
    "v_and_b32 v205, 0xffff0000, v54\n"
    "v_lshlrev_b32_e64 v206, 16, v55\n"
    "v_and_b32 v207, 0xffff0000, v55\n"
    "buffer_load_dwordx4 v212, s[48:51], s77 offen offset:2048 nt lds\n"
    "v_pk_mul_f32 v[200:201], v[200:201], s[66:67]\n"
    "v_pk_mul_f32 v[202:203], v[202:203], s[66:67]\n"
    "v_pk_mul_f32 v[204:205], v[204:205], s[66:67]\n"
    "v_pk_mul_f32 v[206:207], v[206:207], s[66:67]\n"
    "v_cvt_pk_bf16_f32 v200, v200, v201\n"
    "v_cvt_pk_bf16_f32 v202, v202, v203\n"
    "v_cvt_pk_bf16_f32 v204, v204, v205\n"
    "v_cvt_pk_bf16_f32 v206, v206, v207\n"
    "v_accvgpr_write_b32 a244, v200\n"
    "v_accvgpr_write_b32 a245, v202\n"
    "v_accvgpr_write_b32 a246, v204\n"
    "v_accvgpr_write_b32 a247, v206\n"
    "s_waitcnt lgkmcnt(1)\n"
    "v_lshlrev_b32 v192, 16, v56\n"
    "v_and_b32 v193, 0xffff0000, v56\n"
    "v_lshlrev_b32 v194, 16, v57\n"
    "v_and_b32 v195, 0xffff0000, v57\n"
    "v_lshlrev_b32 v196, 16, v58\n"
    "v_and_b32 v197, 0xffff0000, v58\n"
    "v_lshlrev_b32_e64 v198, 16, v59\n"
    "v_and_b32 v199, 0xffff0000, v59\n"
    "v_pk_mul_f32 v[192:193], v[192:193], s[66:67]\n"
    "v_pk_mul_f32 v[194:195], v[194:195], s[66:67]\n"
    "v_pk_mul_f32 v[196:197], v[196:197], s[66:67]\n"
    "v_pk_mul_f32 v[198:199], v[198:199], s[66:67]\n"
    "v_cvt_pk_bf16_f32 v192, v192, v193\n"
    "v_cvt_pk_bf16_f32 v194, v194, v195\n"
    "s_add_u32 m0, s63, 0x9380\n"
    "v_cvt_pk_bf16_f32 v196, v196, v197\n"
    "buffer_load_dwordx4 v211, s[48:51], s77 offen offset:128 nt lds\n"
    "v_cvt_pk_bf16_f32 v198, v198, v199\n"
    "v_accvgpr_write_b32 a248, v192\n"
    "v_accvgpr_write_b32 a249, v194\n"
    "v_accvgpr_write_b32 a250, v196\n"
    "v_accvgpr_write_b32 a251, v198\n"
    "s_waitcnt lgkmcnt(0)\n"
    "v_lshlrev_b32 v200, 16, v60\n"
    "v_and_b32 v201, 0xffff0000, v60\n"
    "v_lshlrev_b32 v202, 16, v61\n"
    "v_and_b32 v203, 0xffff0000, v61\n"
    "v_lshlrev_b32 v204, 16, v62\n"
    "v_and_b32 v205, 0xffff0000, v62\n"
    "v_lshlrev_b32_e64 v206, 16, v63\n"
    "v_and_b32 v207, 0xffff0000, v63\n"
    "v_pk_mul_f32 v[200:201], v[200:201], s[66:67]\n"
    "v_pk_mul_f32 v[202:203], v[202:203], s[66:67]\n"
    "v_pk_mul_f32 v[204:205], v[204:205], s[66:67]\n"
    "v_pk_mul_f32 v[206:207], v[206:207], s[66:67]\n"
    "v_cvt_pk_bf16_f32 v200, v200, v201\n"
    "v_cvt_pk_bf16_f32 v202, v202, v203\n"
    "v_cvt_pk_bf16_f32 v204, v204, v205\n"
    "v_cvt_pk_bf16_f32 v206, v206, v207\n"
    "v_accvgpr_write_b32 a252, v200\n"
    "v_accvgpr_write_b32 a253, v202\n"
    "v_accvgpr_write_b32 a254, v204\n"
    "v_accvgpr_write_b32 a255, v206\n"
    "buffer_load_dwordx4 v212, s[48:51], s77 offen offset:2176 nt lds\n"
    "s_waitcnt vmcnt(4)\n"
    "s_nop 0\n"
    "ds_read_b128 a[0:3], v208 offset:0\n"
    "s_add_u32 m0, s63, 0x18000\n"
    "s_add_u32 s77, s76, 8192\n"
    "ds_read_b128 a[4:7], v208 offset:512\n"
    "ds_read_b128 a[8:11], v208 offset:1024\n"
    "ds_read_b128 a[12:15], v208 offset:1536\n"
    "ds_read_b128 a[16:19], v208 offset:4096\n"
    "ds_read_b128 a[20:23], v208 offset:4608\n"
    "ds_read_b128 a[24:27], v208 offset:5120\n"
    "ds_read_b128 a[28:31], v208 offset:5632\n"
    "v_xor_b32 v208, 0x2000, v208\n"
    "s_barrier\n"
    "buffer_load_dwordx4 v211, s[48:51], s77 offen offset:0 nt lds\n"
    "ds_read_b128 a[32:35], v208 offset:0\n"
    "s_waitcnt lgkmcnt(8)\n"
    "v_mfma_f32_16x16x32_bf16 v[220:223], a[0:3], a[160:163], 0\n"
    "s_waitcnt lgkmcnt(7)\n"
    "ds_read_b128 a[36:39], v208 offset:512\n"
    "v_mfma_f32_16x16x32_bf16 v[220:223], a[4:7], a[164:167], v[220:223]\n"
    "s_waitcnt lgkmcnt(7)\n"
    "ds_read_b128 a[40:43], v208 offset:1024\n"
    "v_mfma_f32_16x16x32_bf16 v[220:223], a[8:11], a[168:171], v[220:223]\n"
    "s_waitcnt lgkmcnt(7)\n"
    "ds_read_b128 a[44:47], v208 offset:1536\n"
    "buffer_load_dwordx4 v212, s[48:51], s77 offen offset:2048 nt lds\n"
    "v_mfma_f32_16x16x32_bf16 v[220:223], a[12:15], a[172:175], v[220:223]\n"
    "s_add_u32 m0, s63, 0x18380\n"
    "ds_read_b128 a[48:51], v208 offset:4096\n"
    "v_mfma_f32_16x16x32_bf16 v[40:43], a[0:3], a[240:243], 0\n"
    "ds_read_b128 a[52:55], v208 offset:4608\n"
    "v_mfma_f32_16x16x32_bf16 v[40:43], a[4:7], a[244:247], v[40:43]\n"
    "ds_read_b128 a[56:59], v208 offset:5120\n"
    "v_max3_f32 v106, v220, v221, v222\n"
    "v_mfma_f32_16x16x32_bf16 v[40:43], a[8:11], a[248:251], v[40:43]\n"
    "s_waitcnt lgkmcnt(7)\n"
    "ds_read_b128 a[60:63], v208 offset:5632\n"
    "v_xor_b32 v208, 0x6000, v208\n"
    "v_mfma_f32_16x16x32_bf16 v[224:227], a[28:31], a[172:175], 0\n"
    "ds_read_b128 a[64:67], v208 offset:0\n"
    "buffer_load_dwordx4 v211, s[48:51], s77 offen offset:128 nt lds\n"
    "v_mfma_f32_16x16x32_bf16 v[224:227], a[24:27], a[168:171], v[224:227]\n"
    "ds_read_b128 a[68:71], v208 offset:512\n"
    "v_mfma_f32_16x16x32_bf16 v[224:227], a[20:23], a[164:167], v[224:227]\n"
    "ds_read_b128 a[72:75], v208 offset:1024\n"
    "v_mfma_f32_16x16x32_bf16 v[224:227], a[16:19], a[160:163], v[224:227]\n"
    "ds_read_b128 a[76:79], v208 offset:1536\n"
    "buffer_load_dwordx4 v212, s[48:51], s77 offen offset:2176 nt lds\n"
    "v_mfma_f32_16x16x32_bf16 v[40:43], a[12:15], a[252:255], v[40:43]\n"
    "s_add_u32 m0, s63, 0x19000\n"
    "ds_read_b128 a[80:83], v208 offset:4096\n"
    "s_add_u32 s77, s76, 12288\n"
    "v_mfma_f32_16x16x32_bf16 v[44:47], a[28:31], a[252:255], 0\n"
    "ds_read_b128 a[84:87], v208 offset:4608\n"
    "v_max3_f32 v107, v224, v225, v226\n"
    "v_mfma_f32_16x16x32_bf16 v[44:47], a[24:27], a[248:251], v[44:47]\n"
    "ds_read_b128 a[88:91], v208 offset:5120\n"
    "v_max3_f32 v106, v106, v223, v227\n"
    "v_mfma_f32_16x16x32_bf16 v[44:47], a[20:23], a[244:247], v[44:47]\n"
    "ds_read_b128 a[92:95], v208 offset:5632\n"
    "v_xor_b32 v208, 0x2000, v208\n"
    "v_mfma_f32_16x16x32_bf16 v[44:47], a[16:19], a[240:243], v[44:47]\n"
    "v_max_f32 v106, v106, v107\n"
    "v_mov_b32 v107, v106\n"
    "ds_read_b128 a[96:99], v208 offset:0\n"
    "v_max3_f32 v104, v40, v41, v42\n"
    "v_permlane16_swap_b32 v106, v107\n"
    "v_max_f32 v106, v106, v107\n"
    "v_mov_b32 v107, v106\n"
    "ds_read_b128 a[100:103], v208 offset:512\n"
    "buffer_load_dwordx4 v211, s[48:51], s77 offen offset:0 nt lds\n"
    "v_permlane32_swap_b32 v106, v107\n"
    "v_max_f32 v106, v106, v107\n"
    "v_sub_f32 v132, 0, v106\n"
    "v_sub_f32 v133, 0, v106\n"
    "v_sub_f32 v134, 0, v106\n"
    "v_sub_f32 v135, 0, v106\n"
    "v_sub_f32 v220, v220, v106\n"
    "v_sub_f32 v221, v221, v106\n"
    "s_waitcnt lgkmcnt(15)\n"
    "v_mfma_f32_16x16x32_bf16 v[228:231], a[32:35], a[160:163], v[132:135]\n"
    "v_sub_f32 v222, v222, v106\n"
    "v_sub_f32 v223, v223, v106\n"
    "v_mfma_f32_16x16x32_bf16 v[228:231], a[36:39], a[164:167], v[228:231]\n"
    "v_sub_f32 v224, v224, v106\n"
    "v_sub_f32 v225, v225, v106\n"
    "v_mfma_f32_16x16x32_bf16 v[228:231], a[40:43], a[168:171], v[228:231]\n"
    "s_waitcnt lgkmcnt(14)\n"
    "v_sub_f32 v226, v226, v106\n"
    "v_sub_f32 v227, v227, v106\n"
    "v_mfma_f32_16x16x32_bf16 v[228:231], a[44:47], a[172:175], v[228:231]\n"
    "s_waitcnt lgkmcnt(10)\n"
    "ds_read_b128 a[104:107], v208 offset:1024\n"
    "v_max3_f32 v105, v44, v45, v46\n"
    "v_mfma_f32_16x16x32_bf16 v[232:235], a[60:63], a[172:175], v[132:135]\n"
    "ds_read_b128 a[108:111], v208 offset:1536\n"
    "v_max3_f32 v104, v104, v43, v47\n"
    "v_mfma_f32_16x16x32_bf16 v[232:235], a[56:59], a[168:171], v[232:235]\n"
    "ds_read_b128 a[112:115], v208 offset:4096\n"
    "v_max_f32 v104, v104, v105\n"
    "v_mfma_f32_16x16x32_bf16 v[232:235], a[52:55], a[164:167], v[232:235]\n"
    "ds_read_b128 a[116:119], v208 offset:4608\n"
    "v_mov_b32 v105, v104\n"
    "v_mfma_f32_16x16x32_bf16 v[232:235], a[48:51], a[160:163], v[232:235]\n"
    "s_waitcnt lgkmcnt(13)\n"
    "ds_read_b128 a[120:123], v208 offset:5120\n"
    "v_permlane16_swap_b32 v104, v105\n"
    "v_mfma_f32_16x16x32_bf16 v[4:7], a[64:67], a[160:163], v[132:135]\n"
    "s_waitcnt lgkmcnt(13)\n"
    "ds_read_b128 a[124:127], v208 offset:5632\n"
    "v_xor_b32 v208, 0xe000, v208\n"
    "v_mfma_f32_16x16x32_bf16 v[4:7], a[68:71], a[164:167], v[4:7]\n"
    "s_waitcnt lgkmcnt(13)\n"
    "v_max_f32 v104, v104, v105\n"
    "v_mov_b32 v105, v104\n"
    "v_mfma_f32_16x16x32_bf16 v[4:7], a[72:75], a[168:171], v[4:7]\n"
    "s_waitcnt lgkmcnt(12)\n"
    "buffer_load_dwordx4 v212, s[48:51], s77 offen offset:2048 nt lds\n"
    "v_mfma_f32_16x16x32_bf16 v[4:7], a[76:79], a[172:175], v[4:7]\n"
    "s_add_u32 m0, s63, 0x19380\n"
    "v_permlane32_swap_b32 v104, v105\n"
    "s_waitcnt lgkmcnt(8)\n"
    "v_mfma_f32_16x16x32_bf16 v[0:3], a[92:95], a[172:175], v[132:135]\n"
    "v_max_f32 v104, v104, v105\n"
    "v_sub_f32 v152, 0, v104\n"
    "v_mfma_f32_16x16x32_bf16 v[0:3], a[88:91], a[168:171], v[0:3]\n"
    "v_sub_f32 v153, 0, v104\n"
    "v_sub_f32 v154, 0, v104\n"
    "v_mfma_f32_16x16x32_bf16 v[0:3], a[84:87], a[164:167], v[0:3]\n"
    "v_sub_f32 v155, 0, v104\n"
    "v_sub_f32 v40, v40, v104\n"
    "v_mfma_f32_16x16x32_bf16 v[0:3], a[80:83], a[160:163], v[0:3]\n"
    "s_waitcnt lgkmcnt(7)\n"
    "v_sub_f32 v41, v41, v104\n"
    "v_sub_f32 v42, v42, v104\n"
    "v_mfma_f32_16x16x32_bf16 v[8:11], a[96:99], a[160:163], v[132:135]\n"
    "s_waitcnt lgkmcnt(6)\n"
    "v_sub_f32 v43, v43, v104\n"
    "v_sub_f32 v44, v44, v104\n"
    "v_mfma_f32_16x16x32_bf16 v[8:11], a[100:103], a[164:167], v[8:11]\n"
    "s_waitcnt lgkmcnt(5)\n"
    "v_sub_f32 v45, v45, v104\n"
    "v_sub_f32 v46, v46, v104\n"
    "v_mfma_f32_16x16x32_bf16 v[8:11], a[104:107], a[168:171], v[8:11]\n"
    "s_waitcnt lgkmcnt(4)\n"
    "v_sub_f32 v47, v47, v104\n"
    "v_exp_f32 v156, v40\n"
    "v_mfma_f32_16x16x32_bf16 v[8:11], a[108:111], a[172:175], v[8:11]\n"
    "s_waitcnt lgkmcnt(0)\n"
    "v_exp_f32 v157, v41\n"
    "v_mfma_f32_16x16x32_bf16 v[12:15], a[124:127], a[172:175], v[132:135]\n"
    "v_cvt_pk_bf16_f32 v108, v156, v157\n"
    "v_exp_f32 v158, v42\n"
    "v_mfma_f32_16x16x32_bf16 v[12:15], a[120:123], a[168:171], v[12:15]\n"
    "buffer_load_dwordx4 v211, s[48:51], s77 offen offset:128 nt lds\n"
    "v_mfma_f32_16x16x32_bf16 v[12:15], a[116:119], a[164:167], v[12:15]\n"
    "v_exp_f32 v159, v43\n"
    "v_mfma_f32_16x16x32_bf16 v[12:15], a[112:115], a[160:163], v[12:15]\n"
    "v_cvt_pk_bf16_f32 v109, v158, v159\n"
    "v_exp_f32 v160, v44\n"
    "v_exp_f32 v161, v45\n"
    "v_exp_f32 v162, v46\n"
    "v_cvt_pk_bf16_f32 v110, v160, v161\n"
    "v_exp_f32 v163, v47\n"
    "v_exp_f32 v156, v220\n"
    "v_cvt_pk_bf16_f32 v111, v162, v163\n"
    "v_exp_f32 v157, v221\n"
    "v_exp_f32 v158, v222\n"
    "v_cvt_pk_bf16_f32 v88, v156, v157\n"
    "v_exp_f32 v159, v223\n"
    "v_exp_f32 v160, v224\n"
    "v_cvt_pk_bf16_f32 v89, v158, v159\n"
    "v_exp_f32 v161, v225\n"
    "buffer_load_dwordx4 v212, s[48:51], s77 offen offset:2176 nt lds\n"
    "v_cvt_pk_bf16_f32 v90, v160, v161\n"
    "s_waitcnt vmcnt(8)\n"
    "ds_read_b128 v[176:179], v218 offset:4096\n"
    "s_mov_b32 s58, s60\n"
    "s_mov_b32 s59, s61\n"
    "s_xor_b32 s62, s62, 0x8000\n"
    "ds_read_b128 v[180:183], v218 offset:4608\n"
    "s_add_u32 m0, s62, 0x10000\n"
    "v_exp_f32 v162, v226\n"
    "ds_read_b128 v[184:187], v218 offset:5120\n"
    "buffer_load_dwordx4 v213, s[44:47], s58 offen offset:0 lds\n"
    "v_exp_f32 v163, v227\n"
    "ds_read_b128 v[188:191], v218 offset:5632\n"
    "v_cvt_pk_bf16_f32 v91, v162, v163\n"
    "v_exp_f32 v156, v228\n"
    "v_exp_f32 v157, v229\n"
    "v_exp_f32 v158, v230\n"
    "buffer_load_dwordx4 v213, s[44:47], s58 offen offset:2048 lds\n"
    "v_cvt_pk_bf16_f32 v92, v156, v157\n"
    "v_exp_f32 v159, v231\n"
    "v_exp_f32 v160, v232\n"
    "v_cvt_pk_bf16_f32 v93, v158, v159\n"
    "v_exp_f32 v161, v233\n"
    "s_add_u32 m0, s62, 0x10380\n"
    "v_cvt_pk_bf16_f32 v94, v160, v161\n"
    "buffer_load_dwordx4 v213, s[44:47], s58 offen offset:128 lds\n"
    "s_waitcnt lgkmcnt(3)\n"
    "v_lshlrev_b32 v192, 16, v176\n"
    "v_and_b32 v193, 0xffff0000, v176\n"
    "v_lshlrev_b32 v194, 16, v177\n"
    "v_and_b32 v195, 0xffff0000, v177\n"
    "v_lshlrev_b32 v196, 16, v178\n"
    "v_and_b32 v197, 0xffff0000, v178\n"
    "v_lshlrev_b32_e64 v198, 16, v179\n"
    "v_and_b32 v199, 0xffff0000, v179\n"
    "v_pk_mul_f32 v[192:193], v[192:193], s[66:67]\n"
    "v_pk_mul_f32 v[194:195], v[194:195], s[66:67]\n"
    "v_pk_mul_f32 v[196:197], v[196:197], s[66:67]\n"
    "v_pk_mul_f32 v[198:199], v[198:199], s[66:67]\n"
    "v_cvt_pk_bf16_f32 v192, v192, v193\n"
    "v_cvt_pk_bf16_f32 v194, v194, v195\n"
    "v_cvt_pk_bf16_f32 v196, v196, v197\n"
    "v_cvt_pk_bf16_f32 v198, v198, v199\n"
    "v_accvgpr_write_b32 a176, v192\n"
    "v_accvgpr_write_b32 a177, v194\n"
    "v_accvgpr_write_b32 a178, v196\n"
    "v_accvgpr_write_b32 a179, v198\n"
    "s_waitcnt lgkmcnt(2)\n"
    "v_lshlrev_b32 v200, 16, v180\n"
    "v_and_b32 v201, 0xffff0000, v180\n"
    "v_mfma_f32_16x16x32_bf16 v[240:243], a[16:19], a[176:179], 0\n"
    "v_lshlrev_b32 v202, 16, v181\n"
    "v_and_b32 v203, 0xffff0000, v181\n"
    "v_mfma_f32_16x16x32_bf16 v[236:239], a[0:3], a[176:179], 0\n"
    "v_lshlrev_b32 v204, 16, v182\n"
    "v_and_b32 v205, 0xffff0000, v182\n"
    "v_lshlrev_b32 v206, 16, v183\n"
    "v_and_b32 v207, 0xffff0000, v183\n"
    "s_waitcnt lgkmcnt(1)\n"
    "v_lshlrev_b32_e64 v192, 16, v184\n"
    "v_pk_mul_f32 v[200:201], v[200:201], s[66:67]\n"
    "v_pk_mul_f32 v[202:203], v[202:203], s[66:67]\n"
    "v_pk_mul_f32 v[204:205], v[204:205], s[66:67]\n"
    "v_pk_mul_f32 v[206:207], v[206:207], s[66:67]\n"
    "v_cvt_pk_bf16_f32 v200, v200, v201\n"
    "v_cvt_pk_bf16_f32 v202, v202, v203\n"
    "v_cvt_pk_bf16_f32 v204, v204, v205\n"
    "v_cvt_pk_bf16_f32 v206, v206, v207\n"
    "v_accvgpr_write_b32 a180, v200\n"
    "v_accvgpr_write_b32 a181, v202\n"
    "v_accvgpr_write_b32 a182, v204\n"
    "v_accvgpr_write_b32 a183, v206\n"
    "v_and_b32 v193, 0xffff0000, v184\n"
    "v_lshlrev_b32 v194, 16, v185\n"
    "v_mfma_f32_16x16x32_bf16 v[236:239], a[4:7], a[180:183], v[236:239]\n"
    "v_and_b32 v195, 0xffff0000, v185\n"
    "v_lshlrev_b32 v196, 16, v186\n"
    "v_mfma_f32_16x16x32_bf16 v[240:243], a[20:23], a[180:183], v[240:243]\n"
    "v_and_b32 v197, 0xffff0000, v186\n"
    "v_lshlrev_b32 v198, 16, v187\n"
    "v_mfma_f32_16x16x32_bf16 v[164:167], v[252:255], v[88:91], 0\n"
    "s_waitcnt lgkmcnt(0)\n"
    "v_and_b32 v199, 0xffff0000, v187\n"
    "v_lshlrev_b32 v200, 16, v188\n"
    "v_and_b32 v201, 0xffff0000, v188\n"
    "v_lshlrev_b32 v202, 16, v189\n"
    "v_and_b32 v203, 0xffff0000, v189\n"
    "v_pk_mul_f32 v[192:193], v[192:193], s[66:67]\n"
    "v_pk_mul_f32 v[194:195], v[194:195], s[66:67]\n"
    "v_pk_mul_f32 v[196:197], v[196:197], s[66:67]\n"
    "v_pk_mul_f32 v[198:199], v[198:199], s[66:67]\n"
    "v_cvt_pk_bf16_f32 v192, v192, v193\n"
    "v_cvt_pk_bf16_f32 v194, v194, v195\n"
    "v_cvt_pk_bf16_f32 v196, v196, v197\n"
    "v_cvt_pk_bf16_f32 v198, v198, v199\n"
    "v_accvgpr_write_b32 a184, v192\n"
    "v_accvgpr_write_b32 a185, v194\n"
    "v_accvgpr_write_b32 a186, v196\n"
    "v_accvgpr_write_b32 a187, v198\n"
    "v_lshlrev_b32 v204, 16, v190\n"
    "v_and_b32 v205, 0xffff0000, v190\n"
    "v_mfma_f32_16x16x32_bf16 v[240:243], a[24:27], a[184:187], v[240:243]\n"
    "v_lshlrev_b32 v206, 16, v191\n"
    "v_and_b32 v207, 0xffff0000, v191\n"
    "v_exp_f32 v162, v234\n"
    "v_exp_f32 v163, v235\n"
    "v_pk_mul_f32 v[200:201], v[200:201], s[66:67]\n"
    "v_pk_mul_f32 v[202:203], v[202:203], s[66:67]\n"
    "v_pk_mul_f32 v[204:205], v[204:205], s[66:67]\n"
    "v_pk_mul_f32 v[206:207], v[206:207], s[66:67]\n"
    "v_mfma_f32_16x16x32_bf16 v[236:239], a[8:11], a[184:187], v[236:239]\n"
    "v_cvt_pk_bf16_f32 v200, v200, v201\n"
    "v_cvt_pk_bf16_f32 v202, v202, v203\n"
    "v_cvt_pk_bf16_f32 v204, v204, v205\n"
    "v_cvt_pk_bf16_f32 v206, v206, v207\n"
    "v_accvgpr_write_b32 a188, v200\n"
    "v_accvgpr_write_b32 a189, v202\n"
    "v_accvgpr_write_b32 a190, v204\n"
    "v_accvgpr_write_b32 a191, v206\n"
    "v_cvt_pk_bf16_f32 v95, v162, v163\n"
    "buffer_load_dwordx4 v213, s[44:47], s58 offen offset:2176 lds\n"
    "v_mfma_f32_16x16x32_bf16 v[236:239], a[12:15], a[188:191], v[236:239]\n"
    ".Lgqa_T0:\n"
    "s_cbranch_execz .Lgqa_T4\n"
    "v_mfma_f32_16x16x32_bf16 v[240:243], a[28:31], a[188:191], v[240:243]\n"
    "v_mfma_f32_16x16x32_bf16 v[164:167], v[252:255], v[92:95], v[164:167]\n"
    "s_nop 0\n"
    "v_max3_f32 v104, v236, v237, v238\n"
    "s_nop 2\n"
    "v_max3_f32 v105, v240, v241, v242\n"
    "v_max3_f32 v104, v104, v239, v243\n"
    "v_max_f32 v104, v104, v105\n"
    "v_mov_b32 v105, v104\n"
    "v_cmp_lt_f32 vcc, s64, v164\n"
    "s_nop 0\n"
    "v_permlane16_swap_b32 v104, v105\n"
    "v_max_f32 v104, v104, v105\n"
    "v_mov_b32 v105, v104\n"
    "s_cbranch_vccnz .Lgqa_STUB0\n"
    ".Lgqa_RET0:\n"
    "s_nop 0\n"
    "v_permlane32_swap_b32 v104, v105\n"
    "v_max_f32 v104, v104, v105\n"
    "v_sub_f32 v136, 0, v104\n"
    "v_sub_f32 v137, 0, v104\n"
    "v_sub_f32 v138, 0, v104\n"
    "v_sub_f32 v139, 0, v104\n"
    "v_sub_f32 v236, v236, v104\n"
    "v_sub_f32 v237, v237, v104\n"
    "v_mfma_f32_16x16x32_bf16 v[244:247], a[32:35], a[176:179], v[136:139]\n"
    "v_sub_f32 v238, v238, v104\n"
    "v_sub_f32 v239, v239, v104\n"
    "v_mfma_f32_16x16x32_bf16 v[244:247], a[36:39], a[180:183], v[244:247]\n"
    "v_sub_f32 v240, v240, v104\n"
    "v_sub_f32 v241, v241, v104\n"
    "v_mfma_f32_16x16x32_bf16 v[244:247], a[40:43], a[184:187], v[244:247]\n"
    "v_sub_f32 v242, v242, v104\n"
    "v_sub_f32 v243, v243, v104\n"
    "v_mfma_f32_16x16x32_bf16 v[244:247], a[44:47], a[188:191], v[244:247]\n"
    "v_exp_f32 v156, v236\n"
    "v_mfma_f32_16x16x32_bf16 v[248:251], a[60:63], a[188:191], v[136:139]\n"
    "v_exp_f32 v157, v237\n"
    "v_mfma_f32_16x16x32_bf16 v[248:251], a[56:59], a[184:187], v[248:251]\n"
    "v_cvt_pk_bf16_f32 v112, v156, v157\n"
    "v_exp_f32 v158, v238\n"
    "v_mfma_f32_16x16x32_bf16 v[248:251], a[52:55], a[180:183], v[248:251]\n"
    "v_exp_f32 v159, v239\n"
    "v_mfma_f32_16x16x32_bf16 v[248:251], a[48:51], a[176:179], v[248:251]\n"
    "v_cvt_pk_bf16_f32 v113, v158, v159\n"
    "v_exp_f32 v160, v240\n"
    "v_mfma_f32_16x16x32_bf16 v[52:55], a[64:67], a[176:179], v[136:139]\n"
    "v_exp_f32 v161, v241\n"
    "v_mfma_f32_16x16x32_bf16 v[52:55], a[68:71], a[180:183], v[52:55]\n"
    "v_cvt_pk_bf16_f32 v114, v160, v161\n"
    "s_waitcnt vmcnt(8)\n"
    "v_mfma_f32_16x16x32_bf16 v[52:55], a[72:75], a[184:187], v[52:55]\n"
    "s_add_u32 m0, s62, 0x11000\n"
    "ds_read_b128 v[176:179], v217 offset:0\n"
    "buffer_load_dwordx4 v213, s[44:47], s59 offen offset:0 lds\n"
    "v_mfma_f32_16x16x32_bf16 v[52:55], a[76:79], a[188:191], v[52:55]\n"
    "ds_read_b128 v[180:183], v217 offset:512\n"
    "v_exp_f32 v162, v242\n"
    "v_mfma_f32_16x16x32_bf16 v[48:51], a[92:95], a[188:191], v[136:139]\n"
    "ds_read_b128 v[184:187], v217 offset:1024\n"
    "v_exp_f32 v163, v243\n"
    "v_mfma_f32_16x16x32_bf16 v[48:51], a[88:91], a[184:187], v[48:51]\n"
    "ds_read_b128 v[188:191], v217 offset:1536\n"
    "v_cvt_pk_bf16_f32 v115, v162, v163\n"
    "v_mfma_f32_16x16x32_bf16 v[48:51], a[84:87], a[180:183], v[48:51]\n"
    "buffer_load_dwordx4 v213, s[44:47], s59 offen offset:2048 lds\n"
    "v_mfma_f32_16x16x32_bf16 v[48:51], a[80:83], a[176:179], v[48:51]\n"
    "s_add_u32 m0, s62, 0x11380\n"
    "v_exp_f32 v156, v244\n"
    "v_mfma_f32_16x16x32_bf16 v[56:59], a[96:99], a[176:179], v[136:139]\n"
    "v_exp_f32 v157, v245\n"
    "v_mfma_f32_16x16x32_bf16 v[56:59], a[100:103], a[180:183], v[56:59]\n"
    "v_cvt_pk_bf16_f32 v116, v156, v157\n"
    "v_exp_f32 v158, v246\n"
    "v_mfma_f32_16x16x32_bf16 v[56:59], a[104:107], a[184:187], v[56:59]\n"
    "s_waitcnt lgkmcnt(3)\n"
    "v_lshlrev_b32 v192, 16, v176\n"
    "v_and_b32 v193, 0xffff0000, v176\n"
    "v_mfma_f32_16x16x32_bf16 v[56:59], a[108:111], a[188:191], v[56:59]\n"
    "v_lshlrev_b32 v194, 16, v177\n"
    "v_and_b32 v195, 0xffff0000, v177\n"
    "v_mfma_f32_16x16x32_bf16 v[60:63], a[124:127], a[188:191], v[136:139]\n"
    "v_lshlrev_b32 v196, 16, v178\n"
    "v_and_b32 v197, 0xffff0000, v178\n"
    "v_mfma_f32_16x16x32_bf16 v[60:63], a[120:123], a[184:187], v[60:63]\n"
    "s_waitcnt lgkmcnt(2)\n"
    "v_lshlrev_b32 v198, 16, v179\n"
    "v_and_b32 v199, 0xffff0000, v179\n"
    "v_lshlrev_b32 v200, 16, v180\n"
    "v_and_b32 v201, 0xffff0000, v180\n"
    "v_lshlrev_b32 v202, 16, v181\n"
    "v_pk_mul_f32 v[192:193], v[192:193], s[66:67]\n"
    "v_pk_mul_f32 v[194:195], v[194:195], s[66:67]\n"
    "v_pk_mul_f32 v[196:197], v[196:197], s[66:67]\n"
    "v_pk_mul_f32 v[198:199], v[198:199], s[66:67]\n"
    "v_mfma_f32_16x16x32_bf16 v[60:63], a[116:119], a[180:183], v[60:63]\n"
    "v_cvt_pk_bf16_f32 v192, v192, v193\n"
    "v_cvt_pk_bf16_f32 v194, v194, v195\n"
    "v_mfma_f32_16x16x32_bf16 v[60:63], a[112:115], a[176:179], v[60:63]\n"
    "v_cvt_pk_bf16_f32 v196, v196, v197\n"
    "v_cvt_pk_bf16_f32 v198, v198, v199\n"
    "v_accvgpr_write_b32 a192, v192\n"
    "v_accvgpr_write_b32 a193, v194\n"
    "v_accvgpr_write_b32 a194, v196\n"
    "v_accvgpr_write_b32 a195, v198\n"
    "v_and_b32 v203, 0xffff0000, v181\n"
    "v_lshlrev_b32 v204, 16, v182\n"
    "v_mfma_f32_16x16x32_bf16 v[224:227], a[16:19], a[192:195], 0\n"
    "v_and_b32 v205, 0xffff0000, v182\n"
    "v_lshlrev_b32 v206, 16, v183\n"
    "v_mfma_f32_16x16x32_bf16 v[220:223], a[0:3], a[192:195], 0\n"
    "s_waitcnt lgkmcnt(1)\n"
    "v_and_b32 v207, 0xffff0000, v183\n"
    "v_lshlrev_b32 v192, 16, v184\n"
    "v_and_b32 v193, 0xffff0000, v184\n"
    "v_lshlrev_b32_e64 v194, 16, v185\n"
    "v_and_b32 v195, 0xffff0000, v185\n"
    "v_pk_mul_f32 v[200:201], v[200:201], s[66:67]\n"
    "v_pk_mul_f32 v[202:203], v[202:203], s[66:67]\n"
    "v_pk_mul_f32 v[204:205], v[204:205], s[66:67]\n"
    "v_pk_mul_f32 v[206:207], v[206:207], s[66:67]\n"
    "v_cvt_pk_bf16_f32 v200, v200, v201\n"
    "v_cvt_pk_bf16_f32 v202, v202, v203\n"
    "v_cvt_pk_bf16_f32 v204, v204, v205\n"
    "v_cvt_pk_bf16_f32 v206, v206, v207\n"
    "v_accvgpr_write_b32 a196, v200\n"
    "v_accvgpr_write_b32 a197, v202\n"
    "v_accvgpr_write_b32 a198, v204\n"
    "v_accvgpr_write_b32 a199, v206\n"
    "v_lshlrev_b32 v196, 16, v186\n"
    "v_and_b32 v197, 0xffff0000, v186\n"
    "v_mfma_f32_16x16x32_bf16 v[220:223], a[4:7], a[196:199], v[220:223]\n"
    "s_waitcnt lgkmcnt(0)\n"
    "v_lshlrev_b32 v198, 16, v187\n"
    "v_and_b32 v199, 0xffff0000, v187\n"
    "v_lshlrev_b32 v200, 16, v188\n"
    "v_and_b32 v201, 0xffff0000, v188\n"
    "v_lshlrev_b32_e64 v202, 16, v189\n"
    "v_pk_mul_f32 v[192:193], v[192:193], s[66:67]\n"
    "v_pk_mul_f32 v[194:195], v[194:195], s[66:67]\n"
    "v_pk_mul_f32 v[196:197], v[196:197], s[66:67]\n"
    "v_pk_mul_f32 v[198:199], v[198:199], s[66:67]\n"
    "v_mfma_f32_16x16x32_bf16 v[224:227], a[20:23], a[196:199], v[224:227]\n"
    "v_cvt_pk_bf16_f32 v192, v192, v193\n"
    "v_cvt_pk_bf16_f32 v194, v194, v195\n"
    "v_mfma_f32_16x16x32_bf16 v[168:171], v[252:255], v[112:115], 0\n"
    "v_cvt_pk_bf16_f32 v196, v196, v197\n"
    "v_cvt_pk_bf16_f32 v198, v198, v199\n"
    "v_accvgpr_write_b32 a200, v192\n"
    "v_accvgpr_write_b32 a201, v194\n"
    "v_accvgpr_write_b32 a202, v196\n"
    "v_accvgpr_write_b32 a203, v198\n"
    "v_and_b32 v203, 0xffff0000, v189\n"
    "v_lshlrev_b32 v204, 16, v190\n"
    "v_mfma_f32_16x16x32_bf16 v[224:227], a[24:27], a[200:203], v[224:227]\n"
    "v_and_b32 v205, 0xffff0000, v190\n"
    "v_lshlrev_b32 v206, 16, v191\n"
    "v_mfma_f32_16x16x32_bf16 v[220:223], a[8:11], a[200:203], v[220:223]\n"
    "v_and_b32 v207, 0xffff0000, v191\n"
    "v_exp_f32 v159, v247\n"
    "v_exp_f32 v160, v248\n"
    "v_pk_mul_f32 v[200:201], v[200:201], s[66:67]\n"
    "v_pk_mul_f32 v[202:203], v[202:203], s[66:67]\n"
    "v_pk_mul_f32 v[204:205], v[204:205], s[66:67]\n"
    "v_pk_mul_f32 v[206:207], v[206:207], s[66:67]\n"
    "v_cvt_pk_bf16_f32 v200, v200, v201\n"
    "v_cvt_pk_bf16_f32 v202, v202, v203\n"
    "v_cvt_pk_bf16_f32 v204, v204, v205\n"
    "v_cvt_pk_bf16_f32 v206, v206, v207\n"
    "v_accvgpr_write_b32 a204, v200\n"
    "v_accvgpr_write_b32 a205, v202\n"
    "v_accvgpr_write_b32 a206, v204\n"
    "v_accvgpr_write_b32 a207, v206\n"
    "v_cvt_pk_bf16_f32 v117, v158, v159\n"
    "v_exp_f32 v161, v249\n"
    "v_mfma_f32_16x16x32_bf16 v[220:223], a[12:15], a[204:207], v[220:223]\n"
    "buffer_load_dwordx4 v213, s[44:47], s59 offen offset:128 lds\n"
    "v_mfma_f32_16x16x32_bf16 v[224:227], a[28:31], a[204:207], v[224:227]\n"
    "v_cvt_pk_bf16_f32 v118, v160, v161\n"
    "v_exp_f32 v162, v250\n"
    "v_exp_f32 v163, v251\n"
    "s_nop 0\n"
    "v_cvt_pk_bf16_f32 v119, v162, v163\n"
    "v_max3_f32 v106, v220, v221, v222\n"
    "buffer_load_dwordx4 v213, s[44:47], s59 offen offset:2176 lds\n"
    "v_mfma_f32_16x16x32_bf16 v[168:171], v[252:255], v[116:119], v[168:171]\n"
    "s_nop 0\n"
    "v_max3_f32 v107, v224, v225, v226\n"
    "v_max3_f32 v106, v106, v223, v227\n"
    "v_max_f32 v106, v106, v107\n"
    "v_mov_b32 v107, v106\n"
    "s_nop 1\n"
    "v_permlane16_swap_b32 v106, v107\n"
    "v_max_f32 v106, v106, v107\n"
    "v_mov_b32 v107, v106\n"
    "v_cmp_lt_f32 vcc, s64, v168\n"
    "s_nop 0\n"
    "v_permlane32_swap_b32 v106, v107\n"
    "v_max_f32 v106, v106, v107\n"
    "v_sub_f32 v140, 0, v106\n"
    "v_sub_f32 v141, 0, v106\n"
    "v_sub_f32 v142, 0, v106\n"
    "v_sub_f32 v143, 0, v106\n"
    "v_sub_f32 v220, v220, v106\n"
    "v_sub_f32 v221, v221, v106\n"
    "v_mfma_f32_16x16x32_bf16 v[228:231], a[32:35], a[192:195], v[140:143]\n"
    "s_cbranch_vccnz .Lgqa_STUB1\n"
    ".Lgqa_RET1:\n"
    "v_sub_f32 v222, v222, v106\n"
    "v_mfma_f32_16x16x32_bf16 v[228:231], a[36:39], a[196:199], v[228:231]\n"
    "v_sub_f32 v223, v223, v106\n"
    "v_sub_f32 v224, v224, v106\n"
    "v_mfma_f32_16x16x32_bf16 v[228:231], a[40:43], a[200:203], v[228:231]\n"
    "v_sub_f32 v225, v225, v106\n"
    "v_sub_f32 v226, v226, v106\n"
    "v_mfma_f32_16x16x32_bf16 v[228:231], a[44:47], a[204:207], v[228:231]\n"
    "v_sub_f32 v227, v227, v106\n"
    "v_exp_f32 v156, v220\n"
    "v_mfma_f32_16x16x32_bf16 v[232:235], a[60:63], a[204:207], v[140:143]\n"
    "v_exp_f32 v157, v221\n"
    "v_mfma_f32_16x16x32_bf16 v[232:235], a[56:59], a[200:203], v[232:235]\n"
    "v_cvt_pk_bf16_f32 v96, v156, v157\n"
    "v_exp_f32 v158, v222\n"
    "v_mfma_f32_16x16x32_bf16 v[232:235], a[52:55], a[196:199], v[232:235]\n"
    "v_exp_f32 v159, v223\n"
    "v_mfma_f32_16x16x32_bf16 v[232:235], a[48:51], a[192:195], v[232:235]\n"
    "v_cvt_pk_bf16_f32 v97, v158, v159\n"
    "v_exp_f32 v160, v224\n"
    "v_mfma_f32_16x16x32_bf16 v[16:19], a[64:67], a[192:195], v[140:143]\n"
    "v_exp_f32 v161, v225\n"
    "v_mfma_f32_16x16x32_bf16 v[16:19], a[68:71], a[196:199], v[16:19]\n"
    "v_cvt_pk_bf16_f32 v98, v160, v161\n"
    "s_waitcnt vmcnt(8)\n"
    "v_mfma_f32_16x16x32_bf16 v[16:19], a[72:75], a[200:203], v[16:19]\n"
    "s_add_u32 m0, s63, 0x20000\n"
    "ds_read_b128 v[176:179], v217 offset:4096\n"
    "s_add_u32 s77, s76, 16384\n"
    "v_mfma_f32_16x16x32_bf16 v[16:19], a[76:79], a[204:207], v[16:19]\n"
    "ds_read_b128 v[180:183], v217 offset:4608\n"
    "buffer_load_dwordx4 v211, s[48:51], s77 offen offset:0 nt lds\n"
    "v_mfma_f32_16x16x32_bf16 v[20:23], a[92:95], a[204:207], v[140:143]\n"
    "ds_read_b128 v[184:187], v217 offset:5120\n"
    "v_exp_f32 v162, v226\n"
    "v_mfma_f32_16x16x32_bf16 v[20:23], a[88:91], a[200:203], v[20:23]\n"
    "ds_read_b128 v[188:191], v217 offset:5632\n"
    "v_exp_f32_e64 v163, v227\n"
    "v_mfma_f32_16x16x32_bf16 v[20:23], a[84:87], a[196:199], v[20:23]\n"
    "buffer_load_dwordx4 v212, s[48:51], s77 offen offset:2048 nt lds\n"
    "v_mfma_f32_16x16x32_bf16 v[20:23], a[80:83], a[192:195], v[20:23]\n"
    "s_add_u32 m0, s63, 0x20380\n"
    "s_add_u32 s60, s60, 0x8000\n"
    "s_and_b32 s60, s60, 0x3ffff\n"
    "v_mfma_f32_16x16x32_bf16 v[64:67], a[96:99], a[192:195], v[140:143]\n"
    "s_add_u32 s61, s60, 0x1000\n"
    "s_xor_b32 s57, s57, 0x8000\n"
    "v_cvt_pk_bf16_f32 v99, v162, v163\n"
    "v_mfma_f32_16x16x32_bf16 v[64:67], a[100:103], a[196:199], v[64:67]\n"
    "buffer_load_dwordx4 v211, s[48:51], s77 offen offset:128 nt lds\n"
    "v_mfma_f32_16x16x32_bf16 v[64:67], a[104:107], a[200:203], v[64:67]\n"
    "s_waitcnt lgkmcnt(3)\n"
    "v_lshlrev_b32 v192, 16, v176\n"
    "v_and_b32 v193, 0xffff0000, v176\n"
    "v_mfma_f32_16x16x32_bf16 v[64:67], a[108:111], a[204:207], v[64:67]\n"
    "v_lshlrev_b32 v194, 16, v177\n"
    "v_and_b32 v195, 0xffff0000, v177\n"
    "v_mfma_f32_16x16x32_bf16 v[68:71], a[124:127], a[204:207], v[140:143]\n"
    "v_lshlrev_b32 v196, 16, v178\n"
    "v_and_b32 v197, 0xffff0000, v178\n"
    "v_mfma_f32_16x16x32_bf16 v[68:71], a[120:123], a[200:203], v[68:71]\n"
    "s_waitcnt lgkmcnt(2)\n"
    "v_lshlrev_b32 v198, 16, v179\n"
    "v_and_b32 v199, 0xffff0000, v179\n"
    "v_lshlrev_b32 v200, 16, v180\n"
    "v_and_b32 v201, 0xffff0000, v180\n"
    "v_lshlrev_b32 v202, 16, v181\n"
    "v_pk_mul_f32 v[192:193], v[192:193], s[66:67]\n"
    "v_pk_mul_f32 v[194:195], v[194:195], s[66:67]\n"
    "v_pk_mul_f32 v[196:197], v[196:197], s[66:67]\n"
    "v_pk_mul_f32 v[198:199], v[198:199], s[66:67]\n"
    "v_mfma_f32_16x16x32_bf16 v[68:71], a[116:119], a[196:199], v[68:71]\n"
    "v_cvt_pk_bf16_f32 v192, v192, v193\n"
    "v_cvt_pk_bf16_f32 v194, v194, v195\n"
    "v_mfma_f32_16x16x32_bf16 v[68:71], a[112:115], a[192:195], v[68:71]\n"
    "v_cvt_pk_bf16_f32 v196, v196, v197\n"
    "v_cvt_pk_bf16_f32 v198, v198, v199\n"
    "v_accvgpr_write_b32 a208, v192\n"
    "v_accvgpr_write_b32 a209, v194\n"
    "v_accvgpr_write_b32 a210, v196\n"
    "v_accvgpr_write_b32 a211, v198\n"
    "v_and_b32 v203, 0xffff0000, v181\n"
    "v_lshlrev_b32 v204, 16, v182\n"
    "v_mfma_f32_16x16x32_bf16 v[240:243], a[16:19], a[208:211], 0\n"
    "v_and_b32 v205, 0xffff0000, v182\n"
    "v_lshlrev_b32 v206, 16, v183\n"
    "v_mfma_f32_16x16x32_bf16 v[236:239], a[0:3], a[208:211], 0\n"
    "s_waitcnt lgkmcnt(1)\n"
    "v_and_b32 v207, 0xffff0000, v183\n"
    "v_lshlrev_b32 v192, 16, v184\n"
    "v_and_b32 v193, 0xffff0000, v184\n"
    "v_lshlrev_b32_e64 v194, 16, v185\n"
    "v_and_b32 v195, 0xffff0000, v185\n"
    "v_pk_mul_f32 v[200:201], v[200:201], s[66:67]\n"
    "v_pk_mul_f32 v[202:203], v[202:203], s[66:67]\n"
    "v_pk_mul_f32 v[204:205], v[204:205], s[66:67]\n"
    "v_pk_mul_f32 v[206:207], v[206:207], s[66:67]\n"
    "v_cvt_pk_bf16_f32 v200, v200, v201\n"
    "v_cvt_pk_bf16_f32 v202, v202, v203\n"
    "v_cvt_pk_bf16_f32 v204, v204, v205\n"
    "v_cvt_pk_bf16_f32 v206, v206, v207\n"
    "v_accvgpr_write_b32 a212, v200\n"
    "v_accvgpr_write_b32 a213, v202\n"
    "v_accvgpr_write_b32 a214, v204\n"
    "v_accvgpr_write_b32 a215, v206\n"
    "v_lshlrev_b32 v196, 16, v186\n"
    "v_and_b32 v197, 0xffff0000, v186\n"
    "v_mfma_f32_16x16x32_bf16 v[236:239], a[4:7], a[212:215], v[236:239]\n"
    "s_waitcnt lgkmcnt(0)\n"
    "v_lshlrev_b32 v198, 16, v187\n"
    "v_and_b32 v199, 0xffff0000, v187\n"
    "v_lshlrev_b32 v200, 16, v188\n"
    "v_and_b32 v201, 0xffff0000, v188\n"
    "v_lshlrev_b32_e64 v202, 16, v189\n"
    "v_pk_mul_f32 v[192:193], v[192:193], s[66:67]\n"
    "v_pk_mul_f32 v[194:195], v[194:195], s[66:67]\n"
    "v_pk_mul_f32 v[196:197], v[196:197], s[66:67]\n"
    "v_pk_mul_f32 v[198:199], v[198:199], s[66:67]\n"
    "v_mfma_f32_16x16x32_bf16 v[240:243], a[20:23], a[212:215], v[240:243]\n"
    "v_cvt_pk_bf16_f32 v192, v192, v193\n"
    "v_cvt_pk_bf16_f32 v194, v194, v195\n"
    "v_mfma_f32_16x16x32_bf16 v[128:131], v[252:255], v[96:99], 0\n"
    "v_cvt_pk_bf16_f32 v196, v196, v197\n"
    "v_cvt_pk_bf16_f32 v198, v198, v199\n"
    "v_accvgpr_write_b32 a216, v192\n"
    "v_accvgpr_write_b32 a217, v194\n"
    "v_accvgpr_write_b32 a218, v196\n"
    "v_accvgpr_write_b32 a219, v198\n"
    "v_and_b32 v203, 0xffff0000, v189\n"
    "v_lshlrev_b32 v204, 16, v190\n"
    "v_mfma_f32_16x16x32_bf16 v[240:243], a[24:27], a[216:219], v[240:243]\n"
    "v_and_b32 v205, 0xffff0000, v190\n"
    "v_lshlrev_b32 v206, 16, v191\n"
    "v_mfma_f32_16x16x32_bf16 v[236:239], a[8:11], a[216:219], v[236:239]\n"
    "v_and_b32 v207, 0xffff0000, v191\n"
    "buffer_load_dwordx4 v212, s[48:51], s77 offen offset:2176 nt lds\n"
    "s_add_u32 m0, s57, 0x0\n"
    "v_exp_f32 v156, v228\n"
    "v_pk_mul_f32 v[200:201], v[200:201], s[66:67]\n"
    "v_pk_mul_f32 v[202:203], v[202:203], s[66:67]\n"
    "v_pk_mul_f32 v[204:205], v[204:205], s[66:67]\n"
    "v_pk_mul_f32 v[206:207], v[206:207], s[66:67]\n"
    "v_cvt_pk_bf16_f32 v200, v200, v201\n"
    "v_cvt_pk_bf16_f32 v202, v202, v203\n"
    "v_cvt_pk_bf16_f32 v204, v204, v205\n"
    "v_cvt_pk_bf16_f32 v206, v206, v207\n"
    "v_accvgpr_write_b32 a220, v200\n"
    "v_accvgpr_write_b32 a221, v202\n"
    "v_accvgpr_write_b32 a222, v204\n"
    "v_accvgpr_write_b32 a223, v206\n"
    "buffer_load_dwordx4 v211, s[40:43], s60 offen offset:0 nt lds\n"
    "v_exp_f32 v157, v229\n"
    "v_mfma_f32_16x16x32_bf16 v[236:239], a[12:15], a[220:223], v[236:239]\n"
    "v_cvt_pk_bf16_f32 v120, v156, v157\n"
    "v_exp_f32 v158, v230\n"
    "v_mfma_f32_16x16x32_bf16 v[240:243], a[28:31], a[220:223], v[240:243]\n"
    "v_exp_f32 v159, v231\n"
    "v_exp_f32 v160, v232\n"
    "v_cvt_pk_bf16_f32 v121, v158, v159\n"
    "v_exp_f32 v161, v233\n"
    "v_exp_f32 v162, v234\n"
    "v_max3_f32 v104, v236, v237, v238\n"
    "v_cvt_pk_bf16_f32 v122, v160, v161\n"
    "v_exp_f32 v163, v235\n"
    "buffer_load_dwordx4 v212, s[40:43], s60 offen offset:2048 nt lds\n"
    "v_max3_f32 v105, v240, v241, v242\n"
    "v_max3_f32 v104, v104, v239, v243\n"
    "v_max_f32 v104, v104, v105\n"
    "v_mov_b32 v105, v104\n"
    "v_cvt_pk_bf16_f32 v123, v162, v163\n"
    "s_add_u32 m0, s57, 0x380\n"
    "v_permlane16_swap_b32 v104, v105\n"
    "v_mfma_f32_16x16x32_bf16 v[128:131], v[252:255], v[120:123], v[128:131]\n"
    "v_max_f32 v104, v104, v105\n"
    "v_mov_b32 v105, v104\n"
    "buffer_load_dwordx4 v211, s[40:43], s60 offen offset:128 nt lds\n"
    "s_nop 0\n"
    "v_permlane32_swap_b32 v104, v105\n"
    "v_max_f32 v104, v104, v105\n"
    "v_sub_f32 v144, 0, v104\n"
    "v_sub_f32 v145, 0, v104\n"
    "v_sub_f32 v146, 0, v104\n"
    "v_sub_f32 v147, 0, v104\n"
    "v_sub_f32 v236, v236, v104\n"
    "v_sub_f32 v237, v237, v104\n"
    "v_mfma_f32_16x16x32_bf16 v[244:247], a[32:35], a[208:211], v[144:147]\n"
    "v_sub_f32 v238, v238, v104\n"
    "v_sub_f32 v239, v239, v104\n"
    "v_mfma_f32_16x16x32_bf16 v[244:247], a[36:39], a[212:215], v[244:247]\n"
    "v_sub_f32 v240, v240, v104\n"
    "v_sub_f32 v241, v241, v104\n"
    "v_mfma_f32_16x16x32_bf16 v[244:247], a[40:43], a[216:219], v[244:247]\n"
    "v_sub_f32 v242, v242, v104\n"
    "v_sub_f32 v243, v243, v104\n"
    "v_mfma_f32_16x16x32_bf16 v[244:247], a[44:47], a[220:223], v[244:247]\n"
    "v_mul_f32 v172, 0x3e800000, v128\n"
    "v_cmp_lt_f32 vcc, s64, v172\n"
    "v_mfma_f32_16x16x32_bf16 v[248:251], a[60:63], a[220:223], v[144:147]\n"
    "buffer_load_dwordx4 v212, s[40:43], s60 offen offset:2176 nt lds\n"
    "v_mfma_f32_16x16x32_bf16 v[248:251], a[56:59], a[216:219], v[248:251]\n"
    "s_cbranch_vccnz .Lgqa_STUB2\n"
    ".Lgqa_RET2:\n"
    "v_exp_f32 v156, v236\n"
    "v_mfma_f32_16x16x32_bf16 v[248:251], a[52:55], a[212:215], v[248:251]\n"
    "v_exp_f32 v157, v237\n"
    "v_mfma_f32_16x16x32_bf16 v[248:251], a[48:51], a[208:211], v[248:251]\n"
    "v_cvt_pk_bf16_f32 v100, v156, v157\n"
    "v_exp_f32 v158, v238\n"
    "v_mfma_f32_16x16x32_bf16 v[24:27], a[64:67], a[208:211], v[144:147]\n"
    "v_exp_f32 v159, v239\n"
    "v_mfma_f32_16x16x32_bf16 v[24:27], a[68:71], a[212:215], v[24:27]\n"
    "v_cvt_pk_bf16_f32 v101, v158, v159\n"
    "v_exp_f32 v160, v240\n"
    "v_mfma_f32_16x16x32_bf16 v[24:27], a[72:75], a[216:219], v[24:27]\n"
    "v_exp_f32 v161, v241\n"
    "v_mfma_f32_16x16x32_bf16 v[24:27], a[76:79], a[220:223], v[24:27]\n"
    "v_cvt_pk_bf16_f32 v102, v160, v161\n"
    "v_exp_f32 v162, v242\n"
    "v_mfma_f32_16x16x32_bf16 v[28:31], a[92:95], a[220:223], v[144:147]\n"
    "v_exp_f32 v163, v243\n"
    "v_mfma_f32_16x16x32_bf16 v[28:31], a[88:91], a[216:219], v[28:31]\n"
    "v_cvt_pk_bf16_f32 v103, v162, v163\n"
    "v_exp_f32 v156, v244\n"
    "v_mfma_f32_16x16x32_bf16 v[28:31], a[84:87], a[212:215], v[28:31]\n"
    "v_exp_f32 v157, v245\n"
    "v_mfma_f32_16x16x32_bf16 v[28:31], a[80:83], a[208:211], v[28:31]\n"
    "v_cvt_pk_bf16_f32 v124, v156, v157\n"
    "v_exp_f32 v158, v246\n"
    "v_mfma_f32_16x16x32_bf16 v[72:75], a[96:99], a[208:211], v[144:147]\n"
    "v_exp_f32 v159, v247\n"
    "v_mfma_f32_16x16x32_bf16 v[72:75], a[100:103], a[212:215], v[72:75]\n"
    "v_cvt_pk_bf16_f32 v125, v158, v159\n"
    "v_exp_f32 v160, v248\n"
    "v_mfma_f32_16x16x32_bf16 v[72:75], a[104:107], a[216:219], v[72:75]\n"
    "v_exp_f32 v161, v249\n"
    "v_mfma_f32_16x16x32_bf16 v[72:75], a[108:111], a[220:223], v[72:75]\n"
    "s_waitcnt vmcnt(8)\n"
    "ds_read_b64_tr_b16 a[0:1], v209 offset:0\n"
    "v_cvt_pk_bf16_f32 v126, v160, v161\n"
    "v_mfma_f32_16x16x32_bf16 v[76:79], a[124:127], a[220:223], v[144:147]\n"
    "s_add_u32 m0, s57, 0x1000\n"
    "ds_read_b64_tr_b16 a[2:3], v209 offset:4096\n"
    "v_exp_f32 v162, v250\n"
    "v_mfma_f32_16x16x32_bf16 v[76:79], a[120:123], a[216:219], v[76:79]\n"
    "ds_read_b64_tr_b16 a[4:5], v209 offset:8\n"
    "v_exp_f32 v163, v251\n"
    "v_mfma_f32_16x16x32_bf16 v[76:79], a[116:119], a[212:215], v[76:79]\n"
    "ds_read_b64_tr_b16 a[6:7], v209 offset:4104\n"
    "v_cvt_pk_bf16_f32 v127, v162, v163\n"
    "v_mfma_f32_16x16x32_bf16 v[76:79], a[112:115], a[208:211], v[76:79]\n"
    "ds_read_b64_tr_b16 a[8:9], v209 offset:512\n"
    "buffer_load_dwordx4 v211, s[40:43], s61 offen offset:0 nt lds\n"
    "v_mfma_f32_16x16x32_bf16 v[128:131], v[252:255], v[100:103], 0\n"
    "ds_read_b64_tr_b16 a[10:11], v209 offset:4608\n"
    "v_mfma_f32_16x16x32_bf16 v[128:131], v[252:255], v[124:127], v[128:131]\n"
    "ds_read_b64_tr_b16 a[12:13], v209 offset:520\n"
    "ds_read_b64_tr_b16 a[14:15], v209 offset:4616\n"
    "ds_read_b64_tr_b16 a[16:17], v209 offset:1024\n"
    "ds_read_b64_tr_b16 a[18:19], v209 offset:5120\n"
    "s_waitcnt lgkmcnt(8)\n"
    "v_mfma_f32_16x16x32_bf16 v[176:179], a[0:3], v[112:115], 0\n"
    "s_waitcnt lgkmcnt(6)\n"
    "ds_read_b64_tr_b16 a[20:21], v209 offset:1032\n"
    "ds_read_b64_tr_b16 a[22:23], v209 offset:5128\n"
    "v_mfma_f32_16x16x32_bf16 v[180:183], a[4:7], v[112:115], 0\n"
    "ds_read_b64_tr_b16 a[24:25], v209 offset:1536\n"
    "v_mul_f32 v173, 0x3e800000, v128\n"
    "v_cmp_lt_f32 vcc, s64, v173\n"
    "ds_read_b64_tr_b16 a[26:27], v209 offset:5632\n"
    "s_waitcnt lgkmcnt(8)\n"
    "v_mfma_f32_16x16x32_bf16 v[184:187], a[8:11], v[112:115], 0\n"
    "s_cbranch_vccnz .Lgqa_STUB3\n"
    ".Lgqa_RET3:\n"
    "ds_read_b64_tr_b16 a[28:29], v209 offset:1544\n"
    "v_exp_f32 v156, v52\n"
    "v_exp_f32 v157, v53\n"
    "s_waitcnt lgkmcnt(7)\n"
    "v_mfma_f32_16x16x32_bf16 v[188:191], a[12:15], v[112:115], 0\n"
    "ds_read_b64_tr_b16 a[30:31], v209 offset:5640\n"
    "v_exp_f32 v158, v54\n"
    "v_exp_f32 v159, v55\n"
    "v_exp_f32 v160, v48\n"
    "s_waitcnt lgkmcnt(6)\n"
    "v_mfma_f32_16x16x32_bf16 v[192:195], a[16:19], v[112:115], 0\n"
    "s_waitcnt lgkmcnt(4)\n"
    "v_exp_f32 v161, v49\n"
    "v_exp_f32 v162, v50\n"
    "v_exp_f32 v163, v51\n"
    "v_mfma_f32_16x16x32_bf16 v[196:199], a[20:23], v[112:115], 0\n"
    "v_xor_b32 v209, 0x4000, v209\n"
    "v_mfma_f32_16x16x32_bf16 a[52:55], a[20:23], v[96:99], 0\n"
    "s_waitcnt lgkmcnt(2)\n"
    "v_mfma_f32_16x16x32_bf16 v[200:203], a[24:27], v[112:115], 0\n"
    "v_mfma_f32_16x16x32_bf16 a[56:59], a[24:27], v[96:99], 0\n"
    "v_mfma_f32_16x16x32_bf16 a[48:51], a[16:19], v[96:99], 0\n"
    "s_waitcnt lgkmcnt(0)\n"
    "v_mfma_f32_16x16x32_bf16 v[204:207], a[28:31], v[112:115], 0\n"
    "v_cvt_pk_bf16_f32 v112, v156, v157\n"
    "v_cvt_pk_bf16_f32 v113, v158, v159\n"
    "v_mfma_f32_16x16x32_bf16 a[60:63], a[28:31], v[96:99], 0\n"
    "v_cvt_pk_bf16_f32 v114, v160, v161\n"
    "v_cvt_pk_bf16_f32 v115, v162, v163\n"
    "v_mfma_f32_16x16x32_bf16 a[44:47], a[12:15], v[96:99], 0\n"
    "v_exp_f32 v156, v16\n"
    "v_mfma_f32_16x16x32_bf16 a[40:43], a[8:11], v[96:99], 0\n"
    "v_exp_f32 v157, v17\n"
    "v_mfma_f32_16x16x32_bf16 a[36:39], a[4:7], v[96:99], 0\n"
    "v_exp_f32 v158, v18\n"
    "v_mfma_f32_16x16x32_bf16 a[32:35], a[0:3], v[96:99], 0\n"
    "v_cvt_pk_bf16_f32 v96, v156, v157\n"
    "v_exp_f32 v159, v19\n"
    "v_mfma_f32_16x16x32_bf16 a[128:131], a[0:3], v[108:111], 0\n"
    "v_cvt_pk_bf16_f32 v97, v158, v159\n"
    "v_exp_f32 v160, v20\n"
    "v_mfma_f32_16x16x32_bf16 a[132:135], a[4:7], v[108:111], 0\n"
    "v_exp_f32 v161, v21\n"
    "v_mfma_f32_16x16x32_bf16 a[136:139], a[8:11], v[108:111], 0\n"
    "v_cvt_pk_bf16_f32 v98, v160, v161\n"
    "v_exp_f32 v162, v22\n"
    "v_mfma_f32_16x16x32_bf16 a[140:143], a[12:15], v[108:111], 0\n"
    "v_exp_f32 v163, v23\n"
    "v_mfma_f32_16x16x32_bf16 a[144:147], a[16:19], v[108:111], 0\n"
    "v_cvt_pk_bf16_f32 v99, v162, v163\n"
    "v_exp_f32 v156, v4\n"
    "v_mfma_f32_16x16x32_bf16 a[148:151], a[20:23], v[108:111], 0\n"
    "v_exp_f32 v157, v5\n"
    "v_mfma_f32_16x16x32_bf16 a[152:155], a[24:27], v[108:111], 0\n"
    "v_exp_f32 v158, v6\n"
    "v_mfma_f32_16x16x32_bf16 a[156:159], a[28:31], v[108:111], 0\n"
    "s_barrier\n"
    "buffer_load_dwordx4 v212, s[40:43], s61 offen offset:2048 nt lds\n"
    "v_mfma_f32_16x16x32_bf16 v[220:223], a[0:3], v[88:91], 0\n"
    "s_add_u32 m0, s57, 0x1380\n"
    "v_exp_f32 v159, v7\n"
    "v_mfma_f32_16x16x32_bf16 a[64:67], a[0:3], v[100:103], 0\n"
    "ds_read_b64_tr_b16 a[0:1], v210 offset:0\n"
    "v_exp_f32 v160, v0\n"
    "v_mfma_f32_16x16x32_bf16 a[68:71], a[4:7], v[100:103], 0\n"
    "ds_read_b64_tr_b16 a[2:3], v210 offset:4096\n"
    "buffer_load_dwordx4 v211, s[40:43], s61 offen offset:128 nt lds\n"
    "v_mfma_f32_16x16x32_bf16 v[224:227], a[4:7], v[88:91], 0\n"
    "ds_read_b64_tr_b16 a[4:5], v210 offset:8\n"
    "v_exp_f32 v161, v1\n"
    "v_mfma_f32_16x16x32_bf16 v[228:231], a[8:11], v[88:91], 0\n"
    "ds_read_b64_tr_b16 a[6:7], v210 offset:4104\n"
    "v_exp_f32 v162, v2\n"
    "v_mfma_f32_16x16x32_bf16 a[72:75], a[8:11], v[100:103], 0\n"
    "ds_read_b64_tr_b16 a[8:9], v210 offset:512\n"
    "v_exp_f32 v163, v3\n"
    "v_mfma_f32_16x16x32_bf16 a[76:79], a[12:15], v[100:103], 0\n"
    "ds_read_b64_tr_b16 a[10:11], v210 offset:4608\n"
    "buffer_load_dwordx4 v212, s[40:43], s61 offen offset:2176 nt lds\n"
    "v_mfma_f32_16x16x32_bf16 v[232:235], a[12:15], v[88:91], 0\n"
    "ds_read_b64_tr_b16 a[12:13], v210 offset:520\n"
    "v_mfma_f32_16x16x32_bf16 v[236:239], a[16:19], v[88:91], 0\n"
    "ds_read_b64_tr_b16 a[14:15], v210 offset:4616\n"
    "v_mfma_f32_16x16x32_bf16 a[80:83], a[16:19], v[100:103], 0\n"
    "ds_read_b64_tr_b16 a[16:17], v210 offset:1024\n"
    "v_mfma_f32_16x16x32_bf16 a[84:87], a[20:23], v[100:103], 0\n"
    "ds_read_b64_tr_b16 a[18:19], v210 offset:5120\n"
    "v_mfma_f32_16x16x32_bf16 v[240:243], a[20:23], v[88:91], 0\n"
    "ds_read_b64_tr_b16 a[20:21], v210 offset:1032\n"
    "v_mfma_f32_16x16x32_bf16 v[244:247], a[24:27], v[88:91], 0\n"
    "ds_read_b64_tr_b16 a[22:23], v210 offset:5128\n"
    "v_mfma_f32_16x16x32_bf16 a[88:91], a[24:27], v[100:103], 0\n"
    "ds_read_b64_tr_b16 a[24:25], v210 offset:1536\n"
    "v_mfma_f32_16x16x32_bf16 a[92:95], a[28:31], v[100:103], 0\n"
    "ds_read_b64_tr_b16 a[26:27], v210 offset:5632\n"
    "v_mfma_f32_16x16x32_bf16 v[248:251], a[28:31], v[88:91], 0\n"
    "s_waitcnt lgkmcnt(12)\n"
    "ds_read_b64_tr_b16 a[28:29], v210 offset:1544\n"
    "v_cvt_pk_bf16_f32 v88, v156, v157\n"
    "v_mfma_f32_16x16x32_bf16 v[220:223], a[0:3], v[92:95], v[220:223]\n"
    "ds_read_b64_tr_b16 a[30:31], v210 offset:5640\n"
    "v_xor_b32 v210, 0x4000, v210\n"
    "v_mfma_f32_16x16x32_bf16 v[176:179], a[0:3], v[116:119], v[176:179]\n"
    "v_cvt_pk_bf16_f32 v89, v158, v159\n"
    "v_cvt_pk_bf16_f32 v90, v160, v161\n"
    "v_mfma_f32_16x16x32_bf16 a[32:35], a[0:3], v[120:123], a[32:35]\n"
    "v_cvt_pk_bf16_f32 v91, v162, v163\n"
    "v_exp_f32 v156, v24\n"
    "v_mfma_f32_16x16x32_bf16 a[64:67], a[0:3], v[124:127], a[64:67]\n"
    "s_waitcnt lgkmcnt(12)\n"
    "v_exp_f32 v157, v25\n"
    "v_mfma_f32_16x16x32_bf16 a[68:71], a[4:7], v[124:127], a[68:71]\n"
    "v_cvt_pk_bf16_f32 v100, v156, v157\n"
    "v_exp_f32 v158, v26\n"
    "v_mfma_f32_16x16x32_bf16 a[36:39], a[4:7], v[120:123], a[36:39]\n"
    "v_exp_f32 v159, v27\n"
    "v_mfma_f32_16x16x32_bf16 v[180:183], a[4:7], v[116:119], v[180:183]\n"
    "v_cvt_pk_bf16_f32 v101, v158, v159\n"
    "v_exp_f32 v160, v28\n"
    "v_mfma_f32_16x16x32_bf16 v[224:227], a[4:7], v[92:95], v[224:227]\n"
    "s_waitcnt lgkmcnt(10)\n"
    "v_exp_f32 v161, v29\n"
    "v_mfma_f32_16x16x32_bf16 v[228:231], a[8:11], v[92:95], v[228:231]\n"
    "v_cvt_pk_bf16_f32 v102, v160, v161\n"
    "v_exp_f32 v162, v30\n"
    "v_mfma_f32_16x16x32_bf16 v[184:187], a[8:11], v[116:119], v[184:187]\n"
    "v_exp_f32 v163, v31\n"
    "v_mfma_f32_16x16x32_bf16 a[40:43], a[8:11], v[120:123], a[40:43]\n"
    "v_cvt_pk_bf16_f32 v103, v162, v163\n"
    "s_waitcnt vmcnt(8)\n"
    "v_mfma_f32_16x16x32_bf16 a[72:75], a[8:11], v[124:127], a[72:75]\n"
    "s_mov_b32 s58, s60\n"
    "v_mov_b32 v104, 0\n"
    "v_mov_b32 v105, 0\n"
    "s_waitcnt lgkmcnt(8)\n"
    ".Lgqa_T1:\n"
    "s_cbranch_execz .Lgqa_T5\n"
    "v_mfma_f32_16x16x32_bf16 a[76:79], a[12:15], v[124:127], a[76:79]\n"
    "s_mov_b32 s59, s61\n"
    "v_mov_b32 v106, 0\n"
    "v_mov_b32_e64 v107, 0\n"
    "v_mfma_f32_16x16x32_bf16 a[44:47], a[12:15], v[120:123], a[44:47]\n"
    "s_xor_b32 s62, s62, 0x8000\n"
    "ds_read_b128 v[80:83], v217 offset:32768\n"
    "s_add_u32 m0, s62, 0x10000\n"
    "v_mfma_f32_16x16x32_bf16 v[188:191], a[12:15], v[116:119], v[188:191]\n"
    "ds_read_b128 v[84:87], v217 offset:33280\n"
    "buffer_load_dwordx4 v213, s[44:47], s58 offen offset:0 nt lds\n"
    "v_mfma_f32_16x16x32_bf16 v[232:235], a[12:15], v[92:95], v[232:235]\n"
    "s_waitcnt lgkmcnt(8)\n"
    "ds_read_b128 v[32:35], v217 offset:33792\n"
    "v_exp_f32 v156, v8\n"
    "v_mfma_f32_16x16x32_bf16 v[236:239], a[16:19], v[92:95], v[236:239]\n"
    "ds_read_b128 v[36:39], v217 offset:34304\n"
    "v_exp_f32 v157, v9\n"
    "v_mfma_f32_16x16x32_bf16 v[192:195], a[16:19], v[116:119], v[192:195]\n"
    "buffer_load_dwordx4 v213, s[44:47], s58 offen offset:2048 nt lds\n"
    "v_mfma_f32_16x16x32_bf16 a[48:51], a[16:19], v[120:123], a[48:51]\n"
    "s_add_u32 m0, s62, 0x10380\n"
    "v_exp_f32 v158, v10\n"
    "v_mfma_f32_16x16x32_bf16 a[80:83], a[16:19], v[124:127], a[80:83]\n"
    "s_waitcnt lgkmcnt(8)\n"
    "v_exp_f32 v159, v11\n"
    "v_mfma_f32_16x16x32_bf16 a[84:87], a[20:23], v[124:127], a[84:87]\n"
    "buffer_load_dwordx4 v213, s[44:47], s58 offen offset:128 nt lds\n"
    "v_mfma_f32_16x16x32_bf16 a[52:55], a[20:23], v[120:123], a[52:55]\n"
    "s_waitcnt lgkmcnt(3)\n"
    "v_lshlrev_b32 v40, 16, v80\n"
    "v_and_b32 v41, 0xffff0000, v80\n"
    "v_mfma_f32_16x16x32_bf16 v[196:199], a[20:23], v[116:119], v[196:199]\n"
    "v_lshlrev_b32 v42, 16, v81\n"
    "v_and_b32 v43, 0xffff0000, v81\n"
    "v_mfma_f32_16x16x32_bf16 v[240:243], a[20:23], v[92:95], v[240:243]\n"
    "v_lshlrev_b32 v44, 16, v82\n"
    "v_and_b32 v45, 0xffff0000, v82\n"
    "v_mfma_f32_16x16x32_bf16 v[244:247], a[24:27], v[92:95], v[244:247]\n"
    "v_lshlrev_b32_e64 v46, 16, v83\n"
    "v_and_b32 v47, 0xffff0000, v83\n"
    "buffer_load_dwordx4 v213, s[44:47], s58 offen offset:2176 nt lds\n"
    "s_add_u32 m0, s62, 0x11000\n"
    "v_pk_mul_f32 v[40:41], v[40:41], s[66:67]\n"
    "v_pk_mul_f32 v[42:43], v[42:43], s[66:67]\n"
    "v_pk_mul_f32 v[44:45], v[44:45], s[66:67]\n"
    "v_pk_mul_f32 v[46:47], v[46:47], s[66:67]\n"
    "v_mfma_f32_16x16x32_bf16 v[200:203], a[24:27], v[116:119], v[200:203]\n"
    "v_cvt_pk_bf16_f32 v40, v40, v41\n"
    "v_cvt_pk_bf16_f32 v42, v42, v43\n"
    "v_mfma_f32_16x16x32_bf16 a[56:59], a[24:27], v[120:123], a[56:59]\n"
    "v_cvt_pk_bf16_f32 v44, v44, v45\n"
    "v_cvt_pk_bf16_f32 v46, v46, v47\n"
    "v_mfma_f32_16x16x32_bf16 a[88:91], a[24:27], v[124:127], a[88:91]\n"
    "v_accvgpr_write_b32 a224, v40\n"
    "v_accvgpr_write_b32 a225, v42\n"
    "v_mfma_f32_16x16x32_bf16 a[92:95], a[28:31], v[124:127], a[92:95]\n"
    "v_accvgpr_write_b32 a226, v44\n"
    "v_accvgpr_write_b32 a227, v46\n"
    "v_mfma_f32_16x16x32_bf16 a[60:63], a[28:31], v[120:123], a[60:63]\n"
    "s_waitcnt lgkmcnt(2)\n"
    "v_lshlrev_b32 v40, 16, v84\n"
    "v_and_b32 v41, 0xffff0000, v84\n"
    "v_mfma_f32_16x16x32_bf16 v[204:207], a[28:31], v[116:119], v[204:207]\n"
    "v_lshlrev_b32 v42, 16, v85\n"
    "v_and_b32 v43, 0xffff0000, v85\n"
    "v_mfma_f32_16x16x32_bf16 v[248:251], a[28:31], v[92:95], v[248:251]\n"
    "v_lshlrev_b32 v44, 16, v86\n"
    "v_and_b32 v45, 0xffff0000, v86\n"
    "v_mfma_f32_16x16x32_bf16 v[80:83], a[96:99], a[224:227], 0\n"
    "v_lshlrev_b32_e64 v46, 16, v87\n"
    "v_and_b32 v47, 0xffff0000, v87\n"
    "buffer_load_dwordx4 v213, s[44:47], s59 offen offset:0 nt lds\n"
    "v_cvt_pk_bf16_f32 v92, v156, v157\n"
    "v_pk_mul_f32 v[40:41], v[40:41], s[66:67]\n"
    "v_pk_mul_f32 v[42:43], v[42:43], s[66:67]\n"
    "v_pk_mul_f32 v[44:45], v[44:45], s[66:67]\n"
    "v_pk_mul_f32 v[46:47], v[46:47], s[66:67]\n"
    "v_cvt_pk_bf16_f32 v40, v40, v41\n"
    "v_cvt_pk_bf16_f32 v42, v42, v43\n"
    "v_cvt_pk_bf16_f32 v44, v44, v45\n"
    "v_cvt_pk_bf16_f32 v46, v46, v47\n"
    "v_accvgpr_write_b32 a228, v40\n"
    "v_accvgpr_write_b32 a229, v42\n"
    "v_accvgpr_write_b32 a230, v44\n"
    "v_accvgpr_write_b32 a231, v46\n"
    "s_waitcnt lgkmcnt(1)\n"
    "v_lshlrev_b32 v40, 16, v32\n"
    "v_and_b32 v41, 0xffff0000, v32\n"
    "v_mfma_f32_16x16x32_bf16 v[80:83], a[100:103], a[228:231], v[80:83]\n"
    "v_lshlrev_b32 v42, 16, v33\n"
    "v_and_b32 v43, 0xffff0000, v33\n"
    "v_lshlrev_b32 v44, 16, v34\n"
    "v_and_b32 v45, 0xffff0000, v34\n"
    "v_lshlrev_b32_e64 v46, 16, v35\n"
    "v_and_b32 v47, 0xffff0000, v35\n"
    "v_pk_mul_f32 v[40:41], v[40:41], s[66:67]\n"
    "v_pk_mul_f32 v[42:43], v[42:43], s[66:67]\n"
    "v_pk_mul_f32 v[44:45], v[44:45], s[66:67]\n"
    "v_pk_mul_f32 v[46:47], v[46:47], s[66:67]\n"
    "v_cvt_pk_bf16_f32 v40, v40, v41\n"
    "v_cvt_pk_bf16_f32 v42, v42, v43\n"
    "v_cvt_pk_bf16_f32 v44, v44, v45\n"
    "v_cvt_pk_bf16_f32 v46, v46, v47\n"
    "v_accvgpr_write_b32 a232, v40\n"
    "v_accvgpr_write_b32 a233, v42\n"
    "v_accvgpr_write_b32 a234, v44\n"
    "v_accvgpr_write_b32 a235, v46\n"
    "s_waitcnt lgkmcnt(0)\n"
    "v_lshlrev_b32 v40, 16, v36\n"
    "v_and_b32 v41, 0xffff0000, v36\n"
    "v_mfma_f32_16x16x32_bf16 v[80:83], a[104:107], a[232:235], v[80:83]\n"
    "v_lshlrev_b32 v42, 16, v37\n"
    "v_and_b32 v43, 0xffff0000, v37\n"
    "v_mfma_f32_16x16x32_bf16 v[164:167], v[252:255], v[88:91], v[164:167]\n"
    "v_lshlrev_b32 v44, 16, v38\n"
    "v_and_b32 v45, 0xffff0000, v38\n"
    "v_lshlrev_b32_e64 v46, 16, v39\n"
    "v_and_b32 v47, 0xffff0000, v39\n"
    "buffer_load_dwordx4 v213, s[44:47], s59 offen offset:2048 nt lds\n"
    "v_pk_mul_f32 v[40:41], v[40:41], s[66:67]\n"
    "v_pk_mul_f32 v[42:43], v[42:43], s[66:67]\n"
    "v_pk_mul_f32 v[44:45], v[44:45], s[66:67]\n"
    "v_pk_mul_f32 v[46:47], v[46:47], s[66:67]\n"
    "v_cvt_pk_bf16_f32 v40, v40, v41\n"
    "v_cvt_pk_bf16_f32 v42, v42, v43\n"
    "v_cvt_pk_bf16_f32 v44, v44, v45\n"
    "v_cvt_pk_bf16_f32 v46, v46, v47\n"
    "v_accvgpr_write_b32 a236, v40\n"
    "v_accvgpr_write_b32 a237, v42\n"
    "v_accvgpr_write_b32 a238, v44\n"
    "v_accvgpr_write_b32 a239, v46\n"
    "v_cvt_pk_bf16_f32 v93, v158, v159\n"
    "s_add_u32 m0, s62, 0x11380\n"
    "v_mfma_f32_16x16x32_bf16 v[80:83], a[108:111], a[236:239], v[80:83]\n"
    "buffer_load_dwordx4 v213, s[44:47], s59 offen offset:128 nt lds\n"
    "v_mfma_f32_16x16x32_bf16 v[84:87], a[124:127], a[236:239], 0\n"
    "v_exp_f32 v160, v12\n"
    "v_mfma_f32_16x16x32_bf16 v[84:87], a[120:123], a[232:235], v[84:87]\n"
    "v_exp_f32 v161, v13\n"
    "v_mfma_f32_16x16x32_bf16 v[84:87], a[116:119], a[228:231], v[84:87]\n"
    "v_cvt_pk_bf16_f32 v94, v160, v161\n"
    "v_exp_f32 v162, v14\n"
    "v_mfma_f32_16x16x32_bf16 v[84:87], a[112:115], a[224:227], v[84:87]\n"
    "v_exp_f32 v163, v15\n"
    "v_mfma_f32_16x16x32_bf16 v[216:219], v[252:255], v[108:111], 0\n"
    "v_cvt_pk_bf16_f32 v95, v162, v163\n"
    "buffer_load_dwordx4 v213, s[44:47], s59 offen offset:2176 nt lds\n"
    "s_waitcnt vmcnt(8)\n"
    "v_mfma_f32_16x16x32_bf16 v[164:167], v[252:255], v[92:95], v[164:167]\n"
    "s_add_u32 s60, s60, 0x8000\n"
    "ds_read_b128 a[4:7], v208 offset:512\n"
    "v_max3_f32 v46, v80, v81, v82\n"
    "v_mfma_f32_32x32x16_bf16 a[96:111], v[104:107], v[104:107], 0\n"
    "s_and_b32 s60, s60, 0x3ffff\n"
    "ds_read_b128 a[0:3], v208 offset:0\n"
    "v_max3_f32 v47, v84, v85, v86\n"
    "v_max3_f32 v46, v46, v83, v87\n"
    "v_max_f32 v46, v46, v47\n"
    "ds_read_b128 a[12:15], v208 offset:1536\n"
    "v_mov_b32 v47, v46\n"
    "v_mfma_f32_32x32x16_bf16 a[112:127], v[104:107], v[104:107], 0\n"
    "s_add_u32 s61, s60, 0x1000\n"
    "ds_read_b128 a[8:11], v208 offset:1024\n"
    "v_permlane16_swap_b32 v46, v47\n"
    "v_max_f32 v46, v46, v47\n"
    "ds_read_b128 a[20:23], v208 offset:4608\n"
    "v_mov_b32 v47, v46\n"
    "s_xor_b32 s57, s57, 0x8000\n"
    "s_add_u32 m0, s57, 0x0\n"
    "ds_read_b128 a[16:19], v208 offset:4096\n"
    "v_permlane32_swap_b32 v46, v47\n"
    "v_max_f32 v46, v46, v47\n"
    "ds_read_b128 a[28:31], v208 offset:5632\n"
    "buffer_load_dwordx4 v211, s[40:43], s60 offen offset:0 nt lds\n"
    "v_sub_f32 v148, 0, v46\n"
    "ds_read_b128 a[24:27], v208 offset:5120\n"
    "v_xor_b32 v208, 0x2000, v208\n"
    "v_sub_f32 v149, 0, v46\n"
    "v_sub_f32 v150, 0, v46\n"
    "v_sub_f32 v151, 0, v46\n"
    "v_sub_f32 v80, v80, v46\n"
    "v_sub_f32 v81, v81, v46\n"
    "s_waitcnt lgkmcnt(7)\n"
    "v_mfma_f32_16x16x32_bf16 v[52:55], a[4:7], a[180:183], v[136:139]\n"
    "s_waitcnt lgkmcnt(6)\n"
    "v_sub_f32 v82, v82, v46\n"
    "v_sub_f32 v83, v83, v46\n"
    "v_mfma_f32_16x16x32_bf16 v[4:7], a[0:3], a[160:163], v[132:135]\n"
    "v_sub_f32 v84, v84, v46\n"
    "v_sub_f32 v85, v85, v46\n"
    "v_mfma_f32_16x16x32_bf16 v[4:7], a[4:7], a[164:167], v[4:7]\n"
    "v_sub_f32 v86, v86, v46\n"
    "v_sub_f32 v87, v87, v46\n"
    "v_mfma_f32_16x16x32_bf16 v[52:55], a[0:3], a[176:179], v[52:55]\n"
    "v_exp_f32 v156, v56\n"
    "v_mfma_f32_16x16x32_bf16 v[16:19], a[0:3], a[192:195], v[140:143]\n"
    "v_exp_f32 v157, v57\n"
    "v_mfma_f32_16x16x32_bf16 v[16:19], a[4:7], a[196:199], v[16:19]\n"
    "v_cvt_pk_bf16_f32 v116, v156, v157\n"
    "v_mov_b32 v165, 0\n"
    "v_mfma_f32_16x16x32_bf16 v[24:27], a[4:7], a[212:215], v[144:147]\n"
    "v_exp_f32 v158, v58\n"
    "v_mfma_f32_16x16x32_bf16 v[24:27], a[0:3], a[208:211], v[24:27]\n"
    "buffer_load_dwordx4 v212, s[40:43], s60 offen offset:2048 nt lds\n"
    "v_mfma_f32_16x16x32_bf16 v[40:43], a[0:3], a[240:243], v[152:155]\n"
    "s_add_u32 m0, s57, 0x380\n"
    "v_exp_f32 v159, v59\n"
    "v_mfma_f32_16x16x32_bf16 v[40:43], a[4:7], a[244:247], v[40:43]\n"
    "v_cvt_pk_bf16_f32 v117, v158, v159\n"
    "v_exp_f32 v160, v60\n"
    "v_mfma_f32_16x16x32_bf16 v[32:35], a[4:7], a[228:231], v[148:151]\n"
    "v_exp_f32 v161, v61\n"
    "v_mfma_f32_16x16x32_bf16 v[168:171], v[252:255], v[112:115], v[168:171]\n"
    "v_cvt_pk_bf16_f32 v118, v160, v161\n"
    "v_exp_f32 v162, v62\n"
    "v_mfma_f32_16x16x32_bf16 v[32:35], a[0:3], a[224:227], v[32:35]\n"
    "s_waitcnt lgkmcnt(4)\n"
    "v_exp_f32 v163, v63\n"
    "v_mfma_f32_16x16x32_bf16 v[32:35], a[8:11], a[232:235], v[32:35]\n"
    "v_cvt_pk_bf16_f32 v119, v162, v163\n"
    "v_exp_f32 v156, v64\n"
    "v_mfma_f32_16x16x32_bf16 v[32:35], a[12:15], a[236:239], v[32:35]\n"
    "s_barrier\n"
    "buffer_load_dwordx4 v211, s[40:43], s60 offen offset:128 nt lds\n"
    "v_mfma_f32_16x16x32_bf16 v[168:171], v[252:255], v[116:119], v[168:171]\n"
    "v_exp_f32 v157, v65\n"
    "v_mfma_f32_16x16x32_bf16 v[40:43], a[12:15], a[252:255], v[40:43]\n"
    "ds_read_b128 a[4:7], v208 offset:512\n"
    "v_cvt_pk_bf16_f32 v120, v156, v157\n"
    "v_mfma_f32_16x16x32_bf16 v[40:43], a[8:11], a[248:251], v[40:43]\n"
    "ds_read_b128 a[0:3], v208 offset:0\n"
    "v_mov_b32 v169, 0\n"
    "v_mfma_f32_16x16x32_bf16 v[24:27], a[8:11], a[216:219], v[24:27]\n"
    "v_exp_f32 v158, v66\n"
    "v_mfma_f32_16x16x32_bf16 v[24:27], a[12:15], a[220:223], v[24:27]\n"
    "v_exp_f32 v159, v67\n"
    "v_mfma_f32_16x16x32_bf16 v[16:19], a[12:15], a[204:207], v[16:19]\n"
    "v_cvt_pk_bf16_f32 v121, v158, v159\n"
    "v_exp_f32 v160, v68\n"
    "v_mfma_f32_16x16x32_bf16 v[16:19], a[8:11], a[200:203], v[16:19]\n"
    "v_exp_f32 v161, v69\n"
    "v_mfma_f32_16x16x32_bf16 v[52:55], a[8:11], a[184:187], v[52:55]\n"
    "v_cvt_pk_bf16_f32 v122, v160, v161\n"
    "v_exp_f32 v162, v70\n"
    "v_mfma_f32_16x16x32_bf16 v[52:55], a[12:15], a[188:191], v[52:55]\n"
    "v_exp_f32 v163, v71\n"
    "v_mfma_f32_16x16x32_bf16 v[68:71], v[252:255], v[96:99], 0\n"
    "v_cvt_pk_bf16_f32 v123, v162, v163\n"
    "buffer_load_dwordx4 v212, s[40:43], s60 offen offset:2176 nt lds\n"
    "v_mfma_f32_16x16x32_bf16 v[4:7], a[12:15], a[172:175], v[4:7]\n"
    "s_add_u32 m0, s57, 0x1000\n"
    "v_exp_f32 v156, v72\n"
    "v_mfma_f32_16x16x32_bf16 v[68:71], v[252:255], v[120:123], v[68:71]\n"
    "v_exp_f32 v157, v73\n"
    "v_mfma_f32_16x16x32_bf16 v[4:7], a[8:11], a[168:171], v[4:7]\n"
    "s_waitcnt lgkmcnt(4)\n"
    "v_cvt_pk_bf16_f32 v124, v156, v157\n"
    "v_exp_f32 v158, v74\n"
    "v_mfma_f32_16x16x32_bf16 v[0:3], a[16:19], a[160:163], v[132:135]\n"
    "v_exp_f32 v159, v75\n"
    "v_mfma_f32_16x16x32_bf16 v[0:3], a[20:23], a[164:167], v[0:3]\n"
    "v_fmac_f32 v172, 0x3e800000, v68\n"
    "ds_read_b128 a[12:15], v208 offset:1536\n"
    "v_mfma_f32_16x16x32_bf16 v[48:51], a[20:23], a[180:183], v[136:139]\n"
    "v_cvt_pk_bf16_f32 v125, v158, v159\n"
    "ds_read_b128 a[8:11], v208 offset:1024\n"
    "v_mfma_f32_16x16x32_bf16 v[48:51], a[16:19], a[176:179], v[48:51]\n"
    "v_exp_f32 v160, v76\n"
    "v_mfma_f32_16x16x32_bf16 v[20:23], a[16:19], a[192:195], v[140:143]\n"
    "v_exp_f32 v161, v77\n"
    "v_mfma_f32_16x16x32_bf16 v[20:23], a[20:23], a[196:199], v[20:23]\n"
    "v_cvt_pk_bf16_f32 v126, v160, v161\n"
    "v_max3_f32 v214, v164, v168, v172\n"
    "v_mfma_f32_16x16x32_bf16 v[28:31], a[20:23], a[212:215], v[144:147]\n"
    "buffer_load_dwordx4 v211, s[40:43], s61 offen offset:0 nt lds\n"
    "v_mfma_f32_16x16x32_bf16 v[28:31], a[16:19], a[208:211], v[28:31]\n"
    "v_exp_f32 v162, v78\n"
    "v_mfma_f32_16x16x32_bf16 v[44:47], a[16:19], a[240:243], v[152:155]\n"
    "v_exp_f32 v163, v79\n"
    "v_mfma_f32_16x16x32_bf16 v[76:79], v[252:255], v[100:103], 0\n"
    "v_cvt_pk_bf16_f32 v127, v162, v163\n"
    "buffer_load_dwordx4 v212, s[40:43], s61 offen offset:2048 nt lds\n"
    "v_mfma_f32_16x16x32_bf16 v[44:47], a[20:23], a[244:247], v[44:47]\n"
    "s_add_u32 m0, s57, 0x1380\n"
    "v_exp_f32 v156, v80\n"
    "v_mfma_f32_16x16x32_bf16 v[76:79], v[252:255], v[124:127], v[76:79]\n"
    "v_exp_f32 v157, v81\n"
    "v_mfma_f32_16x16x32_bf16 v[36:39], a[20:23], a[228:231], v[148:151]\n"
    "v_add_f32 v174, v156, v157\n"
    "v_cvt_pk_bf16_f32 v128, v156, v157\n"
    "v_mfma_f32_16x16x32_bf16 v[36:39], a[16:19], a[224:227], v[36:39]\n"
    "s_waitcnt lgkmcnt(4)\n"
    "v_exp_f32 v158, v82\n"
    "v_mfma_f32_16x16x32_bf16 v[36:39], a[24:27], a[232:235], v[36:39]\n"
    "v_fmac_f32 v173, 0x3e800000, v76\n"
    "v_max_f32 v214, v214, v173\n"
    "v_mfma_f32_16x16x32_bf16 v[36:39], a[28:31], a[236:239], v[36:39]\n"
    "v_cmp_lt_f32 vcc, s64, v214\n"
    "v_add_f32 v174, v174, v158\n"
    "v_mfma_f32_16x16x32_bf16 v[44:47], a[28:31], a[252:255], v[44:47]\n"
    "v_exp_f32 v159, v83\n"
    "v_mfma_f32_16x16x32_bf16 v[44:47], a[24:27], a[248:251], v[44:47]\n"
    "v_add_f32 v174, v174, v159\n"
    "v_cvt_pk_bf16_f32 v129, v158, v159\n"
    "v_mfma_f32_16x16x32_bf16 v[28:31], a[24:27], a[216:219], v[28:31]\n"
    "v_exp_f32 v160, v84\n"
    "v_mfma_f32_16x16x32_bf16 v[28:31], a[28:31], a[220:223], v[28:31]\n"
    "v_add_f32 v174, v174, v160\n"
    "buffer_load_dwordx4 v211, s[40:43], s61 offen offset:128 nt lds\n"
    "v_mfma_f32_16x16x32_bf16 v[20:23], a[28:31], a[204:207], v[20:23]\n"
    "v_exp_f32 v161, v85\n"
    "v_mfma_f32_16x16x32_bf16 v[20:23], a[24:27], a[200:203], v[20:23]\n"
    "v_add_f32 v174, v174, v161\n"
    "v_cvt_pk_bf16_f32 v130, v160, v161\n"
    "v_mfma_f32_16x16x32_bf16 v[48:51], a[24:27], a[184:187], v[48:51]\n"
    "v_exp_f32 v162, v86\n"
    "v_mfma_f32_16x16x32_bf16 v[48:51], a[28:31], a[188:191], v[48:51]\n"
    "v_add_f32 v174, v174, v162\n"
    "v_exp_f32 v163, v87\n"
    "v_mfma_f32_16x16x32_bf16 v[0:3], a[28:31], a[172:175], v[0:3]\n"
    "v_add_f32 v174, v174, v163\n"
    "v_cvt_pk_bf16_f32 v131, v162, v163\n"
    "v_mfma_f32_16x16x32_bf16 v[0:3], a[24:27], a[168:171], v[0:3]\n"
    "v_exp_f32 v156, v40\n"
    "v_exp_f32 v157, v41\n"
    "v_exp_f32 v158, v42\n"
    "v_cvt_pk_bf16_f32 v108, v156, v157\n"
    "v_exp_f32 v159, v43\n"
    "s_nop 0\n"
    "v_cvt_pk_bf16_f32 v109, v158, v159\n"
    "s_cbranch_vccnz .Lgqa_STUB4\n"
    ".Lgqa_RET4:\n"
    "buffer_load_dwordx4 v212, s[40:43], s61 offen offset:2176 nt lds\n"
    "s_waitcnt lgkmcnt(0)\n"
    "s_branch .Lgqa_ENTRY\n"
    ".fill 695, 4, 0xbf800000\n"
    ".Lgqa_T3:\n"
    "s_branch .Lgqa_TBACK\n"
    ".Lgqa_LOOP:\n"
    "v_mfma_f32_16x16x32_bf16 v[28:31], a[28:31], a[220:223], v[28:31]\n"
    "ds_read_b128 a[8:11], v208 offset:1024\n"
    "v_add_f32 v174, v174, v163\n"
    "v_mfma_f32_16x16x32_bf16 v[28:31], a[24:27], a[216:219], v[28:31]\n"
    "v_add_f32 v174, v174, v156\n"
    "v_add_f32 v174, v174, v157\n"
    "v_mfma_f32_16x16x32_bf16 v[20:23], a[24:27], a[200:203], v[20:23]\n"
    "v_add_f32 v174, v174, v158\n"
    "v_add_f32 v174, v174, v159\n"
    "v_mfma_f32_16x16x32_bf16 v[20:23], a[28:31], a[204:207], v[20:23]\n"
    "v_max3_f32 v214, v214, v173, v174\n"
    "v_cmp_lt_f32 vcc, s64, v214\n"
    "v_mfma_f32_16x16x32_bf16 v[48:51], a[28:31], a[188:191], v[48:51]\n"
    "v_cvt_pk_bf16_f32 v80, v160, v161\n"
    "v_cvt_pk_bf16_f32 v81, v162, v163\n"
    "v_mfma_f32_16x16x32_bf16 v[48:51], a[24:27], a[184:187], v[48:51]\n"
    "v_cvt_pk_bf16_f32 v82, v156, v157\n"
    "v_cvt_pk_bf16_f32 v83, v158, v159\n"
    "v_mfma_f32_16x16x32_bf16 v[0:3], a[24:27], a[168:171], v[0:3]\n"
    "v_mfma_f32_16x16x32_bf16 v[0:3], a[28:31], a[172:175], v[0:3]\n"
    "s_waitcnt lgkmcnt(2)\n"
    "s_cbranch_vccnz .Lgqa_STUB5\n"
    ".Lgqa_RET5:\n"
    "v_mfma_f32_16x16x32_bf16 v[92:95], a[0:3], a[160:163], v[132:135]\n"
    "s_mov_b32 s58, s60\n"
    "ds_read_b128 a[20:23], v208 offset:4608\n"
    "v_mfma_f32_16x16x32_bf16 v[92:95], a[4:7], a[164:167], v[92:95]\n"
    "s_mov_b32 s59, s61\n"
    "v_exp_f32 v156, v88\n"
    "v_mfma_f32_16x16x32_bf16 v[116:119], a[4:7], a[180:183], v[136:139]\n"
    "s_xor_b32 s62, s62, 0x8000\n"
    "ds_read_b128 a[16:19], v208 offset:4096\n"
    "v_mfma_f32_16x16x32_bf16 v[116:119], a[0:3], a[176:179], v[116:119]\n"
    "s_add_u32 s60, s60, 0x8000\n"
    "v_exp_f32 v157, v89\n"
    "v_mfma_f32_16x16x32_bf16 v[120:123], a[0:3], a[192:195], v[140:143]\n"
    "s_and_b32 s60, s60, 0x3ffff\n"
    "ds_read_b128 a[28:31], v208 offset:5632\n"
    "v_cvt_pk_bf16_f32 v88, v156, v157\n"
    "v_mfma_f32_16x16x32_bf16 v[120:123], a[4:7], a[196:199], v[120:123]\n"
    "s_add_u32 s61, s60, 0x1000\n"
    "v_exp_f32 v158, v90\n"
    "v_mfma_f32_16x16x32_bf16 v[124:127], a[4:7], a[212:215], v[144:147]\n"
    "s_xor_b32 s57, s57, 0x8000\n"
    "ds_read_b128 a[24:27], v208 offset:5120\n"
    "v_xor_b32 v208, 0xe000, v208\n"
    "v_mfma_f32_16x16x32_bf16 v[124:127], a[0:3], a[208:211], v[124:127]\n"
    "v_exp_f32 v159, v91\n"
    "v_mfma_f32_16x16x32_bf16 v[128:131], a[0:3], a[224:227], v[148:151]\n"
    "v_exp_f32 v160, v0\n"
    "v_mfma_f32_16x16x32_bf16 v[128:131], a[4:7], a[228:231], v[128:131]\n"
    "s_waitcnt lgkmcnt(4)\n"
    "v_exp_f32 v161, v1\n"
    "v_mfma_f32_16x16x32_bf16 v[128:131], a[8:11], a[232:235], v[128:131]\n"
    "v_exp_f32 v162, v2\n"
    "v_mfma_f32_16x16x32_bf16 v[128:131], a[12:15], a[236:239], v[128:131]\n"
    "v_exp_f32 v163, v3\n"
    "v_mfma_f32_16x16x32_bf16 v[124:127], a[12:15], a[220:223], v[124:127]\n"
    "v_exp_f32 v156, v112\n"
    "v_mfma_f32_16x16x32_bf16 v[124:127], a[8:11], a[216:219], v[124:127]\n"
    "v_exp_f32 v157, v113\n"
    "v_mfma_f32_16x16x32_bf16 v[120:123], a[8:11], a[200:203], v[120:123]\n"
    "v_cvt_pk_bf16_f32 v89, v158, v159\n"
    "v_cvt_pk_bf16_f32 v90, v160, v161\n"
    "v_mfma_f32_16x16x32_bf16 v[120:123], a[12:15], a[204:207], v[120:123]\n"
    "s_waitcnt vmcnt(0) lgkmcnt(0)\n"
    "v_exp_f32 v158, v114\n"
    "v_mfma_f32_16x16x32_bf16 v[116:119], a[12:15], a[188:191], v[116:119]\n"
    "v_exp_f32 v159, v115\n"
    "v_mfma_f32_16x16x32_bf16 v[116:119], a[8:11], a[184:187], v[116:119]\n"
    "s_barrier\n"
    "v_cvt_pk_bf16_f32 v91, v162, v163\n"
    "v_mfma_f32_16x16x32_bf16 v[92:95], a[8:11], a[168:171], v[92:95]\n"
    "v_exp_f32 v160, v48\n"
    "v_mfma_f32_16x16x32_bf16 v[92:95], a[12:15], a[172:175], v[92:95]\n"
    "v_exp_f32 v161, v49\n"
    "v_mfma_f32_16x16x32_bf16 v[12:15], a[16:19], a[160:163], v[132:135]\n"
    "s_add_u32 m0, s62, 0x10000\n"
    "ds_read_b64_tr_b16 a[4:5], v210 offset:0\n"
    "v_cvt_pk_bf16_f32 v112, v156, v157\n"
    "v_mfma_f32_16x16x32_bf16 v[12:15], a[20:23], a[164:167], v[12:15]\n"
    "buffer_load_dwordx4 v213, s[44:47], s58 offen offset:0 nt lds\n"
    "v_mfma_f32_16x16x32_bf16 v[60:63], a[20:23], a[180:183], v[136:139]\n"
    "ds_read_b64_tr_b16 a[6:7], v210 offset:4096\n"
    "v_cvt_pk_bf16_f32 v113, v158, v159\n"
    "v_mfma_f32_16x16x32_bf16 v[60:63], a[16:19], a[176:179], v[60:63]\n"
    "v_exp_f32 v162, v50\n"
    "v_mfma_f32_16x16x32_bf16 v[68:71], a[16:19], a[192:195], v[140:143]\n"
    "ds_read_b64_tr_b16 a[0:1], v209 offset:0\n"
    "v_cvt_pk_bf16_f32 v114, v160, v161\n"
    "v_mfma_f32_16x16x32_bf16 v[68:71], a[20:23], a[196:199], v[68:71]\n"
    "v_exp_f32 v163, v51\n"
    "v_mfma_f32_16x16x32_bf16 v[76:79], a[20:23], a[212:215], v[144:147]\n"
    "ds_read_b64_tr_b16 a[2:3], v209 offset:4096\n"
    "v_cvt_pk_bf16_f32 v115, v162, v163\n"
    "v_mfma_f32_16x16x32_bf16 v[76:79], a[16:19], a[208:211], v[76:79]\n"
    "v_exp_f32 v156, v96\n"
    "v_mfma_f32_16x16x32_bf16 v[84:87], a[16:19], a[224:227], v[148:151]\n"
    "v_exp_f32 v157, v97\n"
    "v_mfma_f32_16x16x32_bf16 v[84:87], a[20:23], a[228:231], v[84:87]\n"
    "v_exp_f32 v158, v98\n"
    "v_mfma_f32_16x16x32_bf16 v[84:87], a[24:27], a[232:235], v[84:87]\n"
    "ds_read_b64_tr_b16 a[8:9], v209 offset:8\n"
    "v_add_f32 v172, v172, v156\n"
    "v_mfma_f32_16x16x32_bf16 v[84:87], a[28:31], a[236:239], v[84:87]\n"
    "buffer_load_dwordx4 v213, s[44:47], s58 offen offset:2048 nt lds\n"
    "v_mfma_f32_16x16x32_bf16 v[76:79], a[28:31], a[220:223], v[76:79]\n"
    "ds_read_b64_tr_b16 a[10:11], v209 offset:4104\n"
    "v_add_f32 v172, v172, v157\n"
    "v_mfma_f32_16x16x32_bf16 v[76:79], a[24:27], a[216:219], v[76:79]\n"
    "v_exp_f32 v159, v99\n"
    "v_mfma_f32_16x16x32_bf16 v[68:71], a[24:27], a[200:203], v[68:71]\n"
    "ds_read_b64_tr_b16 a[12:13], v210 offset:8\n"
    "v_cvt_pk_bf16_f32 v96, v156, v157\n"
    "v_mfma_f32_16x16x32_bf16 v[68:71], a[28:31], a[204:207], v[68:71]\n"
    "v_exp_f32 v160, v20\n"
    "v_mfma_f32_16x16x32_bf16 v[60:63], a[28:31], a[188:191], v[60:63]\n"
    "ds_read_b64_tr_b16 a[14:15], v210 offset:4104\n"
    "v_add_f32 v172, v172, v158\n"
    "v_mfma_f32_16x16x32_bf16 v[60:63], a[24:27], a[184:187], v[60:63]\n"
    "v_exp_f32 v161, v21\n"
    "v_mfma_f32_16x16x32_bf16 v[12:15], a[24:27], a[168:171], v[12:15]\n"
    "v_exp_f32 v162, v22\n"
    "v_mfma_f32_16x16x32_bf16 v[12:15], a[28:31], a[172:175], v[12:15]\n"
    "s_waitcnt lgkmcnt(4)\n"
    "v_exp_f32 v163, v23\n"
    "v_mfma_f32_16x16x32_bf16 a[128:131], a[0:3], v[108:111], a[128:131]\n"
    "v_exp_f32 v156, v100\n"
    "v_mfma_f32_16x16x32_bf16 v[220:223], a[0:3], v[4:7], v[220:223]\n"
    "s_add_u32 m0, s62, 0x10380\n"
    "ds_read_b64_tr_b16 a[20:21], v210 offset:512\n"
    "v_add_f32 v172, v172, v159\n"
    "v_mfma_f32_16x16x32_bf16 v[220:223], a[4:7], v[8:11], v[220:223]\n"
    "buffer_load_dwordx4 v213, s[44:47], s58 offen offset:128 nt lds\n"
    "v_mfma_f32_16x16x32_bf16 v[176:179], a[4:7], v[56:59], v[176:179]\n"
    "ds_read_b64_tr_b16 a[22:23], v210 offset:4608\n"
    "v_cvt_pk_bf16_f32 v97, v158, v159\n"
    "v_mfma_f32_16x16x32_bf16 v[176:179], a[0:3], v[52:55], v[176:179]\n"
    "v_exp_f32 v157, v101\n"
    "v_mfma_f32_16x16x32_bf16 a[32:35], a[0:3], v[16:19], a[32:35]\n"
    "ds_read_b64_tr_b16 a[16:17], v209 offset:512\n"
    "v_add_f32 v172, v172, v160\n"
    "v_mfma_f32_16x16x32_bf16 a[32:35], a[4:7], v[64:67], a[32:35]\n"
    "v_exp_f32 v158, v102\n"
    "v_mfma_f32_16x16x32_bf16 a[64:67], a[4:7], v[72:75], a[64:67]\n"
    "ds_read_b64_tr_b16 a[18:19], v209 offset:4608\n"
    "v_add_f32 v172, v172, v161\n"
    "v_mfma_f32_16x16x32_bf16 a[64:67], a[0:3], v[24:27], a[64:67]\n"
    "v_exp_f32 v159, v103\n"
    "v_mfma_f32_16x16x32_bf16 a[96:99], a[0:3], v[32:35], a[96:99]\n"
    "v_cvt_pk_bf16_f32 v98, v160, v161\n"
    "v_add_f32 v172, v172, v162\n"
    "v_mfma_f32_16x16x32_bf16 a[96:99], a[4:7], v[80:83], a[96:99]\n"
    "s_waitcnt lgkmcnt(4)\n"
    "v_exp_f32 v160, v28\n"
    "v_mfma_f32_16x16x32_bf16 a[100:103], a[12:15], v[80:83], a[100:103]\n"
    "ds_read_b64_tr_b16 a[24:25], v209 offset:520\n"
    "v_add_f32 v172, v172, v163\n"
    "v_mfma_f32_16x16x32_bf16 a[100:103], a[8:11], v[32:35], a[100:103]\n"
    "buffer_load_dwordx4 v213, s[44:47], s58 offen offset:2176 nt lds\n"
    "v_mfma_f32_16x16x32_bf16 a[68:71], a[8:11], v[24:27], a[68:71]\n"
    "ds_read_b64_tr_b16 a[26:27], v209 offset:4616\n"
    "v_cvt_pk_bf16_f32 v99, v162, v163\n"
    "v_mfma_f32_16x16x32_bf16 a[68:71], a[12:15], v[72:75], a[68:71]\n"
    "v_exp_f32 v161, v29\n"
    "v_mfma_f32_16x16x32_bf16 a[36:39], a[12:15], v[64:67], a[36:39]\n"
    "ds_read_b64_tr_b16 a[28:29], v210 offset:520\n"
    "v_add_f32 v173, v173, v156\n"
    "v_mfma_f32_16x16x32_bf16 a[36:39], a[8:11], v[16:19], a[36:39]\n"
    "v_exp_f32 v162, v30\n"
    "v_mfma_f32_16x16x32_bf16 v[180:183], a[8:11], v[52:55], v[180:183]\n"
    "ds_read_b64_tr_b16 a[30:31], v210 offset:4616\n"
    "v_add_f32 v173, v173, v157\n"
    "v_mfma_f32_16x16x32_bf16 v[180:183], a[12:15], v[56:59], v[180:183]\n"
    "v_exp_f32 v163, v31\n"
    "v_mfma_f32_16x16x32_bf16 v[224:227], a[12:15], v[8:11], v[224:227]\n"
    "s_add_u32 m0, s62, 0x11000\n"
    "v_cvt_pk_bf16_f32 v100, v156, v157\n"
    "v_add_f32 v173, v173, v158\n"
    "v_mfma_f32_16x16x32_bf16 v[224:227], a[8:11], v[4:7], v[224:227]\n"
    "buffer_load_dwordx4 v213, s[44:47], s59 offen offset:0 nt lds\n"
    "v_mfma_f32_16x16x32_bf16 a[132:135], a[8:11], v[108:111], a[132:135]\n"
    "s_waitcnt lgkmcnt(4)\n"
    "v_exp_f32 v156, v104\n"
    "v_mfma_f32_16x16x32_bf16 a[136:139], a[16:19], v[108:111], a[136:139]\n"
    "v_exp_f32 v157, v105\n"
    "v_mfma_f32_16x16x32_bf16 v[228:231], a[16:19], v[4:7], v[228:231]\n"
    "ds_read_b64_tr_b16 a[4:5], v210 offset:1024\n"
    "v_add_f32 v173, v173, v159\n"
    "v_mfma_f32_16x16x32_bf16 v[228:231], a[20:23], v[8:11], v[228:231]\n"
    "v_cvt_pk_bf16_f32 v101, v158, v159\n"
    "v_add_f32 v173, v173, v160\n"
    "v_mfma_f32_16x16x32_bf16 v[184:187], a[20:23], v[56:59], v[184:187]\n"
    "ds_read_b64_tr_b16 a[6:7], v210 offset:5120\n"
    "v_add_f32 v173, v173, v161\n"
    "v_mfma_f32_16x16x32_bf16 v[184:187], a[16:19], v[52:55], v[184:187]\n"
    "v_exp_f32 v158, v106\n"
    "v_mfma_f32_16x16x32_bf16 a[40:43], a[16:19], v[16:19], a[40:43]\n"
    "ds_read_b64_tr_b16 a[0:1], v209 offset:1024\n"
    "v_cvt_pk_bf16_f32 v102, v160, v161\n"
    "v_mfma_f32_16x16x32_bf16 a[40:43], a[20:23], v[64:67], a[40:43]\n"
    "v_exp_f32 v159, v107\n"
    "v_mfma_f32_16x16x32_bf16 a[72:75], a[20:23], v[72:75], a[72:75]\n"
    "ds_read_b64_tr_b16 a[2:3], v209 offset:5120\n"
    "v_add_f32 v173, v173, v162\n"
    "v_mfma_f32_16x16x32_bf16 a[72:75], a[16:19], v[24:27], a[72:75]\n"
    "buffer_load_dwordx4 v213, s[44:47], s59 offen offset:2048 nt lds\n"
    "v_mfma_f32_16x16x32_bf16 a[104:107], a[16:19], v[32:35], a[104:107]\n"
    "v_exp_f32 v160, v36\n"
    "v_mfma_f32_16x16x32_bf16 a[104:107], a[20:23], v[80:83], a[104:107]\n"
    "s_waitcnt lgkmcnt(4)\n"
    "v_exp_f32 v161, v37\n"
    "v_mfma_f32_16x16x32_bf16 a[108:111], a[28:31], v[80:83], a[108:111]\n"
    "ds_read_b64_tr_b16 a[8:9], v209 offset:1032\n"
    "v_add_f32 v173, v173, v163\n"
    "v_mfma_f32_16x16x32_bf16 a[108:111], a[24:27], v[32:35], a[108:111]\n"
    "v_cvt_pk_bf16_f32 v103, v162, v163\n"
    "v_add_f32 v174, v174, v156\n"
    "v_mfma_f32_16x16x32_bf16 a[76:79], a[24:27], v[24:27], a[76:79]\n"
    "ds_read_b64_tr_b16 a[10:11], v209 offset:5128\n"
    "v_add_f32 v174, v174, v157\n"
    "v_mfma_f32_16x16x32_bf16 a[76:79], a[28:31], v[72:75], a[76:79]\n"
    "v_exp_f32 v162, v38\n"
    "v_mfma_f32_16x16x32_bf16 a[44:47], a[28:31], v[64:67], a[44:47]\n"
    "ds_read_b64_tr_b16 a[12:13], v210 offset:1032\n"
    "v_cvt_pk_bf16_f32 v104, v156, v157\n"
    "v_mfma_f32_16x16x32_bf16 a[44:47], a[24:27], v[16:19], a[44:47]\n"
    "v_exp_f32 v163, v39\n"
    "v_mfma_f32_16x16x32_bf16 v[188:191], a[24:27], v[52:55], v[188:191]\n"
    "s_add_u32 m0, s62, 0x11380\n"
    "ds_read_b64_tr_b16 a[14:15], v210 offset:5128\n"
    "v_add_f32 v174, v174, v158\n"
    "v_mfma_f32_16x16x32_bf16 v[188:191], a[28:31], v[56:59], v[188:191]\n"
    "buffer_load_dwordx4 v213, s[44:47], s59 offen offset:128 nt lds\n"
    "v_mfma_f32_16x16x32_bf16 v[232:235], a[28:31], v[8:11], v[232:235]\n"
    "v_exp_f32 v156, v92\n"
    "v_mfma_f32_16x16x32_bf16 v[232:235], a[24:27], v[4:7], v[232:235]\n"
    "v_exp_f32 v157, v93\n"
    "v_mfma_f32_16x16x32_bf16 a[140:143], a[24:27], v[108:111], a[140:143]\n"
    "s_waitcnt lgkmcnt(4)\n"
    "v_add_f32 v174, v174, v159\n"
    "v_cvt_pk_bf16_f32 v105, v158, v159\n"
    "v_mfma_f32_16x16x32_bf16 a[144:147], a[0:3], v[108:111], a[144:147]\n"
    "v_exp_f32 v158, v94\n"
    "v_mfma_f32_16x16x32_bf16 v[236:239], a[0:3], v[4:7], v[236:239]\n"
    "ds_read_b64_tr_b16 a[20:21], v210 offset:1536\n"
    "v_add_f32 v174, v174, v160\n"
    "v_mfma_f32_16x16x32_bf16 v[236:239], a[4:7], v[8:11], v[236:239]\n"
    "v_exp_f32 v159, v95\n"
    "v_mfma_f32_16x16x32_bf16 v[192:195], a[4:7], v[56:59], v[192:195]\n"
    "ds_read_b64_tr_b16 a[22:23], v210 offset:5632\n"
    "v_add_f32 v174, v174, v161\n"
    "v_mfma_f32_16x16x32_bf16 v[192:195], a[0:3], v[52:55], v[192:195]\n"
    "v_cvt_pk_bf16_f32 v106, v160, v161\n"
    "v_add_f32 v174, v174, v162\n"
    "v_mfma_f32_16x16x32_bf16 a[48:51], a[0:3], v[16:19], a[48:51]\n"
    "ds_read_b64_tr_b16 a[16:17], v209 offset:1536\n"
    "v_add_f32 v174, v174, v163\n"
    "v_mfma_f32_16x16x32_bf16 a[48:51], a[4:7], v[64:67], a[48:51]\n"
    "buffer_load_dwordx4 v213, s[44:47], s59 offen offset:2176 nt lds\n"
    "v_mfma_f32_16x16x32_bf16 a[80:83], a[4:7], v[72:75], a[80:83]\n"
    "ds_read_b64_tr_b16 a[18:19], v209 offset:5632\n"
    "v_cvt_pk_bf16_f32 v107, v162, v163\n"
    "v_mfma_f32_16x16x32_bf16 a[80:83], a[0:3], v[24:27], a[80:83]\n"
    "v_exp_f32 v160, v12\n"
    "v_mfma_f32_16x16x32_bf16 a[112:115], a[0:3], v[32:35], a[112:115]\n"
    "v_exp_f32 v161, v13\n"
    "v_mfma_f32_16x16x32_bf16 a[112:115], a[4:7], v[80:83], a[112:115]\n"
    "s_waitcnt lgkmcnt(4)\n"
    "v_exp_f32 v162, v14\n"
    "v_mfma_f32_16x16x32_bf16 a[116:119], a[12:15], v[80:83], a[116:119]\n"
    "ds_read_b64_tr_b16 a[24:25], v209 offset:1544\n"
    "v_cvt_pk_bf16_f32 v92, v156, v157\n"
    "v_mfma_f32_16x16x32_bf16 a[116:119], a[8:11], v[32:35], a[116:119]\n"
    "v_exp_f32 v163, v15\n"
    "v_mfma_f32_16x16x32_bf16 a[84:87], a[8:11], v[24:27], a[84:87]\n"
    "ds_read_b64_tr_b16 a[26:27], v209 offset:5640\n"
    "v_cvt_pk_bf16_f32 v93, v158, v159\n"
    "v_mfma_f32_16x16x32_bf16 a[84:87], a[12:15], v[72:75], a[84:87]\n"
    "v_exp_f32 v156, v116\n"
    "v_mfma_f32_16x16x32_bf16 a[52:55], a[12:15], v[64:67], a[52:55]\n"
    "s_add_u32 m0, s57, 0x0\n"
    "ds_read_b64_tr_b16 a[28:29], v210 offset:1544\n"
    "v_cvt_pk_bf16_f32 v94, v160, v161\n"
    "v_mfma_f32_16x16x32_bf16 a[52:55], a[8:11], v[16:19], a[52:55]\n"
    "buffer_load_dwordx4 v211, s[40:43], s60 offen offset:0 nt lds\n"
    "v_mfma_f32_16x16x32_bf16 v[196:199], a[8:11], v[52:55], v[196:199]\n"
    "ds_read_b64_tr_b16 a[30:31], v210 offset:5640\n"
    "v_xor_b32 v209, 0x4000, v209\n"
    "v_mfma_f32_16x16x32_bf16 v[196:199], a[12:15], v[56:59], v[196:199]\n"
    "v_exp_f32 v157, v117\n"
    "v_mfma_f32_16x16x32_bf16 v[240:243], a[12:15], v[8:11], v[240:243]\n"
    "v_exp_f32 v158, v118\n"
    "v_mfma_f32_16x16x32_bf16 v[240:243], a[8:11], v[4:7], v[240:243]\n"
    "v_exp_f32 v159, v119\n"
    "v_mfma_f32_16x16x32_bf16 a[148:151], a[8:11], v[108:111], a[148:151]\n"
    "s_waitcnt lgkmcnt(4)\n"
    "v_exp_f32 v160, v60\n"
    "v_mfma_f32_16x16x32_bf16 a[152:155], a[16:19], v[108:111], a[152:155]\n"
    "v_exp_f32 v161, v61\n"
    "v_mfma_f32_16x16x32_bf16 v[244:247], a[16:19], v[4:7], v[244:247]\n"
    "ds_read_b128 a[4:7], v208 offset:512\n"
    "v_xor_b32 v210, 0x4000, v210\n"
    "v_mfma_f32_16x16x32_bf16 v[244:247], a[20:23], v[8:11], v[244:247]\n"
    "v_cvt_pk_bf16_f32 v95, v162, v163\n"
    "v_cvt_pk_bf16_f32 v116, v156, v157\n"
    "v_mfma_f32_16x16x32_bf16 v[200:203], a[20:23], v[56:59], v[200:203]\n"
    "ds_read_b128 a[0:3], v208 offset:0\n"
    "v_cvt_pk_bf16_f32 v117, v158, v159\n"
    "v_mfma_f32_16x16x32_bf16 v[200:203], a[16:19], v[52:55], v[200:203]\n"
    "buffer_load_dwordx4 v212, s[40:43], s60 offen offset:2048 nt lds\n"
    "v_mfma_f32_16x16x32_bf16 a[56:59], a[16:19], v[16:19], a[56:59]\n"
    "v_exp_f32 v162, v62\n"
    "v_mfma_f32_16x16x32_bf16 a[56:59], a[20:23], v[64:67], a[56:59]\n"
    "v_exp_f32 v163, v63\n"
    "v_mfma_f32_16x16x32_bf16 a[88:91], a[20:23], v[72:75], a[88:91]\n"
    "v_exp_f32 v156, v120\n"
    "v_mfma_f32_16x16x32_bf16 a[88:91], a[16:19], v[24:27], a[88:91]\n"
    "v_exp_f32 v157, v121\n"
    "v_mfma_f32_16x16x32_bf16 a[120:123], a[16:19], v[32:35], a[120:123]\n"
    "v_exp_f32 v158, v122\n"
    "v_mfma_f32_16x16x32_bf16 a[120:123], a[20:23], v[80:83], a[120:123]\n"
    "s_waitcnt lgkmcnt(2)\n"
    "v_exp_f32 v159, v123\n"
    "v_mfma_f32_16x16x32_bf16 a[124:127], a[28:31], v[80:83], a[124:127]\n"
    "ds_read_b128 a[12:15], v208 offset:1536\n"
    "v_cvt_pk_bf16_f32 v118, v160, v161\n"
    "v_mfma_f32_16x16x32_bf16 a[124:127], a[24:27], v[32:35], a[124:127]\n"
    "v_exp_f32 v160, v68\n"
    "v_mfma_f32_16x16x32_bf16 a[92:95], a[24:27], v[24:27], a[92:95]\n"
    "s_add_u32 m0, s57, 0x380\n"
    "ds_read_b128 a[8:11], v208 offset:1024\n"
    "v_cvt_pk_bf16_f32 v119, v162, v163\n"
    "v_mfma_f32_16x16x32_bf16 a[92:95], a[28:31], v[72:75], a[92:95]\n"
    "buffer_load_dwordx4 v211, s[40:43], s60 offen offset:128 nt lds\n"
    "v_mfma_f32_16x16x32_bf16 a[60:63], a[28:31], v[64:67], a[60:63]\n"
    "v_exp_f32 v161, v69\n"
    "v_mfma_f32_16x16x32_bf16 a[60:63], a[24:27], v[16:19], a[60:63]\n"
    "v_exp_f32 v162, v70\n"
    "v_mfma_f32_16x16x32_bf16 v[204:207], a[24:27], v[52:55], v[204:207]\n"
    "v_exp_f32 v163, v71\n"
    "v_mfma_f32_16x16x32_bf16 v[204:207], a[28:31], v[56:59], v[204:207]\n"
    "v_add_f32 v172, v172, v156\n"
    "v_add_f32 v172, v172, v157\n"
    "v_mfma_f32_16x16x32_bf16 v[248:251], a[28:31], v[8:11], v[248:251]\n"
    "v_cvt_pk_bf16_f32 v120, v156, v157\n"
    "v_add_f32 v172, v172, v158\n"
    "v_mfma_f32_16x16x32_bf16 v[248:251], a[24:27], v[4:7], v[248:251]\n"
    "v_exp_f32 v156, v124\n"
    "v_mfma_f32_16x16x32_bf16 a[156:159], a[24:27], v[108:111], a[156:159]\n"
    "v_exp_f32 v157, v125\n"
    "v_smfmac_f32_16x16x64_bf16 v[164:167], v[252:255], v[88:95], v175\n"
    "s_waitcnt lgkmcnt(2)\n"
    "v_add_f32 v172, v172, v159\n"
    "v_mfma_f32_16x16x32_bf16 v[4:7], a[0:3], a[160:163], v[132:135]\n"
    "ds_read_b128 a[20:23], v208 offset:4608\n"
    "v_cvt_pk_bf16_f32 v121, v158, v159\n"
    "v_mfma_f32_16x16x32_bf16 v[4:7], a[4:7], a[164:167], v[4:7]\n"
    "buffer_load_dwordx4 v212, s[40:43], s60 offen offset:2176 nt lds\n"
    "v_mfma_f32_16x16x32_bf16 v[52:55], a[4:7], a[180:183], v[136:139]\n"
    "ds_read_b128 a[16:19], v208 offset:4096\n"
    "v_add_f32 v172, v172, v160\n"
    "v_mfma_f32_16x16x32_bf16 v[52:55], a[0:3], a[176:179], v[52:55]\n"
    "v_exp_f32 v158, v126\n"
    "v_mfma_f32_16x16x32_bf16 v[16:19], a[0:3], a[192:195], v[140:143]\n"
    "v_exp_f32 v159, v127\n"
    "v_mfma_f32_16x16x32_bf16 v[16:19], a[4:7], a[196:199], v[16:19]\n"
    "v_add_f32 v172, v172, v161\n"
    "v_cvt_pk_bf16_f32 v122, v160, v161\n"
    "v_mfma_f32_16x16x32_bf16 v[24:27], a[4:7], a[212:215], v[144:147]\n"
    "v_exp_f32 v160, v76\n"
    "v_mfma_f32_16x16x32_bf16 v[24:27], a[0:3], a[208:211], v[24:27]\n"
    "v_exp_f32 v161, v77\n"
    "v_mfma_f32_16x16x32_bf16 v[32:35], a[0:3], a[224:227], v[148:151]\n"
    "v_add_f32 v172, v172, v162\n"
    "v_add_f32 v172, v172, v163\n"
    "v_mfma_f32_16x16x32_bf16 v[32:35], a[4:7], a[228:231], v[32:35]\n"
    "v_cvt_pk_bf16_f32 v123, v162, v163\n"
    "v_add_f32 v173, v173, v156\n"
    "v_mfma_f32_16x16x32_bf16 v[40:43], a[4:7], a[244:247], v[152:155]\n"
    "v_exp_f32 v162, v78\n"
    "v_mfma_f32_16x16x32_bf16 v[40:43], a[0:3], a[240:243], v[40:43]\n"
    "s_waitcnt lgkmcnt(2)\n"
    "v_exp_f32 v163, v79\n"
    "v_mfma_f32_16x16x32_bf16 v[40:43], a[8:11], a[248:251], v[40:43]\n"
    "s_add_u32 m0, s57, 0x1000\n"
    "ds_read_b128 a[28:31], v208 offset:5632\n"
    "v_add_f32 v173, v173, v157\n"
    "v_mfma_f32_16x16x32_bf16 v[40:43], a[12:15], a[252:255], v[40:43]\n"
    "buffer_load_dwordx4 v211, s[40:43], s61 offen offset:0 nt lds\n"
    "v_mfma_f32_16x16x32_bf16 v[32:35], a[12:15], a[236:239], v[32:35]\n"
    "ds_read_b128 a[24:27], v208 offset:5120\n"
    "v_xor_b32 v208, 0x2000, v208\n"
    "v_mfma_f32_16x16x32_bf16 v[32:35], a[8:11], a[232:235], v[32:35]\n"
    "v_cvt_pk_bf16_f32 v124, v156, v157\n"
    "v_add_f32 v173, v173, v158\n"
    "v_mfma_f32_16x16x32_bf16 v[24:27], a[8:11], a[216:219], v[24:27]\n"
    "v_exp_f32 v156, v128\n"
    "v_mfma_f32_16x16x32_bf16 v[24:27], a[12:15], a[220:223], v[24:27]\n"
    "v_exp_f32 v157, v129\n"
    "v_mfma_f32_16x16x32_bf16 v[16:19], a[12:15], a[204:207], v[16:19]\n"
    "v_add_f32 v173, v173, v159\n"
    "v_cvt_pk_bf16_f32 v125, v158, v159\n"
    "v_mfma_f32_16x16x32_bf16 v[16:19], a[8:11], a[200:203], v[16:19]\n"
    "v_exp_f32 v158, v130\n"
    "v_mfma_f32_16x16x32_bf16 v[52:55], a[8:11], a[184:187], v[52:55]\n"
    "v_exp_f32 v159, v131\n"
    "v_mfma_f32_16x16x32_bf16 v[52:55], a[12:15], a[188:191], v[52:55]\n"
    "buffer_load_dwordx4 v212, s[40:43], s61 offen offset:2048 nt lds\n"
    "v_mfma_f32_16x16x32_bf16 v[4:7], a[12:15], a[172:175], v[4:7]\n"
    "v_add_f32 v173, v173, v160\n"
    "v_add_f32 v173, v173, v161\n"
    "v_mfma_f32_16x16x32_bf16 v[4:7], a[8:11], a[168:171], v[4:7]\n"
    "v_cvt_pk_bf16_f32 v126, v160, v161\n"
    "v_add_f32 v173, v173, v162\n"
    "v_smfmac_f32_16x16x64_bf16 v[168:171], v[252:255], v[112:119], v175\n"
    "s_waitcnt lgkmcnt(2)\n"
    "v_add_f32 v173, v173, v163\n"
    "v_mfma_f32_16x16x32_bf16 v[0:3], a[16:19], a[160:163], v[132:135]\n"
    "ds_read_b128 a[4:7], v208 offset:512\n"
    "v_cvt_pk_bf16_f32 v127, v162, v163\n"
    "v_mfma_f32_16x16x32_bf16 v[0:3], a[20:23], a[164:167], v[0:3]\n"
    "v_exp_f32 v160, v84\n"
    "v_mfma_f32_16x16x32_bf16 v[48:51], a[20:23], a[180:183], v[136:139]\n"
    "ds_read_b128 a[0:3], v208 offset:0\n"
    "v_max3_f32 v214, v164, v165, v168\n"
    "v_mfma_f32_16x16x32_bf16 v[48:51], a[16:19], a[176:179], v[48:51]\n"
    "v_exp_f32 v161, v85\n"
    "v_mfma_f32_16x16x32_bf16 v[20:23], a[16:19], a[192:195], v[140:143]\n"
    "s_add_u32 m0, s57, 0x1380\n"
    "v_exp_f32 v162, v86\n"
    "v_mfma_f32_16x16x32_bf16 v[20:23], a[20:23], a[196:199], v[20:23]\n"
    "buffer_load_dwordx4 v211, s[40:43], s61 offen offset:128 nt lds\n"
    "v_mfma_f32_16x16x32_bf16 v[28:31], a[20:23], a[212:215], v[144:147]\n"
    "v_exp_f32 v163, v87\n"
    "v_mfma_f32_16x16x32_bf16 v[28:31], a[16:19], a[208:211], v[28:31]\n"
    "v_max3_f32 v214, v214, v169, v172\n"
    "v_add_f32 v174, v174, v156\n"
    "v_mfma_f32_16x16x32_bf16 v[36:39], a[16:19], a[224:227], v[148:151]\n"
    "v_add_f32 v174, v174, v157\n"
    "v_cvt_pk_bf16_f32 v128, v156, v157\n"
    "v_mfma_f32_16x16x32_bf16 v[36:39], a[20:23], a[228:231], v[36:39]\n"
    "v_exp_f32 v156, v40\n"
    "v_mfma_f32_16x16x32_bf16 v[44:47], a[20:23], a[244:247], v[152:155]\n"
    "v_exp_f32 v157, v41\n"
    "v_mfma_f32_16x16x32_bf16 v[44:47], a[16:19], a[240:243], v[44:47]\n"
    "s_waitcnt lgkmcnt(2)\n"
    "v_add_f32 v174, v174, v158\n"
    "v_add_f32 v174, v174, v159\n"
    "v_mfma_f32_16x16x32_bf16 v[44:47], a[24:27], a[248:251], v[44:47]\n"
    "ds_read_b128 a[12:15], v208 offset:1536\n"
    "v_cvt_pk_bf16_f32 v129, v158, v159\n"
    "v_mfma_f32_16x16x32_bf16 v[44:47], a[28:31], a[252:255], v[44:47]\n"
    "v_exp_f32 v158, v42\n"
    "v_mfma_f32_16x16x32_bf16 v[36:39], a[28:31], a[236:239], v[36:39]\n"
    "ds_read_b128 a[8:11], v208 offset:1024\n"
    "v_add_f32 v174, v174, v160\n"
    "v_mfma_f32_16x16x32_bf16 v[36:39], a[24:27], a[232:235], v[36:39]\n"
    "buffer_load_dwordx4 v212, s[40:43], s61 offen offset:2176 nt lds\n"
    "v_mfma_f32_16x16x32_bf16 v[28:31], a[24:27], a[216:219], v[28:31]\n"
    "v_exp_f32 v159, v43\n"
    "v_mfma_f32_16x16x32_bf16 v[28:31], a[28:31], a[220:223], v[28:31]\n"
    "v_add_f32 v174, v174, v161\n"
    "v_add_f32 v174, v174, v162\n"
    "v_mfma_f32_16x16x32_bf16 v[20:23], a[28:31], a[204:207], v[20:23]\n"
    "v_add_f32 v174, v174, v163\n"
    "v_max3_f32 v214, v214, v173, v174\n"
    "v_mfma_f32_16x16x32_bf16 v[20:23], a[24:27], a[200:203], v[20:23]\n"
    "v_cmp_lt_f32 vcc, s64, v214\n"
    "v_cvt_pk_bf16_f32 v130, v160, v161\n"
    "v_mfma_f32_16x16x32_bf16 v[48:51], a[24:27], a[184:187], v[48:51]\n"
    "v_cvt_pk_bf16_f32 v131, v162, v163\n"
    "v_cvt_pk_bf16_f32 v108, v156, v157\n"
    "v_mfma_f32_16x16x32_bf16 v[48:51], a[28:31], a[188:191], v[48:51]\n"
    ".Lgqa_T2:\n"
    "s_cbranch_execz .Lgqa_TBACK\n"
    "v_cvt_pk_bf16_f32 v109, v158, v159\n"
    "v_mfma_f32_16x16x32_bf16 v[0:3], a[28:31], a[172:175], v[0:3]\n"
    "v_mfma_f32_16x16x32_bf16 v[0:3], a[24:27], a[168:171], v[0:3]\n"
    "s_waitcnt lgkmcnt(2)\n"
    "s_cbranch_vccnz .Lgqa_STUB6\n"
    ".Lgqa_RET6:\n"
    ".Lgqa_ENTRY:\n"
    "v_mfma_f32_16x16x32_bf16 v[8:11], a[0:3], a[160:163], v[132:135]\n"
    "ds_read_b128 a[20:23], v208 offset:4608\n"
    "v_mfma_f32_16x16x32_bf16 v[8:11], a[4:7], a[164:167], v[8:11]\n"
    "v_exp_f32 v156, v4\n"
    "v_mfma_f32_16x16x32_bf16 v[56:59], a[4:7], a[180:183], v[136:139]\n"
    "ds_read_b128 a[16:19], v208 offset:4096\n"
    "v_mfma_f32_16x16x32_bf16 v[56:59], a[0:3], a[176:179], v[56:59]\n"
    "v_exp_f32 v157, v5\n"
    "v_mfma_f32_16x16x32_bf16 v[64:67], a[0:3], a[192:195], v[140:143]\n"
    "ds_read_b128 a[28:31], v208 offset:5632\n"
    "v_cvt_pk_bf16_f32 v4, v156, v157\n"
    "v_mfma_f32_16x16x32_bf16 v[64:67], a[4:7], a[196:199], v[64:67]\n"
    "v_exp_f32 v158, v6\n"
    "v_mfma_f32_16x16x32_bf16 v[72:75], a[4:7], a[212:215], v[144:147]\n"
    "ds_read_b128 a[24:27], v208 offset:5120\n"
    "v_xor_b32 v208, 0x6000, v208\n"
    "v_mfma_f32_16x16x32_bf16 v[72:75], a[0:3], a[208:211], v[72:75]\n"
    "v_exp_f32 v159, v7\n"
    "v_mfma_f32_16x16x32_bf16 v[80:83], a[0:3], a[224:227], v[148:151]\n"
    "v_exp_f32 v160, v0\n"
    "v_mfma_f32_16x16x32_bf16 v[80:83], a[4:7], a[228:231], v[80:83]\n"
    "s_waitcnt lgkmcnt(4)\n"
    "v_exp_f32 v161, v1\n"
    "v_mfma_f32_16x16x32_bf16 v[80:83], a[8:11], a[232:235], v[80:83]\n"
    "v_exp_f32 v162, v2\n"
    "v_mfma_f32_16x16x32_bf16 v[80:83], a[12:15], a[236:239], v[80:83]\n"
    "v_exp_f32 v163, v3\n"
    "v_mfma_f32_16x16x32_bf16 v[72:75], a[12:15], a[220:223], v[72:75]\n"
    "v_exp_f32 v156, v52\n"
    "v_mfma_f32_16x16x32_bf16 v[72:75], a[8:11], a[216:219], v[72:75]\n"
    "v_exp_f32 v157, v53\n"
    "v_mfma_f32_16x16x32_bf16 v[64:67], a[8:11], a[200:203], v[64:67]\n"
    "v_cvt_pk_bf16_f32 v5, v158, v159\n"
    "v_cvt_pk_bf16_f32 v6, v160, v161\n"
    "v_mfma_f32_16x16x32_bf16 v[64:67], a[12:15], a[204:207], v[64:67]\n"
    "v_exp_f32 v158, v54\n"
    "v_mfma_f32_16x16x32_bf16 v[56:59], a[12:15], a[188:191], v[56:59]\n"
    "v_exp_f32 v159, v55\n"
    "v_mfma_f32_16x16x32_bf16 v[56:59], a[8:11], a[184:187], v[56:59]\n"
    "v_exp_f32 v160, v48\n"
    "v_mfma_f32_16x16x32_bf16 v[8:11], a[8:11], a[168:171], v[8:11]\n"
    "v_exp_f32 v161, v49\n"
    "v_mfma_f32_16x16x32_bf16 v[8:11], a[12:15], a[172:175], v[8:11]\n"
    "s_waitcnt lgkmcnt(2)\n"
    "v_cvt_pk_bf16_f32 v7, v162, v163\n"
    "v_cvt_pk_bf16_f32 v52, v156, v157\n"
    "v_mfma_f32_16x16x32_bf16 v[12:15], a[16:19], a[160:163], v[132:135]\n"
    "ds_read_b64_tr_b16 a[4:5], v210 offset:0\n"
    "v_cvt_pk_bf16_f32 v53, v158, v159\n"
    "v_mfma_f32_16x16x32_bf16 v[12:15], a[20:23], a[164:167], v[12:15]\n"
    "v_exp_f32 v162, v50\n"
    "v_mfma_f32_16x16x32_bf16 v[60:63], a[20:23], a[180:183], v[136:139]\n"
    "ds_read_b64_tr_b16 a[6:7], v210 offset:4096\n"
    "v_cvt_pk_bf16_f32 v54, v160, v161\n"
    "v_mfma_f32_16x16x32_bf16 v[60:63], a[16:19], a[176:179], v[60:63]\n"
    "v_exp_f32 v163, v51\n"
    "v_mfma_f32_16x16x32_bf16 v[68:71], a[16:19], a[192:195], v[140:143]\n"
    "ds_read_b64_tr_b16 a[0:1], v209 offset:0\n"
    "v_cvt_pk_bf16_f32 v55, v162, v163\n"
    "v_mfma_f32_16x16x32_bf16 v[68:71], a[20:23], a[196:199], v[68:71]\n"
    "v_exp_f32 v156, v44\n"
    "v_mfma_f32_16x16x32_bf16 v[76:79], a[20:23], a[212:215], v[144:147]\n"
    "ds_read_b64_tr_b16 a[2:3], v209 offset:4096\n"
    "v_mfma_f32_16x16x32_bf16 v[76:79], a[16:19], a[208:211], v[76:79]\n"
    "v_exp_f32 v157, v45\n"
    "v_mfma_f32_16x16x32_bf16 v[84:87], a[16:19], a[224:227], v[148:151]\n"
    "v_exp_f32 v158, v46\n"
    "v_mfma_f32_16x16x32_bf16 v[84:87], a[20:23], a[228:231], v[84:87]\n"
    "s_waitcnt lgkmcnt(4)\n"
    "v_exp_f32 v159, v47\n"
    "v_mfma_f32_16x16x32_bf16 v[84:87], a[24:27], a[232:235], v[84:87]\n"
    "ds_read_b64_tr_b16 a[8:9], v209 offset:8\n"
    "v_cvt_pk_bf16_f32 v110, v156, v157\n"
    "v_mfma_f32_16x16x32_bf16 v[84:87], a[28:31], a[236:239], v[84:87]\n"
    "v_exp_f32 v160, v16\n"
    "v_mfma_f32_16x16x32_bf16 v[76:79], a[28:31], a[220:223], v[76:79]\n"
    "ds_read_b64_tr_b16 a[10:11], v209 offset:4104\n"
    "v_cvt_pk_bf16_f32 v111, v158, v159\n"
    "v_mfma_f32_16x16x32_bf16 v[76:79], a[24:27], a[216:219], v[76:79]\n"
    "v_exp_f32 v161, v17\n"
    "v_mfma_f32_16x16x32_bf16 v[68:71], a[24:27], a[200:203], v[68:71]\n"
    "ds_read_b64_tr_b16 a[12:13], v210 offset:8\n"
    "v_add_f32 v172, v172, v160\n"
    "v_mfma_f32_16x16x32_bf16 v[68:71], a[28:31], a[204:207], v[68:71]\n"
    "v_exp_f32 v162, v18\n"
    "v_mfma_f32_16x16x32_bf16 v[60:63], a[28:31], a[188:191], v[60:63]\n"
    "ds_read_b64_tr_b16 a[14:15], v210 offset:4104\n"
    "v_add_f32 v172, v172, v161\n"
    "v_mfma_f32_16x16x32_bf16 v[60:63], a[24:27], a[184:187], v[60:63]\n"
    "v_exp_f32 v163, v19\n"
    "v_mfma_f32_16x16x32_bf16 v[12:15], a[24:27], a[168:171], v[12:15]\n"
    "v_exp_f32 v156, v20\n"
    "v_mfma_f32_16x16x32_bf16 v[12:15], a[28:31], a[172:175], v[12:15]\n"
    "s_waitcnt lgkmcnt(4)\n"
    "v_exp_f32 v157, v21\n"
    "v_mfma_f32_16x16x32_bf16 v[220:223], a[0:3], v[88:91], v[220:223]\n"
    "ds_read_b64_tr_b16 a[20:21], v210 offset:512\n"
    "v_cvt_pk_bf16_f32 v16, v160, v161\n"
    "v_mfma_f32_16x16x32_bf16 v[220:223], a[4:7], v[92:95], v[220:223]\n"
    "v_exp_f32 v158, v22\n"
    "v_mfma_f32_16x16x32_bf16 v[176:179], a[4:7], v[116:119], v[176:179]\n"
    "ds_read_b64_tr_b16 a[22:23], v210 offset:4608\n"
    "v_add_f32 v172, v172, v162\n"
    "v_mfma_f32_16x16x32_bf16 v[176:179], a[0:3], v[112:115], v[176:179]\n"
    "v_exp_f32 v159, v23\n"
    "v_mfma_f32_16x16x32_bf16 a[32:35], a[0:3], v[96:99], a[32:35]\n"
    "ds_read_b64_tr_b16 a[16:17], v209 offset:512\n"
    "v_add_f32 v172, v172, v163\n"
    "v_mfma_f32_16x16x32_bf16 a[32:35], a[4:7], v[120:123], a[32:35]\n"
    "v_exp_f32 v160, v24\n"
    "v_mfma_f32_16x16x32_bf16 a[64:67], a[4:7], v[124:127], a[64:67]\n"
    "ds_read_b64_tr_b16 a[18:19], v209 offset:4608\n"
    "v_cvt_pk_bf16_f32 v17, v162, v163\n"
    "v_mfma_f32_16x16x32_bf16 a[64:67], a[0:3], v[100:103], a[64:67]\n"
    "v_exp_f32 v161, v25\n"
    "v_mfma_f32_16x16x32_bf16 a[96:99], a[0:3], v[104:107], a[96:99]\n"
    "v_exp_f32 v162, v26\n"
    "v_mfma_f32_16x16x32_bf16 a[96:99], a[4:7], v[128:131], a[96:99]\n"
    "s_waitcnt lgkmcnt(4)\n"
    "v_exp_f32 v163, v27\n"
    "v_mfma_f32_16x16x32_bf16 a[100:103], a[12:15], v[128:131], a[100:103]\n"
    "ds_read_b64_tr_b16 a[24:25], v209 offset:520\n"
    "v_add_f32 v172, v172, v156\n"
    "v_mfma_f32_16x16x32_bf16 a[100:103], a[8:11], v[104:107], a[100:103]\n"
    "v_add_f32 v172, v172, v157\n"
    "v_cvt_pk_bf16_f32 v18, v156, v157\n"
    "v_mfma_f32_16x16x32_bf16 a[68:71], a[8:11], v[100:103], a[68:71]\n"
    "ds_read_b64_tr_b16 a[26:27], v209 offset:4616\n"
    "v_add_f32 v172, v172, v158\n"
    "v_mfma_f32_16x16x32_bf16 a[68:71], a[12:15], v[124:127], a[68:71]\n"
    "v_exp_f32 v156, v28\n"
    "v_mfma_f32_16x16x32_bf16 a[36:39], a[12:15], v[120:123], a[36:39]\n"
    "ds_read_b64_tr_b16 a[28:29], v210 offset:520\n"
    "v_add_f32 v172, v172, v159\n"
    "v_mfma_f32_16x16x32_bf16 a[36:39], a[8:11], v[96:99], a[36:39]\n"
    "v_exp_f32 v157, v29\n"
    "v_mfma_f32_16x16x32_bf16 v[180:183], a[8:11], v[112:115], v[180:183]\n"
    "ds_read_b64_tr_b16 a[30:31], v210 offset:4616\n"
    "v_cvt_pk_bf16_f32 v19, v158, v159\n"
    "v_mfma_f32_16x16x32_bf16 v[180:183], a[12:15], v[116:119], v[180:183]\n"
    "v_exp_f32 v158, v30\n"
    "v_mfma_f32_16x16x32_bf16 v[224:227], a[12:15], v[92:95], v[224:227]\n"
    "v_exp_f32 v159, v31\n"
    "v_mfma_f32_16x16x32_bf16 v[224:227], a[8:11], v[88:91], v[224:227]\n"
    "s_waitcnt lgkmcnt(4)\n"
    "v_add_f32 v173, v173, v160\n"
    "v_add_f32 v173, v173, v161\n"
    "v_mfma_f32_16x16x32_bf16 v[228:231], a[16:19], v[88:91], v[228:231]\n"
    "ds_read_b64_tr_b16 a[4:5], v210 offset:1024\n"
    "v_cvt_pk_bf16_f32 v24, v160, v161\n"
    "v_mfma_f32_16x16x32_bf16 v[228:231], a[20:23], v[92:95], v[228:231]\n"
    "v_exp_f32 v160, v32\n"
    "v_mfma_f32_16x16x32_bf16 v[184:187], a[20:23], v[116:119], v[184:187]\n"
    "ds_read_b64_tr_b16 a[6:7], v210 offset:5120\n"
    "v_add_f32 v173, v173, v162\n"
    "v_mfma_f32_16x16x32_bf16 v[184:187], a[16:19], v[112:115], v[184:187]\n"
    "v_exp_f32 v161, v33\n"
    "v_mfma_f32_16x16x32_bf16 a[40:43], a[16:19], v[96:99], a[40:43]\n"
    "ds_read_b64_tr_b16 a[0:1], v209 offset:1024\n"
    "v_add_f32 v173, v173, v163\n"
    "v_mfma_f32_16x16x32_bf16 a[40:43], a[20:23], v[120:123], a[40:43]\n"
    "v_cvt_pk_bf16_f32 v25, v162, v163\n"
    "v_add_f32 v173, v173, v156\n"
    "v_mfma_f32_16x16x32_bf16 a[72:75], a[20:23], v[124:127], a[72:75]\n"
    "ds_read_b64_tr_b16 a[2:3], v209 offset:5120\n"
    "v_add_f32 v173, v173, v157\n"
    "v_mfma_f32_16x16x32_bf16 a[72:75], a[16:19], v[100:103], a[72:75]\n"
    "v_exp_f32 v162, v34\n"
    "v_mfma_f32_16x16x32_bf16 a[104:107], a[16:19], v[104:107], a[104:107]\n"
    "v_exp_f32 v163, v35\n"
    "v_mfma_f32_16x16x32_bf16 a[104:107], a[20:23], v[128:131], a[104:107]\n"
    "v_cvt_pk_bf16_f32 v26, v156, v157\n"
    "v_add_f32 v173, v173, v158\n"
    "v_mfma_f32_16x16x32_bf16 v[216:219], v[252:255], v[108:111], v[216:219]\n"
    "s_waitcnt lgkmcnt(4)\n"
    "v_exp_f32 v156, v36\n"
    "v_mfma_f32_16x16x32_bf16 a[108:111], a[28:31], v[128:131], a[108:111]\n"
    "ds_read_b64_tr_b16 a[8:9], v209 offset:1032\n"
    "v_add_f32 v173, v173, v159\n"
    "v_mfma_f32_16x16x32_bf16 a[108:111], a[24:27], v[104:107], a[108:111]\n"
    "v_exp_f32 v157, v37\n"
    "v_mfma_f32_16x16x32_bf16 a[76:79], a[24:27], v[100:103], a[76:79]\n"
    "ds_read_b64_tr_b16 a[10:11], v209 offset:5128\n"
    "v_cvt_pk_bf16_f32 v27, v158, v159\n"
    "v_mfma_f32_16x16x32_bf16 a[76:79], a[28:31], v[124:127], a[76:79]\n"
    "v_exp_f32 v158, v38\n"
    "v_mfma_f32_16x16x32_bf16 a[44:47], a[28:31], v[120:123], a[44:47]\n"
    "ds_read_b64_tr_b16 a[12:13], v210 offset:1032\n"
    "v_add_f32 v174, v174, v160\n"
    "v_mfma_f32_16x16x32_bf16 a[44:47], a[24:27], v[96:99], a[44:47]\n"
    "v_exp_f32 v159, v39\n"
    "v_mfma_f32_16x16x32_bf16 v[188:191], a[24:27], v[112:115], v[188:191]\n"
    "ds_read_b64_tr_b16 a[14:15], v210 offset:5128\n"
    "v_add_f32 v174, v174, v161\n"
    "v_mfma_f32_16x16x32_bf16 v[188:191], a[28:31], v[116:119], v[188:191]\n"
    "v_cvt_pk_bf16_f32 v32, v160, v161\n"
    "v_add_f32 v174, v174, v162\n"
    "v_mfma_f32_16x16x32_bf16 v[232:235], a[28:31], v[92:95], v[232:235]\n"
    "v_exp_f32 v160, v8\n"
    "v_mfma_f32_16x16x32_bf16 v[232:235], a[24:27], v[88:91], v[232:235]\n"
    "s_waitcnt lgkmcnt(4)\n"
    "v_exp_f32 v161, v9\n"
    "v_mfma_f32_16x16x32_bf16 v[236:239], a[0:3], v[88:91], v[236:239]\n"
    "ds_read_b64_tr_b16 a[20:21], v210 offset:1536\n"
    "v_add_f32 v174, v174, v163\n"
    "v_mfma_f32_16x16x32_bf16 v[236:239], a[4:7], v[92:95], v[236:239]\n"
    "v_cvt_pk_bf16_f32 v33, v162, v163\n"
    "v_add_f32 v174, v174, v156\n"
    "v_mfma_f32_16x16x32_bf16 v[192:195], a[4:7], v[116:119], v[192:195]\n"
    "ds_read_b64_tr_b16 a[22:23], v210 offset:5632\n"
    "v_add_f32 v174, v174, v157\n"
    "v_mfma_f32_16x16x32_bf16 v[192:195], a[0:3], v[112:115], v[192:195]\n"
    "v_exp_f32 v162, v10\n"
    "v_mfma_f32_16x16x32_bf16 a[48:51], a[0:3], v[96:99], a[48:51]\n"
    "ds_read_b64_tr_b16 a[16:17], v209 offset:1536\n"
    "v_cvt_pk_bf16_f32 v34, v156, v157\n"
    "v_mfma_f32_16x16x32_bf16 a[48:51], a[4:7], v[120:123], a[48:51]\n"
    "v_exp_f32 v163, v11\n"
    "v_mfma_f32_16x16x32_bf16 a[80:83], a[4:7], v[124:127], a[80:83]\n"
    "ds_read_b64_tr_b16 a[18:19], v209 offset:5632\n"
    "v_add_f32 v174, v174, v158\n"
    "v_mfma_f32_16x16x32_bf16 a[80:83], a[0:3], v[100:103], a[80:83]\n"
    "v_exp_f32 v156, v12\n"
    "v_mfma_f32_16x16x32_bf16 a[112:115], a[0:3], v[104:107], a[112:115]\n"
    "v_exp_f32 v157, v13\n"
    "v_mfma_f32_16x16x32_bf16 a[112:115], a[4:7], v[128:131], a[112:115]\n"
    "s_waitcnt lgkmcnt(4)\n"
    "v_add_f32 v174, v174, v159\n"
    "v_cvt_pk_bf16_f32 v35, v158, v159\n"
    "v_mfma_f32_16x16x32_bf16 a[116:119], a[12:15], v[128:131], a[116:119]\n"
    "ds_read_b64_tr_b16 a[24:25], v209 offset:1544\n"
    "v_cvt_pk_bf16_f32 v8, v160, v161\n"
    "v_mfma_f32_16x16x32_bf16 a[116:119], a[8:11], v[104:107], a[116:119]\n"
    "v_exp_f32 v158, v14\n"
    "v_mfma_f32_16x16x32_bf16 a[84:87], a[8:11], v[100:103], a[84:87]\n"
    "ds_read_b64_tr_b16 a[26:27], v209 offset:5640\n"
    "v_cvt_pk_bf16_f32 v9, v162, v163\n"
    "v_mfma_f32_16x16x32_bf16 a[84:87], a[12:15], v[124:127], a[84:87]\n"
    "v_exp_f32 v159, v15\n"
    "v_mfma_f32_16x16x32_bf16 a[52:55], a[12:15], v[120:123], a[52:55]\n"
    "ds_read_b64_tr_b16 a[28:29], v210 offset:1544\n"
    "v_cvt_pk_bf16_f32 v10, v156, v157\n"
    "v_mfma_f32_16x16x32_bf16 a[52:55], a[8:11], v[96:99], a[52:55]\n"
    "v_exp_f32 v160, v56\n"
    "v_mfma_f32_16x16x32_bf16 v[196:199], a[8:11], v[112:115], v[196:199]\n"
    "ds_read_b64_tr_b16 a[30:31], v210 offset:5640\n"
    "v_xor_b32 v209, 0xc000, v209\n"
    "v_mfma_f32_16x16x32_bf16 v[196:199], a[12:15], v[116:119], v[196:199]\n"
    "v_exp_f32 v161, v57\n"
    "v_mfma_f32_16x16x32_bf16 v[240:243], a[12:15], v[92:95], v[240:243]\n"
    "v_exp_f32 v162, v58\n"
    "v_mfma_f32_16x16x32_bf16 v[240:243], a[8:11], v[88:91], v[240:243]\n"
    "s_waitcnt lgkmcnt(4)\n"
    "v_exp_f32 v163, v59\n"
    "v_mfma_f32_16x16x32_bf16 v[244:247], a[16:19], v[88:91], v[244:247]\n"
    "ds_read_b128 a[4:7], v208 offset:512\n"
    "v_xor_b32 v210, 0xc000, v210\n"
    "v_mfma_f32_16x16x32_bf16 v[244:247], a[20:23], v[92:95], v[244:247]\n"
    "v_exp_f32 v156, v60\n"
    "v_mfma_f32_16x16x32_bf16 v[200:203], a[20:23], v[116:119], v[200:203]\n"
    "ds_read_b128 a[0:3], v208 offset:0\n"
    "v_cvt_pk_bf16_f32 v11, v158, v159\n"
    "v_mfma_f32_16x16x32_bf16 v[200:203], a[16:19], v[112:115], v[200:203]\n"
    "v_exp_f32 v157, v61\n"
    "v_mfma_f32_16x16x32_bf16 a[56:59], a[16:19], v[96:99], a[56:59]\n"
    "v_exp_f32 v158, v62\n"
    "v_mfma_f32_16x16x32_bf16 a[56:59], a[20:23], v[120:123], a[56:59]\n"
    "v_exp_f32 v159, v63\n"
    "v_mfma_f32_16x16x32_bf16 a[88:91], a[20:23], v[124:127], a[88:91]\n"
    "v_cvt_pk_bf16_f32 v56, v160, v161\n"
    "v_cvt_pk_bf16_f32 v57, v162, v163\n"
    "v_mfma_f32_16x16x32_bf16 a[88:91], a[16:19], v[100:103], a[88:91]\n"
    "v_exp_f32 v160, v64\n"
    "v_mfma_f32_16x16x32_bf16 a[120:123], a[16:19], v[104:107], a[120:123]\n"
    "v_exp_f32 v161, v65\n"
    "v_mfma_f32_16x16x32_bf16 a[120:123], a[20:23], v[128:131], a[120:123]\n"
    "s_waitcnt lgkmcnt(2)\n"
    "v_exp_f32 v162, v66\n"
    "v_mfma_f32_16x16x32_bf16 a[124:127], a[28:31], v[128:131], a[124:127]\n"
    "ds_read_b128 a[12:15], v208 offset:1536\n"
    "v_cvt_pk_bf16_f32 v58, v156, v157\n"
    "v_mfma_f32_16x16x32_bf16 a[124:127], a[24:27], v[104:107], a[124:127]\n"
    "v_exp_f32 v163, v67\n"
    "v_mfma_f32_16x16x32_bf16 a[92:95], a[24:27], v[100:103], a[92:95]\n"
    "ds_read_b128 a[8:11], v208 offset:1024\n"
    "v_cvt_pk_bf16_f32 v59, v158, v159\n"
    "v_mfma_f32_16x16x32_bf16 a[92:95], a[28:31], v[124:127], a[92:95]\n"
    "v_exp_f32 v156, v68\n"
    "v_mfma_f32_16x16x32_bf16 a[60:63], a[28:31], v[120:123], a[60:63]\n"
    "v_exp_f32 v157, v69\n"
    "v_mfma_f32_16x16x32_bf16 a[60:63], a[24:27], v[96:99], a[60:63]\n"
    "v_exp_f32 v158, v70\n"
    "v_mfma_f32_16x16x32_bf16 v[204:207], a[24:27], v[112:115], v[204:207]\n"
    "v_exp_f32 v159, v71\n"
    "v_mfma_f32_16x16x32_bf16 v[204:207], a[28:31], v[116:119], v[204:207]\n"
    "v_add_f32 v172, v172, v160\n"
    "v_add_f32 v172, v172, v161\n"
    "v_mfma_f32_16x16x32_bf16 v[248:251], a[28:31], v[92:95], v[248:251]\n"
    "v_cvt_pk_bf16_f32 v64, v160, v161\n"
    "v_add_f32 v172, v172, v162\n"
    "v_mfma_f32_16x16x32_bf16 v[248:251], a[24:27], v[88:91], v[248:251]\n"
    "v_exp_f32 v160, v72\n"
    "v_smfmac_f32_16x16x64_bf16 v[164:167], v[252:255], v[4:11], v175\n"
    "s_waitcnt lgkmcnt(2)\n"
    "v_add_f32 v172, v172, v163\n"
    "v_mfma_f32_16x16x32_bf16 v[88:91], a[0:3], a[160:163], v[132:135]\n"
    "ds_read_b128 a[20:23], v208 offset:4608\n"
    "v_cvt_pk_bf16_f32 v65, v162, v163\n"
    "v_mfma_f32_16x16x32_bf16 v[88:91], a[4:7], a[164:167], v[88:91]\n"
    "v_exp_f32 v161, v73\n"
    "v_mfma_f32_16x16x32_bf16 v[112:115], a[4:7], a[180:183], v[136:139]\n"
    "ds_read_b128 a[16:19], v208 offset:4096\n"
    "v_max3_f32 v214, v216, v164, v165\n"
    "v_mfma_f32_16x16x32_bf16 v[112:115], a[0:3], a[176:179], v[112:115]\n"
    "v_exp_f32 v162, v74\n"
    "v_mfma_f32_16x16x32_bf16 v[96:99], a[0:3], a[192:195], v[140:143]\n"
    "v_exp_f32 v163, v75\n"
    "v_mfma_f32_16x16x32_bf16 v[96:99], a[4:7], a[196:199], v[96:99]\n"
    "v_add_f32 v172, v172, v156\n"
    "v_add_f32 v172, v172, v157\n"
    "v_mfma_f32_16x16x32_bf16 v[100:103], a[4:7], a[212:215], v[144:147]\n"
    "v_cvt_pk_bf16_f32 v66, v156, v157\n"
    "v_add_f32 v172, v172, v158\n"
    "v_mfma_f32_16x16x32_bf16 v[100:103], a[0:3], a[208:211], v[100:103]\n"
    "v_exp_f32 v156, v76\n"
    "v_mfma_f32_16x16x32_bf16 v[104:107], a[0:3], a[224:227], v[148:151]\n"
    "v_exp_f32 v157, v77\n"
    "v_mfma_f32_16x16x32_bf16 v[104:107], a[4:7], a[228:231], v[104:107]\n"
    "s_waitcnt lgkmcnt(2)\n"
    "v_add_f32 v172, v172, v159\n"
    "v_cvt_pk_bf16_f32 v67, v158, v159\n"
    "v_mfma_f32_16x16x32_bf16 v[104:107], a[8:11], a[232:235], v[104:107]\n"
    "ds_read_b128 a[28:31], v208 offset:5632\n"
    "v_add_f32 v173, v173, v160\n"
    "v_mfma_f32_16x16x32_bf16 v[104:107], a[12:15], a[236:239], v[104:107]\n"
    "v_exp_f32 v158, v78\n"
    "v_mfma_f32_16x16x32_bf16 v[100:103], a[12:15], a[220:223], v[100:103]\n"
    "ds_read_b128 a[24:27], v208 offset:5120\n"
    "v_xor_b32 v208, 0x2000, v208\n"
    "v_mfma_f32_16x16x32_bf16 v[100:103], a[8:11], a[216:219], v[100:103]\n"
    "v_exp_f32 v159, v79\n"
    "v_mfma_f32_16x16x32_bf16 v[96:99], a[8:11], a[200:203], v[96:99]\n"
    "v_add_f32 v173, v173, v161\n"
    "v_cvt_pk_bf16_f32 v72, v160, v161\n"
    "v_mfma_f32_16x16x32_bf16 v[96:99], a[12:15], a[204:207], v[96:99]\n"
    "v_exp_f32 v160, v80\n"
    "v_mfma_f32_16x16x32_bf16 v[112:115], a[12:15], a[188:191], v[112:115]\n"
    "v_exp_f32 v161, v81\n"
    "v_mfma_f32_16x16x32_bf16 v[112:115], a[8:11], a[184:187], v[112:115]\n"
    "v_add_f32 v173, v173, v162\n"
    "v_add_f32 v173, v173, v163\n"
    "v_mfma_f32_16x16x32_bf16 v[88:91], a[8:11], a[168:171], v[88:91]\n"
    "v_cvt_pk_bf16_f32 v73, v162, v163\n"
    "v_add_f32 v173, v173, v156\n"
    "v_mfma_f32_16x16x32_bf16 v[88:91], a[12:15], a[172:175], v[88:91]\n"
    "v_exp_f32 v162, v82\n"
    "v_smfmac_f32_16x16x64_bf16 v[168:171], v[252:255], v[52:59], v175\n"
    "s_waitcnt lgkmcnt(2)\n"
    "v_add_f32 v173, v173, v157\n"
    "v_mfma_f32_16x16x32_bf16 v[0:3], a[16:19], a[160:163], v[132:135]\n"
    "ds_read_b128 a[4:7], v208 offset:512\n"
    "v_cvt_pk_bf16_f32 v74, v156, v157\n"
    "v_mfma_f32_16x16x32_bf16 v[0:3], a[20:23], a[164:167], v[0:3]\n"
    "v_exp_f32 v163, v83\n"
    "v_mfma_f32_16x16x32_bf16 v[48:51], a[20:23], a[180:183], v[136:139]\n"
    "ds_read_b128 a[0:3], v208 offset:0\n"
    "v_max3_f32 v214, v214, v168, v169\n"
    "v_mfma_f32_16x16x32_bf16 v[48:51], a[16:19], a[176:179], v[48:51]\n"
    "v_exp_f32 v156, v84\n"
    "v_mfma_f32_16x16x32_bf16 v[20:23], a[16:19], a[192:195], v[140:143]\n"
    "v_exp_f32 v157, v85\n"
    "v_mfma_f32_16x16x32_bf16 v[20:23], a[20:23], a[196:199], v[20:23]\n"
    "v_max_f32 v214, v214, v172\n"
    "v_add_f32 v173, v173, v158\n"
    "v_mfma_f32_16x16x32_bf16 v[28:31], a[20:23], a[212:215], v[144:147]\n"
    "v_add_f32 v173, v173, v159\n"
    "v_cvt_pk_bf16_f32 v75, v158, v159\n"
    "v_mfma_f32_16x16x32_bf16 v[28:31], a[16:19], a[208:211], v[28:31]\n"
    "v_exp_f32 v158, v86\n"
    "v_mfma_f32_16x16x32_bf16 v[36:39], a[16:19], a[224:227], v[148:151]\n"
    "v_exp_f32 v159, v87\n"
    "v_mfma_f32_16x16x32_bf16 v[36:39], a[20:23], a[228:231], v[36:39]\n"
    "s_waitcnt lgkmcnt(2)\n"
    "v_add_f32 v174, v174, v160\n"
    "v_add_f32 v174, v174, v161\n"
    "v_mfma_f32_16x16x32_bf16 v[36:39], a[24:27], a[232:235], v[36:39]\n"
    "s_cmp_ge_u32 s60, 0x8000\n"
    "ds_read_b128 a[12:15], v208 offset:1536\n"
    "v_add_f32 v174, v174, v162\n"
    "v_mfma_f32_16x16x32_bf16 v[36:39], a[28:31], a[236:239], v[36:39]\n"
    "s_cbranch_scc1 .Lgqa_LOOP\n"
    "v_mfma_f32_16x16x32_bf16 v[28:31], a[28:31], a[220:223], v[28:31]\n"
    "ds_read_b128 a[8:11], v208 offset:1024\n"
    "v_add_f32 v174, v174, v163\n"
    "v_mfma_f32_16x16x32_bf16 v[28:31], a[24:27], a[216:219], v[28:31]\n"
    "v_add_f32 v174, v174, v156\n"
    "v_add_f32 v174, v174, v157\n"
    "v_mfma_f32_16x16x32_bf16 v[20:23], a[24:27], a[200:203], v[20:23]\n"
    "v_add_f32 v174, v174, v158\n"
    "v_add_f32 v174, v174, v159\n"
    "v_mfma_f32_16x16x32_bf16 v[20:23], a[28:31], a[204:207], v[20:23]\n"
    "v_max3_f32 v214, v214, v173, v174\n"
    "v_cmp_lt_f32 vcc, s64, v214\n"
    "v_mfma_f32_16x16x32_bf16 v[48:51], a[28:31], a[188:191], v[48:51]\n"
    "v_cvt_pk_bf16_f32 v80, v160, v161\n"
    "v_cvt_pk_bf16_f32 v81, v162, v163\n"
    "v_mfma_f32_16x16x32_bf16 v[48:51], a[24:27], a[184:187], v[48:51]\n"
    "v_cvt_pk_bf16_f32 v82, v156, v157\n"
    "v_cvt_pk_bf16_f32 v83, v158, v159\n"
    "v_mfma_f32_16x16x32_bf16 v[0:3], a[24:27], a[168:171], v[0:3]\n"
    "v_mfma_f32_16x16x32_bf16 v[0:3], a[28:31], a[172:175], v[0:3]\n"
    "s_cbranch_vccnz .Lgqa_STUB12\n"
    ".Lgqa_RET12:\n"
    "s_waitcnt lgkmcnt(2)\n"
    "v_mfma_f32_16x16x32_bf16 v[92:95], a[0:3], a[160:163], v[132:135]\n"
    "s_mov_b32 s58, s60\n"
    "ds_read_b128 a[28:31], v208 offset:5632\n"
    "s_mov_b32 s59, s61\n"
    "v_mfma_f32_16x16x32_bf16 v[92:95], a[4:7], a[164:167], v[92:95]\n"
    "s_xor_b32 s62, s62, 0x8000\n"
    "ds_read_b128 a[24:27], v208 offset:5120\n"
    "v_add_f32 v164, v164, v165\n"
    "s_waitcnt lgkmcnt(2)\n"
    "v_mfma_f32_16x16x32_bf16 v[92:95], a[8:11], a[168:171], v[92:95]\n"
    "s_add_u32 m0, s62, 0x10000\n"
    "ds_read_b128 a[20:23], v208 offset:4608\n"
    "v_add_f32 v168, v168, v169\n"
    "v_mfma_f32_16x16x32_bf16 v[92:95], a[12:15], a[172:175], v[92:95]\n"
    "ds_read_b128 a[16:19], v208 offset:4096\n"
    "v_xor_b32 v208, 0xe000, v208\n"
    "v_mfma_f32_16x16x32_bf16 v[116:119], a[0:3], a[176:179], v[136:139]\n"
    "s_waitcnt vmcnt(0)\n"
    "s_barrier\n"
    "ds_read_b64_tr_b16 a[240:241], v209 offset:0\n"
    "v_mfma_f32_16x16x32_bf16 v[116:119], a[4:7], a[180:183], v[116:119]\n"
    "buffer_load_dwordx4 v213, s[44:47], s58 offen offset:0 nt lds\n"
    "v_mfma_f32_16x16x32_bf16 v[116:119], a[8:11], a[184:187], v[116:119]\n"
    "ds_read_b64_tr_b16 a[242:243], v209 offset:4096\n"
    "v_mbcnt_lo_u32_b32 v219, -1, 0\n"
    "v_mfma_f32_16x16x32_bf16 v[116:119], a[12:15], a[188:191], v[116:119]\n"
    "ds_read_b64_tr_b16 a[244:245], v209 offset:8\n"
    "v_mbcnt_hi_u32_b32 v219, -1, v219\n"
    "v_mfma_f32_16x16x32_bf16 v[120:123], a[0:3], a[192:195], v[140:143]\n"
    "ds_read_b64_tr_b16 a[246:247], v209 offset:4104\n"
    "v_mov_b32 v153, v216\n"
    "v_mfma_f32_16x16x32_bf16 v[120:123], a[4:7], a[196:199], v[120:123]\n"
    "s_waitcnt lgkmcnt(7)\n"
    "ds_read_b64_tr_b16 a[248:249], v209 offset:512\n"
    "v_exp_f32 v156, v88\n"
    "v_mfma_f32_16x16x32_bf16 v[12:15], a[28:31], a[172:175], v[132:135]\n"
    "s_waitcnt lgkmcnt(7)\n"
    "ds_read_b64_tr_b16 a[250:251], v209 offset:4608\n"
    "v_exp_f32 v157, v89\n"
    "v_mfma_f32_16x16x32_bf16 v[12:15], a[24:27], a[168:171], v[12:15]\n"
    "s_waitcnt lgkmcnt(7)\n"
    "ds_read_b64_tr_b16 a[252:253], v209 offset:520\n"
    "v_cvt_pk_bf16_f32 v132, v156, v157\n"
    "v_mfma_f32_16x16x32_bf16 v[12:15], a[20:23], a[164:167], v[12:15]\n"
    "s_waitcnt lgkmcnt(7)\n"
    "ds_read_b64_tr_b16 a[254:255], v209 offset:4616\n"
    "v_exp_f32 v158, v90\n"
    "v_mfma_f32_16x16x32_bf16 v[12:15], a[16:19], a[160:163], v[12:15]\n"
    "ds_read_b64_tr_b16 v[40:41], v209 offset:1024\n"
    "v_exp_f32 v159, v91\n"
    "v_mfma_f32_16x16x32_bf16 v[60:63], a[28:31], a[188:191], v[136:139]\n"
    "ds_read_b64_tr_b16 v[42:43], v209 offset:5120\n"
    "v_cvt_pk_bf16_f32 v133, v158, v159\n"
    "v_mfma_f32_16x16x32_bf16 v[60:63], a[24:27], a[184:187], v[60:63]\n"
    "ds_read_b64_tr_b16 v[44:45], v209 offset:1032\n"
    "v_exp_f32 v160, v0\n"
    "v_mfma_f32_16x16x32_bf16 v[60:63], a[20:23], a[180:183], v[60:63]\n"
    "ds_read_b64_tr_b16 v[46:47], v209 offset:5128\n"
    "v_exp_f32 v161, v1\n"
    "v_mfma_f32_16x16x32_bf16 v[60:63], a[16:19], a[176:179], v[60:63]\n"
    "v_cvt_pk_bf16_f32 v134, v160, v161\n"
    "v_exp_f32 v162, v2\n"
    "v_mfma_f32_16x16x32_bf16 v[120:123], a[8:11], a[200:203], v[120:123]\n"
    "v_exp_f32 v163, v3\n"
    "v_mfma_f32_16x16x32_bf16 v[120:123], a[12:15], a[204:207], v[120:123]\n"
    "v_cvt_pk_bf16_f32 v135, v162, v163\n"
    "buffer_load_dwordx4 v213, s[44:47], s58 offen offset:2048 nt lds\n"
    "v_mfma_f32_16x16x32_bf16 v[68:71], a[28:31], a[204:207], v[140:143]\n"
    "s_add_u32 m0, s62, 0x10380\n"
    "ds_read_b64_tr_b16 a[160:161], v210 offset:0\n"
    "buffer_load_dwordx4 v213, s[44:47], s58 offen offset:128 nt lds\n"
    "v_mfma_f32_16x16x32_bf16 v[68:71], a[24:27], a[200:203], v[68:71]\n"
    "ds_read_b64_tr_b16 a[162:163], v210 offset:4096\n"
    "v_exp_f32 v156, v92\n"
    "v_mfma_f32_16x16x32_bf16 v[68:71], a[20:23], a[196:199], v[68:71]\n"
    "ds_read_b64_tr_b16 a[164:165], v210 offset:8\n"
    "v_exp_f32 v157, v93\n"
    "v_mfma_f32_16x16x32_bf16 v[68:71], a[16:19], a[192:195], v[68:71]\n"
    "ds_read_b64_tr_b16 a[166:167], v210 offset:4104\n"
    "v_cvt_pk_bf16_f32 v136, v156, v157\n"
    "v_mfma_f32_16x16x32_bf16 v[124:127], a[0:3], a[208:211], v[144:147]\n"
    "ds_read_b64_tr_b16 a[168:169], v210 offset:512\n"
    "v_exp_f32 v158, v94\n"
    "v_mfma_f32_16x16x32_bf16 v[124:127], a[4:7], a[212:215], v[124:127]\n"
    "ds_read_b64_tr_b16 a[170:171], v210 offset:4608\n"
    "v_exp_f32 v159, v95\n"
    "v_mfma_f32_16x16x32_bf16 v[124:127], a[8:11], a[216:219], v[124:127]\n"
    "ds_read_b64_tr_b16 a[172:173], v210 offset:520\n"
    "v_cvt_pk_bf16_f32 v137, v158, v159\n"
    "v_mfma_f32_16x16x32_bf16 v[124:127], a[12:15], a[220:223], v[124:127]\n"
    "ds_read_b64_tr_b16 a[174:175], v210 offset:4616\n"
    "v_exp_f32 v160, v12\n"
    "v_mfma_f32_16x16x32_bf16 v[76:79], a[28:31], a[220:223], v[144:147]\n"
    "ds_read_b64_tr_b16 a[176:177], v210 offset:1024\n"
    "v_exp_f32 v161, v13\n"
    "v_mfma_f32_16x16x32_bf16 v[76:79], a[24:27], a[216:219], v[76:79]\n"
    "ds_read_b64_tr_b16 a[178:179], v210 offset:5120\n"
    "v_cvt_pk_bf16_f32 v138, v160, v161\n"
    "v_mfma_f32_16x16x32_bf16 v[76:79], a[20:23], a[212:215], v[76:79]\n"
    "ds_read_b64_tr_b16 a[180:181], v210 offset:1032\n"
    "v_exp_f32 v162, v14\n"
    "v_mfma_f32_16x16x32_bf16 v[76:79], a[16:19], a[208:211], v[76:79]\n"
    "ds_read_b64_tr_b16 a[182:183], v210 offset:5128\n"
    "v_exp_f32 v163, v15\n"
    "v_mfma_f32_16x16x32_bf16 v[128:131], a[0:3], a[224:227], v[148:151]\n"
    "ds_read_b64_tr_b16 a[184:185], v210 offset:1536\n"
    "v_cvt_pk_bf16_f32 v139, v162, v163\n"
    "v_mfma_f32_16x16x32_bf16 v[128:131], a[4:7], a[228:231], v[128:131]\n"
    "ds_read_b64_tr_b16 a[186:187], v210 offset:5632\n"
    "buffer_load_dwordx4 v213, s[44:47], s58 offen offset:2176 nt lds\n"
    "v_mfma_f32_16x16x32_bf16 v[128:131], a[8:11], a[232:235], v[128:131]\n"
    "s_add_u32 m0, s62, 0x11000\n"
    "ds_read_b64_tr_b16 a[188:189], v210 offset:1544\n"
    "buffer_load_dwordx4 v213, s[44:47], s59 offen offset:0 nt lds\n"
    "v_mfma_f32_16x16x32_bf16 v[128:131], a[12:15], a[236:239], v[128:131]\n"
    "ds_read_b64_tr_b16 a[190:191], v210 offset:5640\n"
    "v_xor_b32 v210, 0x4000, v210\n"
    "v_mfma_f32_16x16x32_bf16 v[84:87], a[28:31], a[236:239], v[148:151]\n"
    "ds_read_b64_tr_b16 v[140:141], v209 offset:1536\n"
    "buffer_load_dwordx4 v213, s[44:47], s59 offen offset:2048 nt lds\n"
    "v_mfma_f32_16x16x32_bf16 v[84:87], a[24:27], a[232:235], v[84:87]\n"
    "s_add_u32 m0, s62, 0x11380\n"
    "ds_read_b64_tr_b16 v[142:143], v209 offset:5632\n"
    "buffer_load_dwordx4 v213, s[44:47], s59 offen offset:128 nt lds\n"
    "v_mfma_f32_16x16x32_bf16 v[84:87], a[20:23], a[228:231], v[84:87]\n"
    "s_add_u32 s72, s63, 0x20000\n"
    "ds_read_b64_tr_b16 v[144:145], v209 offset:1544\n"
    "v_lshl_add_u32 v211, v219, 4, s72\n"
    "v_mfma_f32_16x16x32_bf16 v[84:87], a[16:19], a[224:227], v[84:87]\n"
    "s_lshr_b32 s73, s63, 2\n"
    "ds_read_b64_tr_b16 v[146:147], v209 offset:5640\n"
    "v_xor_b32 v209, 0x4000, v209\n"
    "v_mfma_f32_16x16x32_bf16 v[164:167], v[252:255], v[132:135], v[164:167]\n"
    "s_add_u32 s73, s73, s72\n"
    "ds_read_b64_tr_b16 a[192:193], v209 offset:0\n"
    "v_lshl_add_u32 v212, v219, 4, s73\n"
    "v_mfma_f32_16x16x32_bf16 v[164:167], v[252:255], v[136:139], v[164:167]\n"
    "s_add_u32 s77, s76, 4096\n"
    "ds_read_b64_tr_b16 a[194:195], v209 offset:4096\n"
    "v_xor_b32 v217, 0x2000, v212\n"
    "s_waitcnt lgkmcnt(15)\n"
    "v_mfma_f32_16x16x32_bf16 a[128:131], a[240:243], v[108:111], a[128:131]\n"
    "s_add_u32 s78, s76, 8192\n"
    "ds_read_b64_tr_b16 a[196:197], v209 offset:8\n"
    "buffer_load_dwordx4 v213, s[44:47], s59 offen offset:2176 nt lds\n"
    "v_mfma_f32_16x16x32_bf16 a[132:135], a[244:247], v[108:111], a[132:135]\n"
    "s_add_u32 s79, s76, 12288\n"
    "ds_read_b64_tr_b16 a[198:199], v209 offset:4104\n"
    "v_cmp_lt_f32 vcc, s64, v164\n"
    "v_mfma_f32_16x16x32_bf16 a[136:139], a[248:251], v[108:111], a[136:139]\n"
    "s_add_u32 s80, s76, 16384\n"
    "ds_read_b64_tr_b16 a[200:201], v209 offset:512\n"
    "s_lshl_b32 s81, s56, 6\n"
    "v_mfma_f32_16x16x32_bf16 a[140:143], a[252:255], v[108:111], a[140:143]\n"
    "s_cbranch_vccnz .Lgqa_STUB7\n"
    ".Lgqa_RET7:\n"
    "ds_read_b64_tr_b16 a[202:203], v209 offset:4608\n"
    "v_mfma_f32_16x16x32_bf16 v[220:223], a[240:243], v[4:7], v[220:223]\n"
    "v_rcp_f32 v164, v164\n"
    "v_mfma_f32_16x16x32_bf16 v[220:223], a[160:163], v[8:11], v[220:223]\n"
    "ds_read_b64_tr_b16 a[204:205], v209 offset:520\n"
    "v_lshl_add_u32 v213, v219, 3, s73\n"
    "v_mfma_f32_16x16x32_bf16 v[224:227], a[164:167], v[8:11], v[224:227]\n"
    "ds_read_b64_tr_b16 a[206:207], v209 offset:4616\n"
    "v_xor_b32 v218, 0x2800, v213\n"
    "v_mfma_f32_16x16x32_bf16 v[224:227], a[244:247], v[4:7], v[224:227]\n"
    "ds_read_b64_tr_b16 a[208:209], v209 offset:1024\n"
    "v_exp_f32 v156, v112\n"
    "v_mfma_f32_16x16x32_bf16 v[228:231], a[248:251], v[4:7], v[228:231]\n"
    "ds_read_b64_tr_b16 a[210:211], v209 offset:5120\n"
    "v_exp_f32 v157, v113\n"
    "v_mfma_f32_16x16x32_bf16 v[228:231], a[168:171], v[8:11], v[228:231]\n"
    "s_waitcnt lgkmcnt(15)\n"
    "ds_read_b64_tr_b16 a[212:213], v209 offset:1032\n"
    "v_cvt_pk_bf16_f32 v88, v156, v157\n"
    "v_mfma_f32_16x16x32_bf16 v[232:235], a[172:175], v[8:11], v[232:235]\n"
    "ds_read_b64_tr_b16 a[214:215], v209 offset:5128\n"
    "v_exp_f32 v158, v114\n"
    "v_mfma_f32_16x16x32_bf16 v[232:235], a[252:255], v[4:7], v[232:235]\n"
    "ds_read_b64_tr_b16 a[216:217], v209 offset:1536\n"
    "v_exp_f32 v159, v115\n"
    "v_mfma_f32_16x16x32_bf16 v[236:239], v[40:43], v[4:7], v[236:239]\n"
    "ds_read_b64_tr_b16 a[218:219], v209 offset:5632\n"
    "v_cvt_pk_bf16_f32 v89, v158, v159\n"
    "v_mfma_f32_16x16x32_bf16 v[236:239], a[176:179], v[8:11], v[236:239]\n"
    "ds_read_b64_tr_b16 a[220:221], v209 offset:1544\n"
    "v_exp_f32 v160, v48\n"
    "v_mfma_f32_16x16x32_bf16 v[240:243], a[180:183], v[8:11], v[240:243]\n"
    "ds_read_b64_tr_b16 a[222:223], v209 offset:5640\n"
    "v_xor_b32 v209, 0xc000, v209\n"
    "v_mfma_f32_16x16x32_bf16 v[240:243], v[44:47], v[4:7], v[240:243]\n"
    "s_waitcnt lgkmcnt(15)\n"
    "ds_read_b64_tr_b16 a[0:1], v210 offset:0\n"
    "v_exp_f32 v161, v49\n"
    "v_mfma_f32_16x16x32_bf16 v[244:247], v[140:143], v[4:7], v[244:247]\n"
    ".Lgqa_T4:\n"
    "s_cbranch_execz .Lgqa_TBACK\n"
    "ds_read_b64_tr_b16 a[2:3], v210 offset:4096\n"
    "v_cvt_pk_bf16_f32 v90, v160, v161\n"
    "v_mfma_f32_16x16x32_bf16 v[244:247], a[184:187], v[8:11], v[244:247]\n"
    "ds_read_b64_tr_b16 a[4:5], v210 offset:8\n"
    "v_exp_f32 v162, v50\n"
    "v_mfma_f32_16x16x32_bf16 v[248:251], a[188:191], v[8:11], v[248:251]\n"
    "ds_read_b64_tr_b16 a[6:7], v210 offset:4104\n"
    "v_exp_f32 v163, v51\n"
    "v_mfma_f32_16x16x32_bf16 v[248:251], v[144:147], v[4:7], v[248:251]\n"
    "ds_read_b64_tr_b16 a[8:9], v210 offset:512\n"
    "v_cvt_pk_bf16_f32 v91, v162, v163\n"
    "v_mfma_f32_16x16x32_bf16 a[144:147], v[40:43], v[108:111], a[144:147]\n"
    "ds_read_b64_tr_b16 a[10:11], v210 offset:4608\n"
    "v_exp_f32 v156, v116\n"
    "v_mfma_f32_16x16x32_bf16 a[148:151], v[44:47], v[108:111], a[148:151]\n"
    "ds_read_b64_tr_b16 a[12:13], v210 offset:520\n"
    "v_exp_f32 v157, v117\n"
    "v_mfma_f32_16x16x32_bf16 a[152:155], v[140:143], v[108:111], a[152:155]\n"
    "ds_read_b64_tr_b16 a[14:15], v210 offset:4616\n"
    "v_cvt_pk_bf16_f32 v92, v156, v157\n"
    "v_mfma_f32_16x16x32_bf16 a[156:159], v[144:147], v[108:111], a[156:159]\n"
    "s_add_u32 s81, s81, 81920\n"
    "ds_read_b64_tr_b16 a[16:17], v210 offset:1024\n"
    "v_exp_f32 v158, v118\n"
    "v_mfma_f32_16x16x32_bf16 v[168:171], v[252:255], v[88:91], v[168:171]\n"
    "s_waitcnt lgkmcnt(15)\n"
    "ds_read_b64_tr_b16 a[18:19], v210 offset:5120\n"
    "v_exp_f32 v159, v119\n"
    "v_mfma_f32_16x16x32_bf16 v[220:223], a[192:195], v[132:135], v[220:223]\n"
    "s_waitcnt lgkmcnt(8)\n"
    "ds_read_b64_tr_b16 a[20:21], v210 offset:1032\n"
    "v_cvt_pk_bf16_f32 v93, v158, v159\n"
    "v_mfma_f32_16x16x32_bf16 v[220:223], a[0:3], v[136:139], v[220:223]\n"
    "s_waitcnt lgkmcnt(7)\n"
    "ds_read_b64_tr_b16 a[22:23], v210 offset:5128\n"
    "v_exp_f32 v160, v60\n"
    "v_mfma_f32_16x16x32_bf16 v[224:227], a[4:7], v[136:139], v[224:227]\n"
    "ds_read_b64_tr_b16 a[24:25], v210 offset:1536\n"
    "v_exp_f32 v161, v61\n"
    "v_mfma_f32_16x16x32_bf16 v[224:227], a[196:199], v[132:135], v[224:227]\n"
    "ds_read_b64_tr_b16 a[26:27], v210 offset:5632\n"
    "v_cvt_pk_bf16_f32 v94, v160, v161\n"
    "v_mfma_f32_16x16x32_bf16 v[228:231], a[200:203], v[132:135], v[228:231]\n"
    "s_waitcnt lgkmcnt(8)\n"
    "ds_read_b64_tr_b16 a[28:29], v210 offset:1544\n"
    "v_exp_f32 v162, v62\n"
    "v_mfma_f32_16x16x32_bf16 v[228:231], a[8:11], v[136:139], v[228:231]\n"
    "s_waitcnt lgkmcnt(7)\n"
    "ds_read_b64_tr_b16 a[30:31], v210 offset:5640\n"
    "v_xor_b32 v210, 0xc000, v210\n"
    "v_mfma_f32_16x16x32_bf16 v[232:235], a[12:15], v[136:139], v[232:235]\n"
    "v_exp_f32_e64 v163, v63\n"
    "v_mfma_f32_16x16x32_bf16 v[232:235], a[204:207], v[132:135], v[232:235]\n"
    "v_cvt_pk_bf16_f32 v95, v162, v163\n"
    "ds_write_b128 v211, a[128:131] offset:0\n"
    "v_pk_mul_f32 v[220:221], v[164:165], v[220:221] op_sel:[0,0] op_sel_hi:[0,1]\n"
    "v_pk_mul_f32 v[222:223], v[164:165], v[222:223] op_sel:[0,0] op_sel_hi:[0,1]\n"
    "v_pk_mul_f32 v[224:225], v[164:165], v[224:225] op_sel:[0,0] op_sel_hi:[0,1]\n"
    "v_pk_mul_f32 v[226:227], v[164:165], v[226:227] op_sel:[0,0] op_sel_hi:[0,1]\n"
    "v_mfma_f32_16x16x32_bf16 v[168:171], v[252:255], v[92:95], v[168:171]\n"
    "v_cvt_pk_bf16_f32 v4, v220, v221\n"
    "v_cvt_pk_bf16_f32 v5, v222, v223\n"
    "v_mfma_f32_16x16x32_bf16 v[236:239], a[208:211], v[132:135], v[236:239]\n"
    "s_waitcnt lgkmcnt(7)\n"
    "v_cvt_pk_bf16_f32 v6, v224, v225\n"
    "v_cvt_pk_bf16_f32 v7, v226, v227\n"
    "buffer_store_dwordx4 v[4:7], v215, s[52:55], s76 offen offset:0 sc0 sc1\n"
    "v_mfma_f32_16x16x32_bf16 v[236:239], a[16:19], v[136:139], v[236:239]\n"
    "s_waitcnt lgkmcnt(5)\n"
    "ds_write_b128 v211, a[132:135] offset:1024\n"
    "v_mfma_f32_16x16x32_bf16 v[240:243], a[20:23], v[136:139], v[240:243]\n"
    "v_cmp_lt_f32 vcc, s64, v168\n"
    "ds_write_b128 v211, a[136:139] offset:2048\n"
    "s_cbranch_vccnz .Lgqa_STUB8\n"
    ".Lgqa_RET8:\n"
    "v_mfma_f32_16x16x32_bf16 v[240:243], a[212:215], v[132:135], v[240:243]\n"
    "v_rcp_f32_e64 v168, v168\n"
    "ds_write_b128 v211, a[140:143] offset:3072\n"
    "v_pk_mul_f32 v[228:229], v[164:165], v[228:229] op_sel:[0,0] op_sel_hi:[0,1]\n"
    "v_pk_mul_f32 v[230:231], v[164:165], v[230:231] op_sel:[0,0] op_sel_hi:[0,1]\n"
    "v_pk_mul_f32 v[232:233], v[164:165], v[232:233] op_sel:[0,0] op_sel_hi:[0,1]\n"
    "v_pk_mul_f32 v[234:235], v[164:165], v[234:235] op_sel:[0,0] op_sel_hi:[0,1]\n"
    "v_mfma_f32_16x16x32_bf16 v[244:247], a[216:219], v[132:135], v[244:247]\n"
    "s_waitcnt lgkmcnt(6)\n"
    "v_cvt_pk_bf16_f32 v0, v228, v229\n"
    "v_cvt_pk_bf16_f32 v1, v230, v231\n"
    "v_mfma_f32_16x16x32_bf16 v[244:247], a[24:27], v[136:139], v[244:247]\n"
    "v_cvt_pk_bf16_f32 v2, v232, v233\n"
    "v_cvt_pk_bf16_f32 v3, v234, v235\n"
    "buffer_store_dwordx4 v[0:3], v215, s[52:55], s76 offen offset:64 sc0 sc1\n"
    "ds_write_b128 v211, a[144:147] offset:4096\n"
    "v_pk_mul_f32 v[236:237], v[164:165], v[236:237] op_sel:[0,0] op_sel_hi:[0,1]\n"
    "v_pk_mul_f32 v[238:239], v[164:165], v[238:239] op_sel:[0,0] op_sel_hi:[0,1]\n"
    "v_pk_mul_f32 v[240:241], v[164:165], v[240:241] op_sel:[0,0] op_sel_hi:[0,1]\n"
    "v_pk_mul_f32 v[242:243], v[164:165], v[242:243] op_sel:[0,0] op_sel_hi:[0,1]\n"
    "s_waitcnt lgkmcnt(5)\n"
    "v_mfma_f32_16x16x32_bf16 v[248:251], a[28:31], v[136:139], v[248:251]\n"
    "ds_write_b128 v211, a[148:151] offset:5120\n"
    "v_mfma_f32_16x16x32_bf16 v[248:251], a[220:223], v[132:135], v[248:251]\n"
    "v_cvt_pk_bf16_f32 v8, v236, v237\n"
    "v_cvt_pk_bf16_f32 v9, v238, v239\n"
    "v_mfma_f32_16x16x32_bf16 v[176:179], a[240:243], v[52:55], v[176:179]\n"
    "ds_write_b128 v211, a[152:155] offset:6144\n"
    "v_mfma_f32_16x16x32_bf16 v[176:179], a[160:163], v[56:59], v[176:179]\n"
    "v_cvt_pk_bf16_f32 v10, v240, v241\n"
    "v_cvt_pk_bf16_f32 v11, v242, v243\n"
    "v_exp_f32_e64 v156, v96\n"
    "ds_write_b128 v211, a[156:159] offset:7168\n"
    "buffer_store_dwordx4 v[8:11], v215, s[52:55], s76 offen offset:128 sc0 sc1\n"
    "v_pk_mul_f32 v[244:245], v[164:165], v[244:245] op_sel:[0,0] op_sel_hi:[0,1]\n"
    "ds_read_b128 v[132:135], v212 offset:0\n"
    "v_pk_mul_f32 v[246:247], v[164:165], v[246:247] op_sel:[0,0] op_sel_hi:[0,1]\n"
    "v_pk_mul_f32 v[248:249], v[164:165], v[248:249] op_sel:[0,0] op_sel_hi:[0,1]\n"
    "v_pk_mul_f32 v[250:251], v[164:165], v[250:251] op_sel:[0,0] op_sel_hi:[0,1]\n"
    "v_mfma_f32_16x16x32_bf16 v[176:179], a[192:195], v[88:91], v[176:179]\n"
    "ds_read_b128 v[136:139], v212 offset:1024\n"
    "v_cvt_pk_bf16_f32 v12, v244, v245\n"
    "v_mfma_f32_16x16x32_bf16 v[176:179], a[0:3], v[92:95], v[176:179]\n"
    "ds_write_b64 v213, v[152:153] offset:0\n"
    "v_cvt_pk_bf16_f32 v13, v246, v247\n"
    "v_mfma_f32_16x16x32_bf16 v[180:183], a[4:7], v[92:95], v[180:183]\n"
    "v_cvt_pk_bf16_f32 v14, v248, v249\n"
    "v_cvt_pk_bf16_f32 v15, v250, v251\n"
    "v_mfma_f32_16x16x32_bf16 v[180:183], a[196:199], v[88:91], v[180:183]\n"
    "v_exp_f32 v157, v97\n"
    "v_exp_f32 v158, v98\n"
    "v_cvt_pk_bf16_f32 v112, v156, v157\n"
    "v_pk_mul_f32 v[176:177], v[168:169], v[176:177] op_sel:[0,0] op_sel_hi:[0,1]\n"
    "v_pk_mul_f32 v[178:179], v[168:169], v[178:179] op_sel:[0,0] op_sel_hi:[0,1]\n"
    "v_mfma_f32_16x16x32_bf16 v[180:183], a[164:167], v[56:59], v[180:183]\n"
    "v_cvt_pk_bf16_f32 v236, v176, v177\n"
    "v_cvt_pk_bf16_f32 v237, v178, v179\n"
    "v_mfma_f32_16x16x32_bf16 v[180:183], a[244:247], v[52:55], v[180:183]\n"
    "v_exp_f32 v159, v99\n"
    "v_mfma_f32_16x16x32_bf16 v[184:187], a[248:251], v[52:55], v[184:187]\n"
    "v_cvt_pk_bf16_f32 v113, v158, v159\n"
    "v_exp_f32 v160, v20\n"
    "v_mfma_f32_16x16x32_bf16 v[184:187], a[168:171], v[56:59], v[184:187]\n"
    "v_exp_f32 v161, v21\n"
    "v_exp_f32 v162, v22\n"
    "v_cvt_pk_bf16_f32 v114, v160, v161\n"
    "s_waitcnt lgkmcnt(0)\n"
    "s_waitcnt vmcnt(3)\n"
    "s_barrier\n"
    "buffer_store_dwordx4 v[12:15], v215, s[52:55], s76 offen offset:192 sc0 sc1\n"
    "v_pk_mul_f32 v[180:181], v[168:169], v[180:181] op_sel:[0,0] op_sel_hi:[0,1]\n"
    "v_pk_mul_f32 v[182:183], v[168:169], v[182:183] op_sel:[0,0] op_sel_hi:[0,1]\n"
    "v_mfma_f32_16x16x32_bf16 v[184:187], a[200:203], v[88:91], v[184:187]\n"
    "ds_read_b128 a[128:131], v208 offset:0\n"
    "v_cvt_pk_bf16_f32 v238, v180, v181\n"
    "v_mfma_f32_16x16x32_bf16 v[184:187], a[8:11], v[92:95], v[184:187]\n"
    "ds_read_b128 a[132:135], v208 offset:512\n"
    "v_cvt_pk_bf16_f32 v239, v182, v183\n"
    "v_mfma_f32_16x16x32_bf16 v[188:191], a[12:15], v[92:95], v[188:191]\n"
    "ds_read_b128 a[136:139], v208 offset:1024\n"
    "v_exp_f32 v163, v23\n"
    "v_mfma_f32_16x16x32_bf16 v[188:191], a[204:207], v[88:91], v[188:191]\n"
    "ds_read_b128 a[140:143], v208 offset:1536\n"
    "v_cvt_pk_bf16_f32 v115, v162, v163\n"
    "v_exp_f32 v156, v120\n"
    "v_exp_f32 v157, v121\n"
    "v_pk_mul_f32 v[184:185], v[168:169], v[184:185] op_sel:[0,0] op_sel_hi:[0,1]\n"
    "v_pk_mul_f32 v[186:187], v[168:169], v[186:187] op_sel:[0,0] op_sel_hi:[0,1]\n"
    "v_mfma_f32_16x16x32_bf16 v[188:191], a[172:175], v[56:59], v[188:191]\n"
    "ds_read_b128 a[144:147], v208 offset:4096\n"
    "v_cvt_pk_bf16_f32 v240, v184, v185\n"
    "v_mfma_f32_16x16x32_bf16 v[188:191], a[252:255], v[52:55], v[188:191]\n"
    "ds_read_b128 a[148:151], v208 offset:4608\n"
    "v_cvt_pk_bf16_f32 v241, v186, v187\n"
    "v_mfma_f32_16x16x32_bf16 v[192:195], v[40:43], v[52:55], v[192:195]\n"
    "ds_read_b128 a[152:155], v208 offset:5120\n"
    "v_cvt_pk_bf16_f32 v116, v156, v157\n"
    "v_mfma_f32_16x16x32_bf16 v[192:195], a[176:179], v[56:59], v[192:195]\n"
    "ds_read_b128 a[156:159], v208 offset:5632\n"
    "v_xor_b32 v208, 0x2000, v208\n"
    "buffer_store_dwordx4 v[236:239], v215, s[52:55], s77 offen offset:0 sc0 sc1\n"
    "v_pk_mul_f32 v[188:189], v[168:169], v[188:189] op_sel:[0,0] op_sel_hi:[0,1]\n"
    "v_pk_mul_f32 v[190:191], v[168:169], v[190:191] op_sel:[0,0] op_sel_hi:[0,1]\n"
    "v_mfma_f32_16x16x32_bf16 v[192:195], a[208:211], v[88:91], v[192:195]\n"
    "v_cvt_pk_bf16_f32 v242, v188, v189\n"
    "v_cvt_pk_bf16_f32 v243, v190, v191\n"
    "v_mfma_f32_16x16x32_bf16 v[192:195], a[16:19], v[92:95], v[192:195]\n"
    "v_exp_f32 v158, v122\n"
    "v_mfma_f32_16x16x32_bf16 v[196:199], a[20:23], v[92:95], v[196:199]\n"
    "v_exp_f32 v159, v123\n"
    "v_mfma_f32_16x16x32_bf16 v[196:199], a[212:215], v[88:91], v[196:199]\n"
    "v_cvt_pk_bf16_f32 v117, v158, v159\n"
    "v_exp_f32 v160, v68\n"
    "v_mfma_f32_16x16x32_bf16 v[196:199], a[180:183], v[56:59], v[196:199]\n"
    "v_exp_f32 v161, v69\n"
    "v_mfma_f32_16x16x32_bf16 v[196:199], v[44:47], v[52:55], v[196:199]\n"
    "v_cvt_pk_bf16_f32 v118, v160, v161\n"
    "v_exp_f32 v162, v70\n"
    "v_mfma_f32_16x16x32_bf16 v[200:203], v[140:143], v[52:55], v[200:203]\n"
    "v_exp_f32 v163, v71\n"
    "v_mfma_f32_16x16x32_bf16 v[200:203], a[184:187], v[56:59], v[200:203]\n"
    "v_cvt_pk_bf16_f32 v119, v162, v163\n"
    "v_pk_mul_f32 v[192:193], v[168:169], v[192:193] op_sel:[0,0] op_sel_hi:[0,1]\n"
    "buffer_store_dwordx4 v[240:243], v215, s[52:55], s77 offen offset:64 sc0 sc1\n"
    "v_pk_mul_f32 v[194:195], v[168:169], v[194:195] op_sel:[0,0] op_sel_hi:[0,1]\n"
    "v_pk_mul_f32 v[196:197], v[168:169], v[196:197] op_sel:[0,0] op_sel_hi:[0,1]\n"
    "v_pk_mul_f32 v[198:199], v[168:169], v[198:199] op_sel:[0,0] op_sel_hi:[0,1]\n"
    "v_mfma_f32_16x16x32_bf16 v[200:203], a[216:219], v[88:91], v[200:203]\n"
    "v_cvt_pk_bf16_f32 v4, v192, v193\n"
    "v_cvt_pk_bf16_f32 v5, v194, v195\n"
    "v_mfma_f32_16x16x32_bf16 v[200:203], a[24:27], v[92:95], v[200:203]\n"
    "v_cvt_pk_bf16_f32 v6, v196, v197\n"
    "v_cvt_pk_bf16_f32 v7, v198, v199\n"
    "v_mfma_f32_16x16x32_bf16 v[204:207], a[28:31], v[92:95], v[204:207]\n"
    "v_mfma_f32_16x16x32_bf16 v[204:207], a[220:223], v[88:91], v[204:207]\n"
    "ds_read_b64 v[88:89], v218 offset:0\n"
    "v_xor_b32 v218, 0x5000, v213\n"
    "v_pk_mul_f32 v[200:201], v[168:169], v[200:201] op_sel:[0,0] op_sel_hi:[0,1]\n"
    "v_pk_mul_f32 v[202:203], v[168:169], v[202:203] op_sel:[0,0] op_sel_hi:[0,1]\n"
    "v_mfma_f32_16x16x32_bf16 v[204:207], a[188:191], v[56:59], v[204:207]\n"
    "v_cvt_pk_bf16_f32 v0, v200, v201\n"
    "v_cvt_pk_bf16_f32 v1, v202, v203\n"
    "v_mfma_f32_16x16x32_bf16 v[204:207], v[144:147], v[52:55], v[204:207]\n"
    "ds_read_b128 v[52:55], v217 offset:0\n"
    "v_mfma_f32_16x16x32_bf16 v[108:111], v[252:255], v[112:115], 0\n"
    "ds_read_b128 v[48:51], v217 offset:1024\n"
    "v_xor_b32 v217, 0x4000, v212\n"
    "v_mfma_f32_16x16x32_bf16 v[108:111], v[252:255], v[116:119], v[108:111]\n"
    "ds_read_b64 v[90:91], v218 offset:0\n"
    "v_xor_b32 v218, 0x7800, v213\n"
    "buffer_store_dwordx4 v[4:7], v215, s[52:55], s77 offen offset:128 sc0 sc1\n"
    "v_pk_mul_f32 v[204:205], v[168:169], v[204:205] op_sel:[0,0] op_sel_hi:[0,1]\n"
    "v_pk_mul_f32 v[206:207], v[168:169], v[206:207] op_sel:[0,0] op_sel_hi:[0,1]\n"
    "v_cvt_pk_bf16_f32 v2, v204, v205\n"
    "v_cvt_pk_bf16_f32 v3, v206, v207\n"
    "ds_read_b128 v[56:59], v217 offset:0\n"
    "s_nop 0\n"
    "v_fmac_f32 v172, 0x3e800000, v108\n"
    "v_cmp_lt_f32 vcc, s64, v172\n"
    "ds_read_b128 v[60:63], v217 offset:1024\n"
    "v_xor_b32 v217, 0x6000, v212\n"
    "ds_read_b64 v[92:93], v218 offset:0\n"
    "s_cbranch_vccnz .Lgqa_STUB9\n"
    ".Lgqa_RET9:\n"
    "v_mfma_f32_16x16x32_bf16 a[32:35], a[240:243], v[16:19], a[32:35]\n"
    "ds_read_b128 v[244:247], v217 offset:0\n"
    "v_mov_b32 v175, v172\n"
    "v_mfma_f32_16x16x32_bf16 a[32:35], a[160:163], v[64:67], a[32:35]\n"
    "ds_read_b128 v[248:251], v217 offset:1024\n"
    "v_permlane16_swap_b32 v172, v175\n"
    "v_mfma_f32_16x16x32_bf16 a[32:35], a[192:195], v[112:115], a[32:35]\n"
    "ds_read_b128 v[176:179], v208 offset:0\n"
    "v_add_f32 v172, v172, v175\n"
    "v_mfma_f32_16x16x32_bf16 a[32:35], a[0:3], v[116:119], a[32:35]\n"
    "ds_read_b128 v[180:183], v208 offset:512\n"
    "v_mov_b32 v175, v172\n"
    "v_mfma_f32_16x16x32_bf16 a[36:39], a[4:7], v[116:119], a[36:39]\n"
    "s_waitcnt lgkmcnt(7)\n"
    "ds_read_b128 v[184:187], v208 offset:1024\n"
    "v_min3_f32 v219, v152, v88, v90\n"
    "v_mfma_f32_16x16x32_bf16 a[36:39], a[196:199], v[112:115], a[36:39]\n"
    "ds_read_b128 v[188:191], v208 offset:1536\n"
    "v_accvgpr_read_b32 v220, a32\n"
    "v_mfma_f32_16x16x32_bf16 a[36:39], a[164:167], v[64:67], a[36:39]\n"
    "ds_read_b128 v[192:195], v208 offset:4096\n"
    "v_accvgpr_read_b32 v221, a33\n"
    "v_mfma_f32_16x16x32_bf16 a[36:39], a[244:247], v[16:19], a[36:39]\n"
    "s_waitcnt lgkmcnt(7)\n"
    "ds_read_b128 v[196:199], v208 offset:4608\n"
    "v_min_f32 v219, v219, v92\n"
    "v_mfma_f32_16x16x32_bf16 a[40:43], a[248:251], v[16:19], a[40:43]\n"
    "ds_read_b128 v[200:203], v208 offset:5120\n"
    "v_sub_f32 v94, v219, v152\n"
    "v_mfma_f32_16x16x32_bf16 a[40:43], a[168:171], v[64:67], a[40:43]\n"
    "ds_read_b128 v[204:207], v208 offset:5632\n"
    "v_xor_b32 v208, 0x6000, v208\n"
    "v_mfma_f32_16x16x32_bf16 a[40:43], a[200:203], v[112:115], a[40:43]\n"
    "v_permlane32_swap_b32 v172, v175\n"
    "v_mfma_f32_16x16x32_bf16 a[40:43], a[8:11], v[116:119], a[40:43]\n"
    "v_add_f32 v172, v172, v175\n"
    "v_sub_f32 v95, v219, v88\n"
    "v_mfma_f32_16x16x32_bf16 a[44:47], a[12:15], v[116:119], a[44:47]\n"
    "v_rcp_f32 v172, v172\n"
    "v_mfma_f32_16x16x32_bf16 a[44:47], a[204:207], v[112:115], a[44:47]\n"
    "buffer_store_dwordx4 v[0:3], v215, s[52:55], s77 offen offset:192 sc0 sc1\n"
    "v_mfma_f32_16x16x32_bf16 a[44:47], a[172:175], v[64:67], a[44:47]\n"
    "v_sub_f32 v154, v219, v90\n"
    "v_sub_f32 v155, v219, v92\n"
    "v_mfma_f32_16x16x32_bf16 a[44:47], a[252:255], v[16:19], a[44:47]\n"
    "v_exp_f32 v94, v94\n"
    "v_mfma_f32_16x16x32_bf16 a[48:51], v[40:43], v[16:19], a[48:51]\n"
    "v_exp_f32 v95, v95\n"
    "v_mfma_f32_16x16x32_bf16 a[48:51], a[176:179], v[64:67], a[48:51]\n"
    "v_exp_f32 v154, v154\n"
    "v_mfma_f32_16x16x32_bf16 a[48:51], a[208:211], v[112:115], a[48:51]\n"
    "v_exp_f32 v155, v155\n"
    "v_mfma_f32_16x16x32_bf16 a[48:51], a[16:19], v[116:119], a[48:51]\n"
    "v_mul_f32 v219, v153, v94\n"
    "v_fmac_f32 v219, v89, v95\n"
    "v_mfma_f32_16x16x32_bf16 a[52:55], a[20:23], v[116:119], a[52:55]\n"
    "v_fmac_f32 v219, v91, v154\n"
    "v_fmac_f32 v219, v93, v155\n"
    "v_mfma_f32_16x16x32_bf16 a[52:55], a[212:215], v[112:115], a[52:55]\n"
    "v_rcp_f32 v219, v219\n"
    "v_accvgpr_read_b32 v222, a34\n"
    "v_accvgpr_read_b32 v223, a35\n"
    "v_accvgpr_read_b32 v224, a36\n"
    "v_pk_mul_f32 v[132:133], v[94:95], v[132:133] op_sel:[0,0] op_sel_hi:[0,1]\n"
    "v_pk_mul_f32 v[134:135], v[94:95], v[134:135] op_sel:[0,0] op_sel_hi:[0,1]\n"
    "v_pk_mul_f32 v[136:137], v[94:95], v[136:137] op_sel:[0,0] op_sel_hi:[0,1]\n"
    "v_pk_mul_f32 v[138:139], v[94:95], v[138:139] op_sel:[0,0] op_sel_hi:[0,1]\n"
    "v_pk_fma_f32 v[132:133], v[94:95], v[52:53], v[132:133] op_sel:[1,0,0] op_sel_hi:[1,1,1]\n"
    "v_pk_fma_f32 v[134:135], v[94:95], v[54:55], v[134:135] op_sel:[1,0,0] op_sel_hi:[1,1,1]\n"
    "v_pk_fma_f32 v[136:137], v[94:95], v[48:49], v[136:137] op_sel:[1,0,0] op_sel_hi:[1,1,1]\n"
    "v_pk_fma_f32 v[138:139], v[94:95], v[50:51], v[138:139] op_sel:[1,0,0] op_sel_hi:[1,1,1]\n"
    "v_pk_fma_f32 v[132:133], v[154:155], v[56:57], v[132:133] op_sel:[0,0,0] op_sel_hi:[0,1,1]\n"
    "v_pk_fma_f32 v[134:135], v[154:155], v[58:59], v[134:135] op_sel:[0,0,0] op_sel_hi:[0,1,1]\n"
    "v_pk_fma_f32 v[136:137], v[154:155], v[60:61], v[136:137] op_sel:[0,0,0] op_sel_hi:[0,1,1]\n"
    "v_pk_fma_f32 v[138:139], v[154:155], v[62:63], v[138:139] op_sel:[0,0,0] op_sel_hi:[0,1,1]\n"
    "s_waitcnt lgkmcnt(9)\n"
    "v_pk_fma_f32 v[132:133], v[154:155], v[244:245], v[132:133] op_sel:[1,0,0] op_sel_hi:[1,1,1]\n"
    "v_pk_fma_f32 v[134:135], v[154:155], v[246:247], v[134:135] op_sel:[1,0,0] op_sel_hi:[1,1,1]\n"
    "s_waitcnt lgkmcnt(8)\n"
    "v_pk_fma_f32 v[136:137], v[154:155], v[248:249], v[136:137] op_sel:[1,0,0] op_sel_hi:[1,1,1]\n"
    "v_pk_fma_f32 v[138:139], v[154:155], v[250:251], v[138:139] op_sel:[1,0,0] op_sel_hi:[1,1,1]\n"
    "v_pk_mul_f32 v[132:133], v[218:219], v[132:133] op_sel:[1,0] op_sel_hi:[1,1]\n"
    "v_pk_mul_f32 v[134:135], v[218:219], v[134:135] op_sel:[1,0] op_sel_hi:[1,1]\n"
    "v_pk_mul_f32 v[136:137], v[218:219], v[136:137] op_sel:[1,0] op_sel_hi:[1,1]\n"
    "v_pk_mul_f32 v[138:139], v[218:219], v[138:139] op_sel:[1,0] op_sel_hi:[1,1]\n"
    "v_cvt_pk_bf16_f32 v8, v132, v133\n"
    "v_mfma_f32_16x16x32_bf16 a[52:55], a[180:183], v[64:67], a[52:55]\n"
    "v_cvt_pk_bf16_f32 v9, v134, v135\n"
    "v_cvt_pk_bf16_f32 v10, v136, v137\n"
    "v_mfma_f32_16x16x32_bf16 a[52:55], v[44:47], v[16:19], a[52:55]\n"
    "v_cvt_pk_bf16_f32 v11, v138, v139\n"
    "buffer_store_dwordx4 v[8:11], v215, s[52:55], s81 offen offset:0 sc0 sc1\n"
    "v_mfma_f32_16x16x32_bf16 a[56:59], v[140:143], v[16:19], a[56:59]\n"
    "v_accvgpr_read_b32 v225, a37\n"
    "v_accvgpr_read_b32 v226, a38\n"
    "v_mfma_f32_16x16x32_bf16 a[56:59], a[184:187], v[64:67], a[56:59]\n"
    "v_accvgpr_read_b32 v227, a39\n"
    "v_accvgpr_read_b32 v228, a40\n"
    "v_accvgpr_read_b32 v229, a41\n"
    "v_accvgpr_read_b32 v230, a42\n"
    "v_accvgpr_read_b32 v231, a43\n"
    "v_pk_mul_f32 v[220:221], v[172:173], v[220:221] op_sel:[0,0] op_sel_hi:[0,1]\n"
    "v_pk_mul_f32 v[222:223], v[172:173], v[222:223] op_sel:[0,0] op_sel_hi:[0,1]\n"
    "v_pk_mul_f32 v[224:225], v[172:173], v[224:225] op_sel:[0,0] op_sel_hi:[0,1]\n"
    "v_pk_mul_f32 v[226:227], v[172:173], v[226:227] op_sel:[0,0] op_sel_hi:[0,1]\n"
    "v_mfma_f32_16x16x32_bf16 a[56:59], a[216:219], v[112:115], a[56:59]\n"
    "v_cvt_pk_bf16_f32 v12, v220, v221\n"
    "v_cvt_pk_bf16_f32 v13, v222, v223\n"
    "v_mfma_f32_16x16x32_bf16 a[56:59], a[24:27], v[116:119], a[56:59]\n"
    "v_cvt_pk_bf16_f32 v14, v224, v225\n"
    "v_cvt_pk_bf16_f32 v15, v226, v227\n"
    "v_mfma_f32_16x16x32_bf16 a[60:63], a[28:31], v[116:119], a[60:63]\n"
    "v_accvgpr_read_b32 v232, a44\n"
    "v_accvgpr_read_b32 v233, a45\n"
    "v_mfma_f32_16x16x32_bf16 a[60:63], a[220:223], v[112:115], a[60:63]\n"
    "v_accvgpr_read_b32 v234, a46\n"
    "v_accvgpr_read_b32 v235, a47\n"
    "v_exp_f32 v156, v100\n"
    "v_exp_f32 v157, v101\n"
    "v_pk_mul_f32 v[228:229], v[172:173], v[228:229] op_sel:[0,0] op_sel_hi:[0,1]\n"
    "v_pk_mul_f32 v[230:231], v[172:173], v[230:231] op_sel:[0,0] op_sel_hi:[0,1]\n"
    "v_pk_mul_f32 v[232:233], v[172:173], v[232:233] op_sel:[0,0] op_sel_hi:[0,1]\n"
    "v_pk_mul_f32 v[234:235], v[172:173], v[234:235] op_sel:[0,0] op_sel_hi:[0,1]\n"
    "v_mfma_f32_16x16x32_bf16 a[60:63], a[188:191], v[64:67], a[60:63]\n"
    "buffer_store_dwordx4 v[12:15], v215, s[52:55], s78 offen offset:0 sc0 sc1\n"
    "v_mfma_f32_16x16x32_bf16 a[60:63], v[144:147], v[16:19], a[60:63]\n"
    "v_cvt_pk_bf16_f32 v236, v228, v229\n"
    "v_cvt_pk_bf16_f32 v237, v230, v231\n"
    "v_mfma_f32_16x16x32_bf16 v[52:55], a[128:131], a[224:227], v[148:151]\n"
    "ds_read_b128 a[128:131], v208 offset:0\n"
    "v_cvt_pk_bf16_f32 v238, v232, v233\n"
    "v_mfma_f32_16x16x32_bf16 v[52:55], a[132:135], a[228:231], v[52:55]\n"
    "ds_read_b128 a[132:135], v208 offset:512\n"
    "v_cvt_pk_bf16_f32 v239, v234, v235\n"
    "v_mfma_f32_16x16x32_bf16 v[52:55], a[136:139], a[232:235], v[52:55]\n"
    "ds_read_b128 a[136:139], v208 offset:1024\n"
    "v_cvt_pk_bf16_f32 v96, v156, v157\n"
    "v_mfma_f32_16x16x32_bf16 v[52:55], a[140:143], a[236:239], v[52:55]\n"
    "ds_read_b128 a[140:143], v208 offset:1536\n"
    "v_accvgpr_read_b32 v220, a48\n"
    "v_mfma_f32_16x16x32_bf16 v[48:51], a[156:159], a[236:239], v[148:151]\n"
    "v_exp_f32 v158, v102\n"
    "v_mfma_f32_16x16x32_bf16 v[48:51], a[152:155], a[232:235], v[48:51]\n"
    "v_exp_f32 v159, v103\n"
    "v_mfma_f32_16x16x32_bf16 v[48:51], a[148:151], a[228:231], v[48:51]\n"
    "v_cvt_pk_bf16_f32 v97, v158, v159\n"
    "v_accvgpr_read_b32 v221, a49\n"
    "v_mfma_f32_16x16x32_bf16 v[48:51], a[144:147], a[224:227], v[48:51]\n"
    "s_waitcnt lgkmcnt(11)\n"
    "ds_read_b128 a[144:147], v208 offset:4096\n"
    "v_accvgpr_read_b32 v222, a50\n"
    "v_mfma_f32_16x16x32_bf16 v[56:59], v[176:179], a[224:227], v[148:151]\n"
    "s_waitcnt lgkmcnt(11)\n"
    "ds_read_b128 a[148:151], v208 offset:4608\n"
    "v_accvgpr_read_b32 v223, a51\n"
    "v_mfma_f32_16x16x32_bf16 v[56:59], v[180:183], a[228:231], v[56:59]\n"
    "s_waitcnt lgkmcnt(11)\n"
    "ds_read_b128 a[152:155], v208 offset:5120\n"
    "buffer_store_dwordx4 v[236:239], v215, s[52:55], s78 offen offset:64 sc0 sc1\n"
    "v_mfma_f32_16x16x32_bf16 v[56:59], v[184:187], a[232:235], v[56:59]\n"
    "s_waitcnt lgkmcnt(11)\n"
    "ds_read_b128 a[156:159], v208 offset:5632\n"
    "v_accvgpr_read_b32 v224, a52\n"
    "v_mfma_f32_16x16x32_bf16 v[56:59], v[188:191], a[236:239], v[56:59]\n"
    "s_waitcnt lgkmcnt(8)\n"
    "ds_read_b64_tr_b16 v[176:177], v209 offset:0\n"
    "v_accvgpr_read_b32 v225, a53\n"
    "v_mfma_f32_16x16x32_bf16 v[60:63], v[204:207], a[236:239], v[148:151]\n"
    "ds_read_b64_tr_b16 v[178:179], v209 offset:4096\n"
    "v_accvgpr_read_b32 v226, a54\n"
    "v_mfma_f32_16x16x32_bf16 v[60:63], v[200:203], a[232:235], v[60:63]\n"
    "ds_read_b64_tr_b16 v[180:181], v209 offset:8\n"
    "v_accvgpr_read_b32 v227, a55\n"
    "v_mfma_f32_16x16x32_bf16 v[60:63], v[196:199], a[228:231], v[60:63]\n"
    "ds_read_b64_tr_b16 v[182:183], v209 offset:4104\n"
    "v_accvgpr_read_b32 v228, a56\n"
    "v_mfma_f32_16x16x32_bf16 v[60:63], v[192:195], a[224:227], v[60:63]\n"
    "s_waitcnt lgkmcnt(11)\n"
    "ds_read_b64_tr_b16 v[184:185], v209 offset:512\n"
    "v_accvgpr_read_b32 v229, a57\n"
    "v_mfma_f32_16x16x32_bf16 v[16:19], a[128:131], a[224:227], v[148:151]\n"
    "s_waitcnt lgkmcnt(11)\n"
    "ds_read_b64_tr_b16 v[186:187], v209 offset:4608\n"
    "v_accvgpr_read_b32 v230, a58\n"
    "v_mfma_f32_16x16x32_bf16 v[16:19], a[132:135], a[228:231], v[16:19]\n"
    "s_waitcnt lgkmcnt(11)\n"
    "ds_read_b64_tr_b16 v[188:189], v209 offset:520\n"
    "v_accvgpr_read_b32 v231, a59\n"
    "v_mfma_f32_16x16x32_bf16 v[16:19], a[136:139], a[232:235], v[16:19]\n"
    "s_waitcnt lgkmcnt(11)\n"
    "ds_read_b64_tr_b16 v[190:191], v209 offset:4616\n"
    "v_accvgpr_read_b32 v232, a60\n"
    "v_mfma_f32_16x16x32_bf16 v[16:19], a[140:143], a[236:239], v[16:19]\n"
    "s_waitcnt lgkmcnt(8)\n"
    "ds_read_b64_tr_b16 v[192:193], v209 offset:1024\n"
    "v_accvgpr_read_b32 v233, a61\n"
    "v_mfma_f32_16x16x32_bf16 v[20:23], a[156:159], a[236:239], v[148:151]\n"
    "ds_read_b64_tr_b16 v[194:195], v209 offset:5120\n"
    "v_accvgpr_read_b32 v234, a62\n"
    "v_mfma_f32_16x16x32_bf16 v[20:23], a[152:155], a[232:235], v[20:23]\n"
    "ds_read_b64_tr_b16 v[196:197], v209 offset:1032\n"
    "v_accvgpr_read_b32 v235, a63\n"
    "v_mfma_f32_16x16x32_bf16 v[20:23], a[148:151], a[228:231], v[20:23]\n"
    "ds_read_b64_tr_b16 v[198:199], v209 offset:5128\n"
    "v_exp_f32 v160, v28\n"
    "v_mfma_f32_16x16x32_bf16 v[20:23], a[144:147], a[224:227], v[20:23]\n"
    "ds_read_b64_tr_b16 v[200:201], v209 offset:1536\n"
    "v_exp_f32 v161, v29\n"
    "v_exp_f32 v162, v30\n"
    "v_cvt_pk_bf16_f32 v98, v160, v161\n"
    "v_exp_f32 v163, v31\n"
    "v_exp_f32 v156, v124\n"
    "v_cvt_pk_bf16_f32 v99, v162, v163\n"
    "v_exp_f32 v157, v125\n"
    "v_pk_mul_f32 v[220:221], v[172:173], v[220:221] op_sel:[0,0] op_sel_hi:[0,1]\n"
    "v_mfma_f32_16x16x32_bf16 v[108:111], v[252:255], v[96:99], 0\n"
    "ds_read_b64_tr_b16 v[202:203], v209 offset:5632\n"
    "v_cvt_pk_bf16_f32 v120, v156, v157\n"
    "v_exp_f32 v158, v126\n"
    "v_exp_f32 v159, v127\n"
    "v_exp_f32 v160, v76\n"
    "v_cvt_pk_bf16_f32 v121, v158, v159\n"
    "v_exp_f32 v161, v77\n"
    "v_exp_f32 v162, v78\n"
    "v_cvt_pk_bf16_f32 v122, v160, v161\n"
    "v_exp_f32 v163, v79\n"
    "v_pk_mul_f32 v[222:223], v[172:173], v[222:223] op_sel:[0,0] op_sel_hi:[0,1]\n"
    "v_cvt_pk_bf16_f32 v123, v162, v163\n"
    "v_pk_mul_f32 v[224:225], v[172:173], v[224:225] op_sel:[0,0] op_sel_hi:[0,1]\n"
    "v_pk_mul_f32 v[226:227], v[172:173], v[226:227] op_sel:[0,0] op_sel_hi:[0,1]\n"
    "v_mfma_f32_16x16x32_bf16 v[108:111], v[252:255], v[120:123], v[108:111]\n"
    "ds_read_b64_tr_b16 v[204:205], v209 offset:1544\n"
    "v_cvt_pk_bf16_f32 v240, v220, v221\n"
    "v_cvt_pk_bf16_f32 v241, v222, v223\n"
    "v_cvt_pk_bf16_f32 v242, v224, v225\n"
    "v_cvt_pk_bf16_f32 v243, v226, v227\n"
    "buffer_store_dwordx4 v[240:243], v215, s[52:55], s78 offen offset:128 sc0 sc1\n"
    "v_pk_mul_f32 v[228:229], v[172:173], v[228:229] op_sel:[0,0] op_sel_hi:[0,1]\n"
    "v_pk_mul_f32 v[230:231], v[172:173], v[230:231] op_sel:[0,0] op_sel_hi:[0,1]\n"
    "v_pk_mul_f32 v[232:233], v[172:173], v[232:233] op_sel:[0,0] op_sel_hi:[0,1]\n"
    "v_fmac_f32 v173, 0x3e800000, v108\n"
    "v_cmp_lt_f32_e64 vcc, s64, v173\n"
    "v_pk_mul_f32 v[234:235], v[172:173], v[234:235] op_sel:[0,0] op_sel_hi:[0,1]\n"
    "v_cvt_pk_bf16_f32 v4, v228, v229\n"
    "v_cvt_pk_bf16_f32 v5, v230, v231\n"
    "v_cvt_pk_bf16_f32 v6, v232, v233\n"
    "v_cvt_pk_bf16_f32 v7, v234, v235\n"
    "ds_read_b64_tr_b16 v[206:207], v209 offset:5640\n"
    "s_cbranch_vccnz .Lgqa_STUB10\n"
    ".Lgqa_RET10:\n"
    "v_mfma_f32_16x16x32_bf16 a[64:67], a[240:243], v[24:27], a[64:67]\n"
    "ds_read_b64_tr_b16 a[32:33], v210 offset:0\n"
    "v_mov_b32 v175, v173\n"
    "v_mfma_f32_16x16x32_bf16 a[64:67], a[160:163], v[72:75], a[64:67]\n"
    "ds_read_b64_tr_b16 a[34:35], v210 offset:4096\n"
    "v_xor_b32 v209, 0x4000, v209\n"
    "v_mfma_f32_16x16x32_bf16 a[64:67], a[192:195], v[96:99], a[64:67]\n"
    "ds_read_b64_tr_b16 a[36:37], v210 offset:8\n"
    "v_permlane16_swap_b32 v173, v175\n"
    "v_mfma_f32_16x16x32_bf16 a[64:67], a[0:3], v[120:123], a[64:67]\n"
    "ds_read_b64_tr_b16 a[38:39], v210 offset:4104\n"
    "v_add_f32 v173, v173, v175\n"
    "v_mfma_f32_16x16x32_bf16 a[68:71], a[4:7], v[120:123], a[68:71]\n"
    "ds_read_b64_tr_b16 a[40:41], v210 offset:512\n"
    "v_mov_b32 v175, v173\n"
    "v_mfma_f32_16x16x32_bf16 a[68:71], a[196:199], v[96:99], a[68:71]\n"
    "ds_read_b64_tr_b16 a[42:43], v210 offset:4608\n"
    "v_accvgpr_read_b32 v220, a64\n"
    "v_mfma_f32_16x16x32_bf16 a[68:71], a[164:167], v[72:75], a[68:71]\n"
    "ds_read_b64_tr_b16 a[44:45], v210 offset:520\n"
    "v_accvgpr_read_b32 v221, a65\n"
    "v_mfma_f32_16x16x32_bf16 a[68:71], a[244:247], v[24:27], a[68:71]\n"
    "ds_read_b64_tr_b16 a[46:47], v210 offset:4616\n"
    "v_accvgpr_read_b32 v222, a66\n"
    "v_mfma_f32_16x16x32_bf16 a[72:75], a[248:251], v[24:27], a[72:75]\n"
    "ds_read_b64_tr_b16 a[48:49], v210 offset:1024\n"
    "v_accvgpr_read_b32 v223, a67\n"
    "v_mfma_f32_16x16x32_bf16 a[72:75], a[168:171], v[72:75], a[72:75]\n"
    "ds_read_b64_tr_b16 a[50:51], v210 offset:5120\n"
    "v_accvgpr_read_b32 v224, a68\n"
    "v_mfma_f32_16x16x32_bf16 a[72:75], a[200:203], v[96:99], a[72:75]\n"
    "ds_read_b64_tr_b16 a[52:53], v210 offset:1032\n"
    "v_accvgpr_read_b32 v225, a69\n"
    "v_mfma_f32_16x16x32_bf16 a[72:75], a[8:11], v[120:123], a[72:75]\n"
    "ds_read_b64_tr_b16 a[54:55], v210 offset:5128\n"
    "v_accvgpr_read_b32 v226, a70\n"
    "v_mfma_f32_16x16x32_bf16 a[76:79], a[12:15], v[120:123], a[76:79]\n"
    "ds_read_b64_tr_b16 a[56:57], v210 offset:1536\n"
    "v_accvgpr_read_b32 v227, a71\n"
    "v_mfma_f32_16x16x32_bf16 a[76:79], a[204:207], v[96:99], a[76:79]\n"
    "ds_read_b64_tr_b16 a[58:59], v210 offset:5632\n"
    "v_accvgpr_read_b32 v228, a72\n"
    "v_mfma_f32_16x16x32_bf16 a[76:79], a[172:175], v[72:75], a[76:79]\n"
    "ds_read_b64_tr_b16 a[60:61], v210 offset:1544\n"
    "v_accvgpr_read_b32 v229, a73\n"
    "v_mfma_f32_16x16x32_bf16 a[76:79], a[252:255], v[24:27], a[76:79]\n"
    "ds_read_b64_tr_b16 a[62:63], v210 offset:5640\n"
    "v_xor_b32 v210, 0x4000, v210\n"
    "v_mfma_f32_16x16x32_bf16 a[80:83], v[40:43], v[24:27], a[80:83]\n"
    "ds_read_b64_tr_b16 a[128:129], v209 offset:0\n"
    "v_accvgpr_read_b32 v230, a74\n"
    "v_mfma_f32_16x16x32_bf16 a[80:83], a[176:179], v[72:75], a[80:83]\n"
    "ds_read_b64_tr_b16 a[130:131], v209 offset:4096\n"
    "v_accvgpr_read_b32 v231, a75\n"
    "v_mfma_f32_16x16x32_bf16 a[80:83], a[208:211], v[96:99], a[80:83]\n"
    "ds_read_b64_tr_b16 a[132:133], v209 offset:8\n"
    "v_accvgpr_read_b32 v232, a76\n"
    "v_mfma_f32_16x16x32_bf16 a[80:83], a[16:19], v[120:123], a[80:83]\n"
    "ds_read_b64_tr_b16 a[134:135], v209 offset:4104\n"
    "v_accvgpr_read_b32 v233, a77\n"
    "v_mfma_f32_16x16x32_bf16 a[84:87], a[20:23], v[120:123], a[84:87]\n"
    "ds_read_b64_tr_b16 a[136:137], v209 offset:512\n"
    "v_accvgpr_read_b32 v234, a78\n"
    "v_mfma_f32_16x16x32_bf16 a[84:87], a[212:215], v[96:99], a[84:87]\n"
    "ds_read_b64_tr_b16 a[138:139], v209 offset:4608\n"
    "v_accvgpr_read_b32 v235, a79\n"
    "v_mfma_f32_16x16x32_bf16 a[84:87], a[180:183], v[72:75], a[84:87]\n"
    "ds_read_b64_tr_b16 a[140:141], v209 offset:520\n"
    "v_permlane32_swap_b32 v173, v175\n"
    "v_mfma_f32_16x16x32_bf16 a[84:87], v[44:47], v[24:27], a[84:87]\n"
    "ds_read_b64_tr_b16 a[142:143], v209 offset:4616\n"
    "v_add_f32 v173, v173, v175\n"
    "v_mfma_f32_16x16x32_bf16 a[88:91], v[140:143], v[24:27], a[88:91]\n"
    ".Lgqa_T5:\n"
    "s_cbranch_execz .Lgqa_TBACK\n"
    "ds_read_b64_tr_b16 a[144:145], v209 offset:1024\n"
    "v_rcp_f32_e64 v173, v173\n"
    "buffer_store_dwordx4 v[4:7], v215, s[52:55], s78 offen offset:192 sc0 sc1\n"
    "v_pk_mul_f32 v[220:221], v[172:173], v[220:221] op_sel:[1,0] op_sel_hi:[1,1]\n"
    "v_pk_mul_f32 v[222:223], v[172:173], v[222:223] op_sel:[1,0] op_sel_hi:[1,1]\n"
    "v_pk_mul_f32 v[224:225], v[172:173], v[224:225] op_sel:[1,0] op_sel_hi:[1,1]\n"
    "v_pk_mul_f32 v[226:227], v[172:173], v[226:227] op_sel:[1,0] op_sel_hi:[1,1]\n"
    "v_cvt_pk_bf16_f32 v0, v220, v221\n"
    "v_mfma_f32_16x16x32_bf16 a[88:91], a[184:187], v[72:75], a[88:91]\n"
    "ds_read_b64_tr_b16 a[146:147], v209 offset:5120\n"
    "v_cvt_pk_bf16_f32 v1, v222, v223\n"
    "v_mfma_f32_16x16x32_bf16 a[88:91], a[216:219], v[96:99], a[88:91]\n"
    "ds_read_b64_tr_b16 a[148:149], v209 offset:1032\n"
    "v_cvt_pk_bf16_f32 v2, v224, v225\n"
    "v_mfma_f32_16x16x32_bf16 a[88:91], a[24:27], v[120:123], a[88:91]\n"
    "ds_read_b64_tr_b16 a[150:151], v209 offset:5128\n"
    "v_cvt_pk_bf16_f32 v3, v226, v227\n"
    "v_exp_f32 v156, v104\n"
    "v_exp_f32 v157, v105\n"
    "v_pk_mul_f32 v[228:229], v[172:173], v[228:229] op_sel:[1,0] op_sel_hi:[1,1]\n"
    "v_pk_mul_f32 v[230:231], v[172:173], v[230:231] op_sel:[1,0] op_sel_hi:[1,1]\n"
    "v_pk_mul_f32 v[232:233], v[172:173], v[232:233] op_sel:[1,0] op_sel_hi:[1,1]\n"
    "v_pk_mul_f32 v[234:235], v[172:173], v[234:235] op_sel:[1,0] op_sel_hi:[1,1]\n"
    "v_cvt_pk_bf16_f32 v8, v228, v229\n"
    "v_mfma_f32_16x16x32_bf16 a[92:95], a[28:31], v[120:123], a[92:95]\n"
    "ds_read_b64_tr_b16 a[152:153], v209 offset:1536\n"
    "v_cvt_pk_bf16_f32 v9, v230, v231\n"
    "v_mfma_f32_16x16x32_bf16 a[92:95], a[220:223], v[96:99], a[92:95]\n"
    "ds_read_b64_tr_b16 a[154:155], v209 offset:5632\n"
    "v_cvt_pk_bf16_f32 v10, v232, v233\n"
    "v_mfma_f32_16x16x32_bf16 a[92:95], a[188:191], v[72:75], a[92:95]\n"
    "ds_read_b64_tr_b16 a[156:157], v209 offset:1544\n"
    "v_cvt_pk_bf16_f32 v11, v234, v235\n"
    "v_mfma_f32_16x16x32_bf16 a[92:95], v[144:147], v[24:27], a[92:95]\n"
    "ds_read_b64_tr_b16 a[158:159], v209 offset:5640\n"
    "buffer_store_dwordx4 v[0:3], v215, s[52:55], s79 offen offset:0 sc0 sc1\n"
    "v_mfma_f32_16x16x32_bf16 a[96:99], a[240:243], v[32:35], a[96:99]\n"
    "v_cvt_pk_bf16_f32 v88, v156, v157\n"
    "v_accvgpr_read_b32 v220, a80\n"
    "v_mfma_f32_16x16x32_bf16 a[96:99], a[160:163], v[80:83], a[96:99]\n"
    "v_exp_f32 v158, v106\n"
    "v_mfma_f32_16x16x32_bf16 a[100:103], a[164:167], v[80:83], a[100:103]\n"
    "v_exp_f32 v159, v107\n"
    "v_mfma_f32_16x16x32_bf16 a[100:103], a[244:247], v[32:35], a[100:103]\n"
    "v_cvt_pk_bf16_f32 v89, v158, v159\n"
    "v_accvgpr_read_b32 v221, a81\n"
    "v_mfma_f32_16x16x32_bf16 a[104:107], a[248:251], v[32:35], a[104:107]\n"
    "v_exp_f32 v160, v36\n"
    "v_mfma_f32_16x16x32_bf16 a[104:107], a[168:171], v[80:83], a[104:107]\n"
    "v_exp_f32 v161, v37\n"
    "v_mfma_f32_16x16x32_bf16 a[108:111], a[172:175], v[80:83], a[108:111]\n"
    "v_cvt_pk_bf16_f32 v90, v160, v161\n"
    "v_accvgpr_read_b32 v222, a82\n"
    "v_mfma_f32_16x16x32_bf16 a[108:111], a[252:255], v[32:35], a[108:111]\n"
    "v_exp_f32 v162, v38\n"
    "v_mfma_f32_16x16x32_bf16 a[112:115], v[40:43], v[32:35], a[112:115]\n"
    "v_exp_f32 v163, v39\n"
    "v_mfma_f32_16x16x32_bf16 a[112:115], a[176:179], v[80:83], a[112:115]\n"
    "v_cvt_pk_bf16_f32 v91, v162, v163\n"
    "v_accvgpr_read_b32 v223, a83\n"
    "v_mfma_f32_16x16x32_bf16 a[116:119], a[180:183], v[80:83], a[116:119]\n"
    "buffer_store_dwordx4 v[8:11], v215, s[52:55], s79 offen offset:64 sc0 sc1\n"
    "v_mfma_f32_16x16x32_bf16 a[116:119], v[44:47], v[32:35], a[116:119]\n"
    "v_exp_f32 v156, v128\n"
    "v_mfma_f32_16x16x32_bf16 a[120:123], v[140:143], v[32:35], a[120:123]\n"
    "v_exp_f32 v157, v129\n"
    "v_mfma_f32_16x16x32_bf16 a[120:123], a[184:187], v[80:83], a[120:123]\n"
    "v_cvt_pk_bf16_f32 v92, v156, v157\n"
    "v_accvgpr_read_b32 v224, a84\n"
    "v_mfma_f32_16x16x32_bf16 a[124:127], a[188:191], v[80:83], a[124:127]\n"
    "v_exp_f32 v158, v130\n"
    "v_mfma_f32_16x16x32_bf16 a[124:127], v[144:147], v[32:35], a[124:127]\n"
    "v_exp_f32 v159, v131\n"
    "v_mfma_f32_16x16x32_bf16 v[108:111], v[252:255], v[88:91], 0\n"
    "v_cvt_pk_bf16_f32 v93, v158, v159\n"
    "v_exp_f32 v160, v84\n"
    "v_exp_f32 v161, v85\n"
    "v_exp_f32 v162, v86\n"
    "v_cvt_pk_bf16_f32 v94, v160, v161\n"
    "v_exp_f32 v163, v87\n"
    "v_exp_f32 v156, v52\n"
    "v_cvt_pk_bf16_f32 v95, v162, v163\n"
    "v_exp_f32 v157, v53\n"
    "v_accvgpr_read_b32 v225, a85\n"
    "v_mfma_f32_16x16x32_bf16 v[108:111], v[252:255], v[92:95], v[108:111]\n"
    "v_cvt_pk_bf16_f32 v112, v156, v157\n"
    "v_exp_f32 v158, v54\n"
    "v_exp_f32 v159, v55\n"
    "v_exp_f32 v160, v48\n"
    "v_cvt_pk_bf16_f32 v113, v158, v159\n"
    "v_exp_f32 v161, v49\n"
    "v_exp_f32 v162, v50\n"
    "v_cvt_pk_bf16_f32 v114, v160, v161\n"
    "v_exp_f32 v163, v51\n"
    "v_exp_f32 v156, v56\n"
    "v_cvt_pk_bf16_f32 v115, v162, v163\n"
    "v_exp_f32 v157, v57\n"
    "v_accvgpr_read_b32 v226, a86\n"
    "v_mfma_f32_16x16x32_bf16 v[108:111], v[252:255], v[112:115], v[108:111]\n"
    "v_cvt_pk_bf16_f32 v116, v156, v157\n"
    "v_exp_f32 v158, v58\n"
    "v_exp_f32 v159, v59\n"
    "v_exp_f32 v160, v60\n"
    "v_cvt_pk_bf16_f32 v117, v158, v159\n"
    "v_exp_f32 v161, v61\n"
    "v_exp_f32 v162, v62\n"
    "v_cvt_pk_bf16_f32 v118, v160, v161\n"
    "v_exp_f32 v163, v63\n"
    "v_exp_f32 v156, v16\n"
    "v_cvt_pk_bf16_f32 v119, v162, v163\n"
    "v_exp_f32 v157, v17\n"
    "v_accvgpr_read_b32 v227, a87\n"
    "v_mfma_f32_16x16x32_bf16 v[108:111], v[252:255], v[116:119], v[108:111]\n"
    "v_cvt_pk_bf16_f32 v100, v156, v157\n"
    "v_exp_f32 v158, v18\n"
    "v_exp_f32 v159, v19\n"
    "v_exp_f32 v160, v20\n"
    "v_cvt_pk_bf16_f32 v101, v158, v159\n"
    "v_exp_f32 v161, v21\n"
    "v_exp_f32 v162, v22\n"
    "v_cvt_pk_bf16_f32 v102, v160, v161\n"
    "v_exp_f32 v163, v23\n"
    "v_pk_mul_f32 v[220:221], v[172:173], v[220:221] op_sel:[1,0] op_sel_hi:[1,1]\n"
    "v_cvt_pk_bf16_f32 v103, v162, v163\n"
    "v_pk_mul_f32 v[222:223], v[172:173], v[222:223] op_sel:[1,0] op_sel_hi:[1,1]\n"
    "v_pk_mul_f32 v[224:225], v[172:173], v[224:225] op_sel:[1,0] op_sel_hi:[1,1]\n"
    "v_pk_mul_f32 v[226:227], v[172:173], v[226:227] op_sel:[1,0] op_sel_hi:[1,1]\n"
    "v_cvt_pk_bf16_f32 v12, v220, v221\n"
    "v_mfma_f32_16x16x32_bf16 v[108:111], v[252:255], v[100:103], v[108:111]\n"
    "v_cvt_pk_bf16_f32 v13, v222, v223\n"
    "v_cvt_pk_bf16_f32 v14, v224, v225\n"
    "v_cvt_pk_bf16_f32 v15, v226, v227\n"
    "buffer_store_dwordx4 v[12:15], v215, s[52:55], s79 offen offset:128 sc0 sc1\n"
    "v_accvgpr_read_b32 v228, a88\n"
    "v_accvgpr_read_b32 v229, a89\n"
    "v_accvgpr_read_b32 v230, a90\n"
    "v_accvgpr_read_b32 v231, a91\n"
    "v_accvgpr_read_b32 v232, a92\n"
    "v_fmac_f32 v174, 0x3e800000, v108\n"
    "v_cmp_lt_f32_e64 vcc, s64, v174\n"
    "v_accvgpr_read_b32 v233, a93\n"
    "v_accvgpr_read_b32 v234, a94\n"
    "v_accvgpr_read_b32 v235, a95\n"
    "v_mul_f32 v228, v228, v173\n"
    "v_mul_f32 v229, v229, v173\n"
    "v_mul_f32 v230, v230, v173\n"
    "s_cbranch_vccnz .Lgqa_STUB11\n"
    ".Lgqa_RET11:\n"
    "v_mfma_f32_16x16x32_bf16 a[96:99], a[192:195], v[88:91], a[96:99]\n"
    "v_mov_b32 v175, v174\n"
    "v_mul_f32 v231, v231, v173\n"
    "v_mfma_f32_16x16x32_bf16 a[96:99], a[0:3], v[92:95], a[96:99]\n"
    "s_waitcnt lgkmcnt(15)\n"
    "v_permlane16_swap_b32 v174, v175\n"
    "v_mfma_f32_16x16x32_bf16 a[96:99], v[176:179], v[112:115], a[96:99]\n"
    "v_add_f32 v174, v174, v175\n"
    "v_mov_b32 v175, v174\n"
    "v_mfma_f32_16x16x32_bf16 a[96:99], a[32:35], v[116:119], a[96:99]\n"
    "s_waitcnt lgkmcnt(14)\n"
    "v_mul_f32 v232, v232, v173\n"
    "v_mul_f32 v233, v233, v173\n"
    "v_mfma_f32_16x16x32_bf16 a[96:99], a[128:131], v[100:103], a[96:99]\n"
    "s_waitcnt lgkmcnt(12)\n"
    "v_permlane32_swap_b32 v174, v175\n"
    "v_mfma_f32_16x16x32_bf16 a[100:103], a[132:135], v[100:103], a[100:103]\n"
    "v_add_f32 v174, v174, v175\n"
    "v_mul_f32 v234, v234, v173\n"
    "v_mfma_f32_16x16x32_bf16 a[100:103], a[36:39], v[116:119], a[100:103]\n"
    "v_rcp_f32 v174, v174\n"
    "v_mfma_f32_16x16x32_bf16 a[100:103], v[180:183], v[112:115], a[100:103]\n"
    "v_mul_f32 v235, v235, v173\n"
    "v_cvt_pk_bf16_f32 v236, v228, v229\n"
    "v_mfma_f32_16x16x32_bf16 a[100:103], a[4:7], v[92:95], a[100:103]\n"
    "v_cvt_pk_bf16_f32 v237, v230, v231\n"
    "v_cvt_pk_bf16_f32 v238, v232, v233\n"
    "v_mfma_f32_16x16x32_bf16 a[100:103], a[196:199], v[88:91], a[100:103]\n"
    "v_cvt_pk_bf16_f32 v239, v234, v235\n"
    "buffer_store_dwordx4 v[236:239], v215, s[52:55], s79 offen offset:192 sc0 sc1\n"
    "v_mfma_f32_16x16x32_bf16 a[104:107], a[200:203], v[88:91], a[104:107]\n"
    "v_accvgpr_read_b32 v220, a96\n"
    "v_accvgpr_read_b32 v221, a97\n"
    "v_mfma_f32_16x16x32_bf16 a[104:107], a[8:11], v[92:95], a[104:107]\n"
    "v_accvgpr_read_b32 v222, a98\n"
    "v_accvgpr_read_b32 v223, a99\n"
    "v_mfma_f32_16x16x32_bf16 a[104:107], v[184:187], v[112:115], a[104:107]\n"
    "v_accvgpr_read_b32 v224, a100\n"
    "v_accvgpr_read_b32 v225, a101\n"
    "v_mfma_f32_16x16x32_bf16 a[104:107], a[40:43], v[116:119], a[104:107]\n"
    "s_waitcnt lgkmcnt(10)\n"
    "v_accvgpr_read_b32 v226, a102\n"
    "v_accvgpr_read_b32 v227, a103\n"
    "v_mfma_f32_16x16x32_bf16 a[104:107], a[136:139], v[100:103], a[104:107]\n"
    "s_waitcnt lgkmcnt(8)\n"
    "v_mul_f32 v220, v220, v174\n"
    "v_mul_f32 v221, v221, v174\n"
    "v_mfma_f32_16x16x32_bf16 a[108:111], a[140:143], v[100:103], a[108:111]\n"
    "v_mul_f32 v222, v222, v174\n"
    "v_mul_f32 v223, v223, v174\n"
    "v_mfma_f32_16x16x32_bf16 a[108:111], a[44:47], v[116:119], a[108:111]\n"
    "v_mul_f32 v224, v224, v174\n"
    "v_mul_f32 v225, v225, v174\n"
    "v_mfma_f32_16x16x32_bf16 a[108:111], v[188:191], v[112:115], a[108:111]\n"
    "v_mul_f32 v226, v226, v174\n"
    "v_mul_f32 v227, v227, v174\n"
    "v_mfma_f32_16x16x32_bf16 a[108:111], a[12:15], v[92:95], a[108:111]\n"
    "v_cvt_pk_bf16_f32 v240, v220, v221\n"
    "v_cvt_pk_bf16_f32 v241, v222, v223\n"
    "v_mfma_f32_16x16x32_bf16 a[108:111], a[204:207], v[88:91], a[108:111]\n"
    "v_cvt_pk_bf16_f32 v242, v224, v225\n"
    "v_cvt_pk_bf16_f32 v243, v226, v227\n"
    "v_mfma_f32_16x16x32_bf16 a[112:115], a[208:211], v[88:91], a[112:115]\n"
    "buffer_store_dwordx4 v[240:243], v215, s[52:55], s80 offen offset:0 sc0 sc1\n"
    "v_mfma_f32_16x16x32_bf16 a[112:115], a[16:19], v[92:95], a[112:115]\n"
    "v_accvgpr_read_b32 v228, a104\n"
    "v_accvgpr_read_b32 v229, a105\n"
    "v_mfma_f32_16x16x32_bf16 a[112:115], v[192:195], v[112:115], a[112:115]\n"
    "v_accvgpr_read_b32 v230, a106\n"
    "v_accvgpr_read_b32 v231, a107\n"
    "v_mfma_f32_16x16x32_bf16 a[112:115], a[48:51], v[116:119], a[112:115]\n"
    "s_waitcnt lgkmcnt(6)\n"
    "v_accvgpr_read_b32 v232, a108\n"
    "v_accvgpr_read_b32 v233, a109\n"
    "v_mfma_f32_16x16x32_bf16 a[112:115], a[144:147], v[100:103], a[112:115]\n"
    "s_waitcnt lgkmcnt(4)\n"
    "v_accvgpr_read_b32 v234, a110\n"
    "v_accvgpr_read_b32 v235, a111\n"
    "v_mfma_f32_16x16x32_bf16 a[116:119], a[148:151], v[100:103], a[116:119]\n"
    "v_mul_f32 v228, v228, v174\n"
    "v_mul_f32 v229, v229, v174\n"
    "v_mfma_f32_16x16x32_bf16 a[116:119], a[52:55], v[116:119], a[116:119]\n"
    "v_mul_f32 v230, v230, v174\n"
    "v_mul_f32 v231, v231, v174\n"
    "v_mfma_f32_16x16x32_bf16 a[116:119], v[196:199], v[112:115], a[116:119]\n"
    "v_mul_f32 v232, v232, v174\n"
    "v_mul_f32 v233, v233, v174\n"
    "v_mfma_f32_16x16x32_bf16 a[116:119], a[20:23], v[92:95], a[116:119]\n"
    "v_mul_f32 v234, v234, v174\n"
    "v_mul_f32 v235, v235, v174\n"
    "v_mfma_f32_16x16x32_bf16 a[116:119], a[212:215], v[88:91], a[116:119]\n"
    "v_cvt_pk_bf16_f32 v4, v228, v229\n"
    "v_cvt_pk_bf16_f32 v5, v230, v231\n"
    "v_mfma_f32_16x16x32_bf16 a[120:123], a[216:219], v[88:91], a[120:123]\n"
    "v_cvt_pk_bf16_f32 v6, v232, v233\n"
    "v_cvt_pk_bf16_f32 v7, v234, v235\n"
    "v_mfma_f32_16x16x32_bf16 a[120:123], a[24:27], v[92:95], a[120:123]\n"
    "buffer_store_dwordx4 v[4:7], v215, s[52:55], s80 offen offset:64 sc0 sc1\n"
    "v_mfma_f32_16x16x32_bf16 a[120:123], v[200:203], v[112:115], a[120:123]\n"
    "v_accvgpr_read_b32 v220, a112\n"
    "v_accvgpr_read_b32 v221, a113\n"
    "v_mfma_f32_16x16x32_bf16 a[120:123], a[56:59], v[116:119], a[120:123]\n"
    "s_waitcnt lgkmcnt(2)\n"
    "v_accvgpr_read_b32 v222, a114\n"
    "v_accvgpr_read_b32 v223, a115\n"
    "v_mfma_f32_16x16x32_bf16 a[120:123], a[152:155], v[100:103], a[120:123]\n"
    "s_waitcnt lgkmcnt(0)\n"
    "v_accvgpr_read_b32 v224, a116\n"
    "v_accvgpr_read_b32 v225, a117\n"
    "v_mfma_f32_16x16x32_bf16 a[124:127], a[156:159], v[100:103], a[124:127]\n"
    "v_accvgpr_read_b32 v226, a118\n"
    "v_accvgpr_read_b32 v227, a119\n"
    "v_mfma_f32_16x16x32_bf16 a[124:127], a[60:63], v[116:119], a[124:127]\n"
    "v_mul_f32 v220, v220, v174\n"
    "v_mul_f32 v221, v221, v174\n"
    "v_mfma_f32_16x16x32_bf16 a[124:127], v[204:207], v[112:115], a[124:127]\n"
    "v_mul_f32 v222, v222, v174\n"
    "v_mul_f32 v223, v223, v174\n"
    "v_mfma_f32_16x16x32_bf16 a[124:127], a[28:31], v[92:95], a[124:127]\n"
    "v_mul_f32 v224, v224, v174\n"
    "v_mul_f32 v225, v225, v174\n"
    "v_mfma_f32_16x16x32_bf16 a[124:127], a[220:223], v[88:91], a[124:127]\n"
    "v_mul_f32 v226, v226, v174\n"
    "v_mul_f32_e64 v227, v227, v174\n"
    "v_cvt_pk_bf16_f32 v0, v220, v221\n"
    "v_cvt_pk_bf16_f32 v1, v222, v223\n"
    "v_cvt_pk_bf16_f32 v2, v224, v225\n"
    "v_cvt_pk_bf16_f32 v3, v226, v227\n"
    "v_accvgpr_read_b32 v228, a120\n"
    "v_accvgpr_read_b32 v229, a121\n"
    "v_accvgpr_read_b32 v230, a122\n"
    "v_accvgpr_read_b32 v231, a123\n"
    "buffer_store_dwordx4 v[0:3], v215, s[52:55], s80 offen offset:128 sc0 sc1\n"
    "v_accvgpr_read_b32 v232, a124\n"
    "v_accvgpr_read_b32 v233, a125\n"
    "v_accvgpr_read_b32 v234, a126\n"
    "v_accvgpr_read_b32 v235, a127\n"
    "v_pk_mul_f32 v[228:229], v[174:175], v[228:229] op_sel:[0,0] op_sel_hi:[0,1]\n"
    "v_pk_mul_f32 v[230:231], v[174:175], v[230:231] op_sel:[0,0] op_sel_hi:[0,1]\n"
    "v_pk_mul_f32 v[232:233], v[174:175], v[232:233] op_sel:[0,0] op_sel_hi:[0,1]\n"
    "v_pk_mul_f32 v[234:235], v[174:175], v[234:235] op_sel:[0,0] op_sel_hi:[0,1]\n"
    "v_cvt_pk_bf16_f32 v8, v228, v229\n"
    "v_cvt_pk_bf16_f32 v9, v230, v231\n"
    "v_cvt_pk_bf16_f32 v10, v232, v233\n"
    "v_cvt_pk_bf16_f32 v11, v234, v235\n"
    "buffer_store_dwordx4 v[8:11], v215, s[52:55], s80 offen offset:192 sc0 sc1\n"
    "s_endpgm\n"
    ".Lgqa_HOTEND:\n"
    ".p2align 12\n"
    ".Lgqa_STUB0:\n"
    "s_nop 15\n"
    "s_call_b64 s[68:69], .Lgqa_HH0\n"
    "v_bfe_u32 v175, v211, 4, 1\n"
    "v_mul_u32_u24 v175, 0xaaaa, v175\n"
    "v_add_u32 v175, 0x4444, v175\n"
    "s_nop 3\n"
    "s_branch .Lgqa_RET0\n"
    ".Lgqa_STUB1:\n"
    "s_nop 15\n"
    "s_call_b64 s[68:69], .Lgqa_HH1\n"
    "v_bfe_u32 v175, v211, 4, 1\n"
    "v_mul_u32_u24 v175, 0xaaaa, v175\n"
    "v_add_u32 v175, 0x4444, v175\n"
    "s_nop 3\n"
    "s_branch .Lgqa_RET1\n"
    ".Lgqa_STUB2:\n"
    "s_nop 15\n"
    "s_call_b64 s[68:69], .Lgqa_HH2\n"
    "v_bfe_u32 v175, v211, 4, 1\n"
    "v_mul_u32_u24 v175, 0xaaaa, v175\n"
    "v_add_u32 v175, 0x4444, v175\n"
    "s_nop 3\n"
    "s_branch .Lgqa_RET2\n"
    ".Lgqa_STUB3:\n"
    "s_nop 15\n"
    "s_call_b64 s[68:69], .Lgqa_HH3\n"
    "v_bfe_u32 v175, v211, 4, 1\n"
    "v_mul_u32_u24 v175, 0xaaaa, v175\n"
    "v_add_u32 v175, 0x4444, v175\n"
    "s_nop 3\n"
    "s_branch .Lgqa_RET3\n"
    ".Lgqa_STUB4:\n"
    "s_nop 15\n"
    "s_call_b64 s[68:69], .Lgqa_HL10\n"
    "s_call_b64 s[68:69], .Lgqa_HL11\n"
    "s_call_b64 s[68:69], .Lgqa_HL12\n"
    "s_call_b64 s[68:69], .Lgqa_HL13\n"
    "v_bfe_u32 v175, v211, 4, 1\n"
    "v_mul_u32_u24 v175, 0xaaaa, v175\n"
    "v_add_u32 v175, 0x4444, v175\n"
    "s_nop 3\n"
    "s_branch .Lgqa_RET4\n"
    ".Lgqa_STUB5:\n"
    "s_nop 15\n"
    "s_call_b64 s[68:69], .Lgqa_HL00\n"
    "s_call_b64 s[68:69], .Lgqa_HL01\n"
    "s_call_b64 s[68:69], .Lgqa_HL02\n"
    "s_call_b64 s[68:69], .Lgqa_HL03\n"
    "s_call_b64 s[68:69], .Lgqa_HL04\n"
    "s_call_b64 s[68:69], .Lgqa_HMH\n"
    "v_bfe_u32 v175, v211, 4, 1\n"
    "v_mul_u32_u24 v175, 0xaaaa, v175\n"
    "v_add_u32 v175, 0x4444, v175\n"
    "s_nop 3\n"
    "s_branch .Lgqa_RET5\n"
    ".Lgqa_STUB6:\n"
    "s_nop 15\n"
    "s_call_b64 s[68:69], .Lgqa_HL10\n"
    "s_call_b64 s[68:69], .Lgqa_HL11\n"
    "s_call_b64 s[68:69], .Lgqa_HL12\n"
    "s_call_b64 s[68:69], .Lgqa_HL13\n"
    "s_call_b64 s[68:69], .Lgqa_HL14\n"
    "v_bfe_u32 v175, v211, 4, 1\n"
    "v_mul_u32_u24 v175, 0xaaaa, v175\n"
    "v_add_u32 v175, 0x4444, v175\n"
    "s_nop 3\n"
    "s_branch .Lgqa_RET6\n"
    ".Lgqa_STUB7:\n"
    "s_nop 15\n"
    "s_call_b64 s[68:69], .Lgqa_HT0_30\n"
    "v_bfe_u32 v175, v211, 4, 1\n"
    "v_mul_u32_u24 v175, 0xaaaa, v175\n"
    "v_add_u32 v175, 0x4444, v175\n"
    "s_nop 3\n"
    "s_branch .Lgqa_RET7\n"
    ".Lgqa_STUB8:\n"
    "s_nop 15\n"
    "s_call_b64 s[68:69], .Lgqa_HT1_30\n"
    "v_bfe_u32 v175, v211, 4, 1\n"
    "v_mul_u32_u24 v175, 0xaaaa, v175\n"
    "v_add_u32 v175, 0x4444, v175\n"
    "s_nop 3\n"
    "s_branch .Lgqa_RET8\n"
    ".Lgqa_STUB9:\n"
    "s_nop 15\n"
    "s_call_b64 s[68:69], .Lgqa_HT2_30\n"
    "v_bfe_u32 v175, v211, 4, 1\n"
    "v_mul_u32_u24 v175, 0xaaaa, v175\n"
    "v_add_u32 v175, 0x4444, v175\n"
    "s_nop 3\n"
    "s_branch .Lgqa_RET9\n"
    ".Lgqa_STUB10:\n"
    "s_nop 15\n"
    "s_call_b64 s[68:69], .Lgqa_HT3_30\n"
    "v_bfe_u32 v175, v211, 4, 1\n"
    "v_mul_u32_u24 v175, 0xaaaa, v175\n"
    "v_add_u32 v175, 0x4444, v175\n"
    "s_nop 3\n"
    "s_branch .Lgqa_RET10\n"
    ".Lgqa_STUB11:\n"
    "s_nop 15\n"
    "s_call_b64 s[68:69], .Lgqa_HT4_30\n"
    "v_bfe_u32 v175, v211, 4, 1\n"
    "v_mul_u32_u24 v175, 0xaaaa, v175\n"
    "v_add_u32 v175, 0x4444, v175\n"
    "s_nop 3\n"
    "s_branch .Lgqa_RET11\n"
    ".Lgqa_STUB12:\n"
    "s_nop 15\n"
    "s_call_b64 s[68:69], .Lgqa_HL00\n"
    "s_call_b64 s[68:69], .Lgqa_HL01\n"
    "s_call_b64 s[68:69], .Lgqa_HL02\n"
    "s_call_b64 s[68:69], .Lgqa_HL03\n"
    "s_call_b64 s[68:69], .Lgqa_HL04\n"
    "s_call_b64 s[68:69], .Lgqa_HMH\n"
    "v_bfe_u32 v175, v211, 4, 1\n"
    "v_mul_u32_u24 v175, 0xaaaa, v175\n"
    "v_add_u32 v175, 0x4444, v175\n"
    "s_nop 3\n"
    "s_branch .Lgqa_RET12\n"
    ".Lgqa_HL00:\n"
    "v_add_f32 v164, v164, v165\n"
    "v_mov_b32 v165, 0\n"
    "v_cmp_lt_f32 vcc, s64, v164\n"
    "s_nop 1\n"
    "s_cbranch_vccz .Lgqa_HL00_ret\n"
    "v_mov_b32 v175, v164\n"
    "v_cmp_lt_f32 vcc, 0x71800000, v175\n"
    "s_nop 1\n"
    "s_cbranch_vccnz .Lgqa_HL00_slow\n"
    "v_frexp_exp_i32_f32 v175, v175\n"
    "v_max_i32 v175, 0, v175\n"
    "v_cvt_f32_i32 v214, v175\n"
    "v_sub_f32 v88, v88, v214\n"
    "v_sub_f32 v89, v89, v214\n"
    "v_sub_f32 v90, v90, v214\n"
    "v_sub_f32 v91, v91, v214\n"
    "v_sub_f32 v0, v0, v214\n"
    "v_sub_f32 v1, v1, v214\n"
    "v_sub_f32 v2, v2, v214\n"
    "v_sub_f32 v3, v3, v214\n"
    "v_sub_f32 v132, v132, v214\n"
    "v_sub_f32 v133, v133, v214\n"
    "v_sub_f32 v134, v134, v214\n"
    "v_sub_f32 v135, v135, v214\n"
    "v_sub_u32 v175, 0, v175\n"
    "v_ldexp_f32 v214, 1.0, v175\n"
    "v_mul_f32 v164, v164, v214\n"
    "v_lshlrev_b32 v156, 16, v4\n"
    "v_and_b32 v157, 0xffff0000, v4\n"
    "v_lshlrev_b32 v158, 16, v5\n"
    "v_and_b32 v159, 0xffff0000, v5\n"
    "v_lshlrev_b32 v160, 16, v6\n"
    "v_and_b32 v161, 0xffff0000, v6\n"
    "v_lshlrev_b32 v162, 16, v7\n"
    "v_and_b32 v163, 0xffff0000, v7\n"
    "v_mul_f32 v156, v156, v214\n"
    "v_mul_f32 v157, v157, v214\n"
    "v_mul_f32 v158, v158, v214\n"
    "v_mul_f32 v159, v159, v214\n"
    "v_mul_f32 v160, v160, v214\n"
    "v_mul_f32 v161, v161, v214\n"
    "v_mul_f32 v162, v162, v214\n"
    "v_mul_f32 v163, v163, v214\n"
    "v_cvt_pk_bf16_f32 v4, v156, v157\n"
    "v_cvt_pk_bf16_f32 v5, v158, v159\n"
    "v_cvt_pk_bf16_f32 v6, v160, v161\n"
    "v_cvt_pk_bf16_f32 v7, v162, v163\n"
    "v_lshlrev_b32 v156, 16, v8\n"
    "v_and_b32 v157, 0xffff0000, v8\n"
    "v_lshlrev_b32 v158, 16, v9\n"
    "v_and_b32 v159, 0xffff0000, v9\n"
    "v_lshlrev_b32 v160, 16, v10\n"
    "v_and_b32 v161, 0xffff0000, v10\n"
    "v_lshlrev_b32 v162, 16, v11\n"
    "v_and_b32 v163, 0xffff0000, v11\n"
    "v_mul_f32 v156, v156, v214\n"
    "v_mul_f32 v157, v157, v214\n"
    "v_mul_f32 v158, v158, v214\n"
    "v_mul_f32 v159, v159, v214\n"
    "v_mul_f32 v160, v160, v214\n"
    "v_mul_f32 v161, v161, v214\n"
    "v_mul_f32 v162, v162, v214\n"
    "v_mul_f32 v163, v163, v214\n"
    "v_cvt_pk_bf16_f32 v8, v156, v157\n"
    "v_cvt_pk_bf16_f32 v9, v158, v159\n"
    "v_cvt_pk_bf16_f32 v10, v160, v161\n"
    "v_cvt_pk_bf16_f32 v11, v162, v163\n"
    "s_branch .Lgqa_HL00_rs\n"
    ".Lgqa_HL00_slow:\n"
    "s_sub_u32 s82, s58, 0x0\n"
    "s_and_b32 s82, s82, 0x3ffff\n"
    "v_mov_b32 v214, 0\n"
    "s_xor_b32 s83, s82, 0x0\n"
    "s_nop 1\n"
    "s_call_b64 s[70:71], .Lgqa_SQK0\n"
    "v_max3_f32 v214, v214, v12, v13\n"
    "v_max3_f32 v214, v214, v14, v15\n"
    "s_xor_b32 s83, s82, 0x1000\n"
    "s_nop 1\n"
    "s_call_b64 s[70:71], .Lgqa_SQK0\n"
    "v_max3_f32 v214, v214, v12, v13\n"
    "v_max3_f32 v214, v214, v14, v15\n"
    "s_xor_b32 s83, s82, 0x2000\n"
    "s_nop 1\n"
    "s_call_b64 s[70:71], .Lgqa_SQK0\n"
    "v_max3_f32 v214, v214, v12, v13\n"
    "v_max3_f32 v214, v214, v14, v15\n"
    "s_xor_b32 s83, s82, 0x3000\n"
    "s_nop 1\n"
    "s_call_b64 s[70:71], .Lgqa_SQK0\n"
    "v_max3_f32 v214, v214, v12, v13\n"
    "v_max3_f32 v214, v214, v14, v15\n"
    "v_mov_b32 v156, v214\n"
    "s_nop 1\n"
    "v_permlane16_swap_b32 v214, v156\n"
    "v_max_f32 v214, v214, v156\n"
    "v_mov_b32 v156, v214\n"
    "s_nop 1\n"
    "v_permlane32_swap_b32 v214, v156\n"
    "v_max_f32 v214, v214, v156\n"
    "v_min_f32 v157, 0x71800000, v175\n"
    "v_log_f32 v157, v157\n"
    "s_nop 0\n"
    "v_max_f32 v214, v214, v157\n"
    "v_sub_f32 v88, v88, v214\n"
    "v_sub_f32 v89, v89, v214\n"
    "v_sub_f32 v90, v90, v214\n"
    "v_sub_f32 v91, v91, v214\n"
    "v_sub_f32 v0, v0, v214\n"
    "v_sub_f32 v1, v1, v214\n"
    "v_sub_f32 v2, v2, v214\n"
    "v_sub_f32 v3, v3, v214\n"
    "v_sub_f32 v132, v132, v214\n"
    "v_sub_f32 v133, v133, v214\n"
    "v_sub_f32 v134, v134, v214\n"
    "v_sub_f32 v135, v135, v214\n"
    "v_exp_f32 v214, -v214\n"
    "v_mov_b32 v166, 0\n"
    "s_xor_b32 s83, s82, 0x0\n"
    "s_nop 1\n"
    "s_call_b64 s[70:71], .Lgqa_SQK0\n"
    "v_exp_f32 v12, v12\n"
    "v_exp_f32 v13, v13\n"
    "v_exp_f32 v14, v14\n"
    "v_exp_f32 v15, v15\n"
    "v_add_f32 v166, v166, v12\n"
    "v_add_f32 v166, v166, v13\n"
    "v_add_f32 v166, v166, v14\n"
    "v_add_f32 v166, v166, v15\n"
    "v_cvt_pk_bf16_f32 v4, v12, v13\n"
    "v_cvt_pk_bf16_f32 v5, v14, v15\n"
    "s_xor_b32 s83, s82, 0x1000\n"
    "s_nop 1\n"
    "s_call_b64 s[70:71], .Lgqa_SQK0\n"
    "v_exp_f32 v12, v12\n"
    "v_exp_f32 v13, v13\n"
    "v_exp_f32 v14, v14\n"
    "v_exp_f32 v15, v15\n"
    "v_add_f32 v166, v166, v12\n"
    "v_add_f32 v166, v166, v13\n"
    "v_add_f32 v166, v166, v14\n"
    "v_add_f32 v166, v166, v15\n"
    "v_cvt_pk_bf16_f32 v6, v12, v13\n"
    "v_cvt_pk_bf16_f32 v7, v14, v15\n"
    "s_xor_b32 s83, s82, 0x2000\n"
    "s_nop 1\n"
    "s_call_b64 s[70:71], .Lgqa_SQK0\n"
    "v_exp_f32 v12, v12\n"
    "v_exp_f32 v13, v13\n"
    "v_exp_f32 v14, v14\n"
    "v_exp_f32 v15, v15\n"
    "v_add_f32 v166, v166, v12\n"
    "v_add_f32 v166, v166, v13\n"
    "v_add_f32 v166, v166, v14\n"
    "v_add_f32 v166, v166, v15\n"
    "v_cvt_pk_bf16_f32 v8, v12, v13\n"
    "v_cvt_pk_bf16_f32 v9, v14, v15\n"
    "s_xor_b32 s83, s82, 0x3000\n"
    "s_nop 1\n"
    "s_call_b64 s[70:71], .Lgqa_SQK0\n"
    "v_exp_f32 v12, v12\n"
    "v_exp_f32 v13, v13\n"
    "v_exp_f32 v14, v14\n"
    "v_exp_f32 v15, v15\n"
    "v_add_f32 v166, v166, v12\n"
    "v_add_f32 v166, v166, v13\n"
    "v_add_f32 v166, v166, v14\n"
    "v_add_f32 v166, v166, v15\n"
    "v_cvt_pk_bf16_f32 v10, v12, v13\n"
    "v_cvt_pk_bf16_f32 v11, v14, v15\n"
    "v_mov_b32 v156, v166\n"
    "s_nop 1\n"
    "v_permlane16_swap_b32 v166, v156\n"
    "v_add_f32 v166, v166, v156\n"
    "v_mov_b32 v156, v166\n"
    "s_nop 1\n"
    "v_permlane32_swap_b32 v166, v156\n"
    "v_add_f32 v166, v166, v156\n"
    "v_cmp_lt_f32 vcc, 0x71800000, v175\n"
    "v_mul_f32 v164, v164, v214\n"
    "s_nop 0\n"
    "v_cndmask_b32 v164, v164, v166, vcc\n"
    ".Lgqa_HL00_rs:\n"
    "s_call_b64 s[70:71], .Lgqa_RS0\n"
    ".Lgqa_HL00_ret:\n"
    "s_setpc_b64 s[68:69]\n"
    ".Lgqa_HL10:\n"
    "v_add_f32 v164, v164, v165\n"
    "v_mov_b32 v165, 0\n"
    "v_cmp_lt_f32 vcc, s64, v164\n"
    "s_nop 1\n"
    "s_cbranch_vccz .Lgqa_HL10_ret\n"
    "v_mov_b32 v175, v164\n"
    "v_cmp_lt_f32 vcc, 0x71800000, v175\n"
    "s_nop 1\n"
    "s_cbranch_vccnz .Lgqa_HL10_slow\n"
    "v_frexp_exp_i32_f32 v175, v175\n"
    "v_max_i32 v175, 0, v175\n"
    "v_cvt_f32_i32 v214, v175\n"
    "v_sub_f32 v4, v4, v214\n"
    "v_sub_f32 v5, v5, v214\n"
    "v_sub_f32 v6, v6, v214\n"
    "v_sub_f32 v7, v7, v214\n"
    "v_sub_f32 v0, v0, v214\n"
    "v_sub_f32 v1, v1, v214\n"
    "v_sub_f32 v2, v2, v214\n"
    "v_sub_f32 v3, v3, v214\n"
    "v_sub_f32 v132, v132, v214\n"
    "v_sub_f32 v133, v133, v214\n"
    "v_sub_f32 v134, v134, v214\n"
    "v_sub_f32 v135, v135, v214\n"
    "v_sub_u32 v175, 0, v175\n"
    "v_ldexp_f32 v214, 1.0, v175\n"
    "v_mul_f32 v164, v164, v214\n"
    "v_lshlrev_b32 v156, 16, v88\n"
    "v_and_b32 v157, 0xffff0000, v88\n"
    "v_lshlrev_b32 v158, 16, v89\n"
    "v_and_b32 v159, 0xffff0000, v89\n"
    "v_lshlrev_b32 v160, 16, v90\n"
    "v_and_b32 v161, 0xffff0000, v90\n"
    "v_lshlrev_b32 v162, 16, v91\n"
    "v_and_b32 v163, 0xffff0000, v91\n"
    "v_mul_f32 v156, v156, v214\n"
    "v_mul_f32 v157, v157, v214\n"
    "v_mul_f32 v158, v158, v214\n"
    "v_mul_f32 v159, v159, v214\n"
    "v_mul_f32 v160, v160, v214\n"
    "v_mul_f32 v161, v161, v214\n"
    "v_mul_f32 v162, v162, v214\n"
    "v_mul_f32 v163, v163, v214\n"
    "v_cvt_pk_bf16_f32 v88, v156, v157\n"
    "v_cvt_pk_bf16_f32 v89, v158, v159\n"
    "v_cvt_pk_bf16_f32 v90, v160, v161\n"
    "v_cvt_pk_bf16_f32 v91, v162, v163\n"
    "v_lshlrev_b32 v156, 16, v92\n"
    "v_and_b32 v157, 0xffff0000, v92\n"
    "v_lshlrev_b32 v158, 16, v93\n"
    "v_and_b32 v159, 0xffff0000, v93\n"
    "v_lshlrev_b32 v160, 16, v94\n"
    "v_and_b32 v161, 0xffff0000, v94\n"
    "v_lshlrev_b32 v162, 16, v95\n"
    "v_and_b32 v163, 0xffff0000, v95\n"
    "v_mul_f32 v156, v156, v214\n"
    "v_mul_f32 v157, v157, v214\n"
    "v_mul_f32 v158, v158, v214\n"
    "v_mul_f32 v159, v159, v214\n"
    "v_mul_f32 v160, v160, v214\n"
    "v_mul_f32 v161, v161, v214\n"
    "v_mul_f32 v162, v162, v214\n"
    "v_mul_f32 v163, v163, v214\n"
    "v_cvt_pk_bf16_f32 v92, v156, v157\n"
    "v_cvt_pk_bf16_f32 v93, v158, v159\n"
    "v_cvt_pk_bf16_f32 v94, v160, v161\n"
    "v_cvt_pk_bf16_f32 v95, v162, v163\n"
    "s_branch .Lgqa_HL10_rs\n"
    ".Lgqa_HL10_slow:\n"
    "s_sub_u32 s82, s58, 0x8000\n"
    "s_and_b32 s82, s82, 0x3ffff\n"
    "v_mov_b32 v214, 0\n"
    "s_xor_b32 s83, s82, 0x4000\n"
    "s_nop 1\n"
    "s_call_b64 s[70:71], .Lgqa_SQK0\n"
    "v_max3_f32 v214, v214, v12, v13\n"
    "v_max3_f32 v214, v214, v14, v15\n"
    "s_xor_b32 s83, s82, 0x5000\n"
    "s_nop 1\n"
    "s_call_b64 s[70:71], .Lgqa_SQK0\n"
    "v_max3_f32 v214, v214, v12, v13\n"
    "v_max3_f32 v214, v214, v14, v15\n"
    "s_xor_b32 s83, s82, 0x6000\n"
    "s_nop 1\n"
    "s_call_b64 s[70:71], .Lgqa_SQK0\n"
    "v_max3_f32 v214, v214, v12, v13\n"
    "v_max3_f32 v214, v214, v14, v15\n"
    "s_xor_b32 s83, s82, 0x7000\n"
    "s_nop 1\n"
    "s_call_b64 s[70:71], .Lgqa_SQK0\n"
    "v_max3_f32 v214, v214, v12, v13\n"
    "v_max3_f32 v214, v214, v14, v15\n"
    "v_mov_b32 v156, v214\n"
    "s_nop 1\n"
    "v_permlane16_swap_b32 v214, v156\n"
    "v_max_f32 v214, v214, v156\n"
    "v_mov_b32 v156, v214\n"
    "s_nop 1\n"
    "v_permlane32_swap_b32 v214, v156\n"
    "v_max_f32 v214, v214, v156\n"
    "v_min_f32 v157, 0x71800000, v175\n"
    "v_log_f32 v157, v157\n"
    "s_nop 0\n"
    "v_max_f32 v214, v214, v157\n"
    "v_sub_f32 v4, v4, v214\n"
    "v_sub_f32 v5, v5, v214\n"
    "v_sub_f32 v6, v6, v214\n"
    "v_sub_f32 v7, v7, v214\n"
    "v_sub_f32 v0, v0, v214\n"
    "v_sub_f32 v1, v1, v214\n"
    "v_sub_f32 v2, v2, v214\n"
    "v_sub_f32 v3, v3, v214\n"
    "v_sub_f32 v132, v132, v214\n"
    "v_sub_f32 v133, v133, v214\n"
    "v_sub_f32 v134, v134, v214\n"
    "v_sub_f32 v135, v135, v214\n"
    "v_exp_f32 v214, -v214\n"
    "v_mov_b32 v166, 0\n"
    "s_xor_b32 s83, s82, 0x4000\n"
    "s_nop 1\n"
    "s_call_b64 s[70:71], .Lgqa_SQK0\n"
    "v_exp_f32 v12, v12\n"
    "v_exp_f32 v13, v13\n"
    "v_exp_f32 v14, v14\n"
    "v_exp_f32 v15, v15\n"
    "v_add_f32 v166, v166, v12\n"
    "v_add_f32 v166, v166, v13\n"
    "v_add_f32 v166, v166, v14\n"
    "v_add_f32 v166, v166, v15\n"
    "v_cvt_pk_bf16_f32 v88, v12, v13\n"
    "v_cvt_pk_bf16_f32 v89, v14, v15\n"
    "s_xor_b32 s83, s82, 0x5000\n"
    "s_nop 1\n"
    "s_call_b64 s[70:71], .Lgqa_SQK0\n"
    "v_exp_f32 v12, v12\n"
    "v_exp_f32 v13, v13\n"
    "v_exp_f32 v14, v14\n"
    "v_exp_f32 v15, v15\n"
    "v_add_f32 v166, v166, v12\n"
    "v_add_f32 v166, v166, v13\n"
    "v_add_f32 v166, v166, v14\n"
    "v_add_f32 v166, v166, v15\n"
    "v_cvt_pk_bf16_f32 v90, v12, v13\n"
    "v_cvt_pk_bf16_f32 v91, v14, v15\n"
    "s_xor_b32 s83, s82, 0x6000\n"
    "s_nop 1\n"
    "s_call_b64 s[70:71], .Lgqa_SQK0\n"
    "v_exp_f32 v12, v12\n"
    "v_exp_f32 v13, v13\n"
    "v_exp_f32 v14, v14\n"
    "v_exp_f32 v15, v15\n"
    "v_add_f32 v166, v166, v12\n"
    "v_add_f32 v166, v166, v13\n"
    "v_add_f32 v166, v166, v14\n"
    "v_add_f32 v166, v166, v15\n"
    "v_cvt_pk_bf16_f32 v92, v12, v13\n"
    "v_cvt_pk_bf16_f32 v93, v14, v15\n"
    "s_xor_b32 s83, s82, 0x7000\n"
    "s_nop 1\n"
    "s_call_b64 s[70:71], .Lgqa_SQK0\n"
    "v_exp_f32 v12, v12\n"
    "v_exp_f32 v13, v13\n"
    "v_exp_f32 v14, v14\n"
    "v_exp_f32 v15, v15\n"
    "v_add_f32 v166, v166, v12\n"
    "v_add_f32 v166, v166, v13\n"
    "v_add_f32 v166, v166, v14\n"
    "v_add_f32 v166, v166, v15\n"
    "v_cvt_pk_bf16_f32 v94, v12, v13\n"
    "v_cvt_pk_bf16_f32 v95, v14, v15\n"
    "v_mov_b32 v156, v166\n"
    "s_nop 1\n"
    "v_permlane16_swap_b32 v166, v156\n"
    "v_add_f32 v166, v166, v156\n"
    "v_mov_b32 v156, v166\n"
    "s_nop 1\n"
    "v_permlane32_swap_b32 v166, v156\n"
    "v_add_f32 v166, v166, v156\n"
    "v_cmp_lt_f32 vcc, 0x71800000, v175\n"
    "v_mul_f32 v164, v164, v214\n"
    "s_nop 0\n"
    "v_cndmask_b32 v164, v164, v166, vcc\n"
    ".Lgqa_HL10_rs:\n"
    "s_call_b64 s[70:71], .Lgqa_RS0\n"
    ".Lgqa_HL10_ret:\n"
    "s_setpc_b64 s[68:69]\n"
    ".Lgqa_SQK0:\n"
    "buffer_load_dwordx4 v[156:159], v215, s[40:43], s83 offen offset:0\n"
    "buffer_load_dwordx4 v[160:163], v215, s[40:43], s83 offen offset:64\n"
    "s_waitcnt vmcnt(0)\n"
    "v_mfma_f32_16x16x32_bf16 v[12:15], v[156:159], a[160:163], v[132:135]\n"
    "v_mfma_f32_16x16x32_bf16 v[12:15], v[160:163], a[164:167], v[12:15]\n"
    "s_nop 15\n"
    "buffer_load_dwordx4 v[156:159], v215, s[40:43], s83 offen offset:128\n"
    "buffer_load_dwordx4 v[160:163], v215, s[40:43], s83 offen offset:192\n"
    "s_waitcnt vmcnt(0)\n"
    "v_mfma_f32_16x16x32_bf16 v[12:15], v[156:159], a[168:171], v[12:15]\n"
    "v_mfma_f32_16x16x32_bf16 v[12:15], v[160:163], a[172:175], v[12:15]\n"
    "s_nop 15\n"
    "s_setpc_b64 s[70:71]\n"
    ".Lgqa_HL01:\n"
    "v_add_f32 v168, v168, v169\n"
    "v_mov_b32 v169, 0\n"
    "v_cmp_lt_f32 vcc, s64, v168\n"
    "s_nop 1\n"
    "s_cbranch_vccz .Lgqa_HL01_ret\n"
    "v_mov_b32 v175, v168\n"
    "v_cmp_lt_f32 vcc, 0x71800000, v175\n"
    "s_nop 1\n"
    "s_cbranch_vccnz .Lgqa_HL01_slow\n"
    "v_frexp_exp_i32_f32 v175, v175\n"
    "v_max_i32 v175, 0, v175\n"
    "v_cvt_f32_i32 v214, v175\n"
    "v_sub_f32 v112, v112, v214\n"
    "v_sub_f32 v113, v113, v214\n"
    "v_sub_f32 v114, v114, v214\n"
    "v_sub_f32 v115, v115, v214\n"
    "v_sub_f32 v48, v48, v214\n"
    "v_sub_f32 v49, v49, v214\n"
    "v_sub_f32 v50, v50, v214\n"
    "v_sub_f32 v51, v51, v214\n"
    "v_sub_f32 v136, v136, v214\n"
    "v_sub_f32 v137, v137, v214\n"
    "v_sub_f32 v138, v138, v214\n"
    "v_sub_f32 v139, v139, v214\n"
    "v_sub_u32 v175, 0, v175\n"
    "v_ldexp_f32 v214, 1.0, v175\n"
    "v_mul_f32 v168, v168, v214\n"
    "v_lshlrev_b32 v156, 16, v52\n"
    "v_and_b32 v157, 0xffff0000, v52\n"
    "v_lshlrev_b32 v158, 16, v53\n"
    "v_and_b32 v159, 0xffff0000, v53\n"
    "v_lshlrev_b32 v160, 16, v54\n"
    "v_and_b32 v161, 0xffff0000, v54\n"
    "v_lshlrev_b32 v162, 16, v55\n"
    "v_and_b32 v163, 0xffff0000, v55\n"
    "v_mul_f32 v156, v156, v214\n"
    "v_mul_f32 v157, v157, v214\n"
    "v_mul_f32 v158, v158, v214\n"
    "v_mul_f32 v159, v159, v214\n"
    "v_mul_f32 v160, v160, v214\n"
    "v_mul_f32 v161, v161, v214\n"
    "v_mul_f32 v162, v162, v214\n"
    "v_mul_f32 v163, v163, v214\n"
    "v_cvt_pk_bf16_f32 v52, v156, v157\n"
    "v_cvt_pk_bf16_f32 v53, v158, v159\n"
    "v_cvt_pk_bf16_f32 v54, v160, v161\n"
    "v_cvt_pk_bf16_f32 v55, v162, v163\n"
    "v_lshlrev_b32 v156, 16, v56\n"
    "v_and_b32 v157, 0xffff0000, v56\n"
    "v_lshlrev_b32 v158, 16, v57\n"
    "v_and_b32 v159, 0xffff0000, v57\n"
    "v_lshlrev_b32 v160, 16, v58\n"
    "v_and_b32 v161, 0xffff0000, v58\n"
    "v_lshlrev_b32 v162, 16, v59\n"
    "v_and_b32 v163, 0xffff0000, v59\n"
    "v_mul_f32 v156, v156, v214\n"
    "v_mul_f32 v157, v157, v214\n"
    "v_mul_f32 v158, v158, v214\n"
    "v_mul_f32 v159, v159, v214\n"
    "v_mul_f32 v160, v160, v214\n"
    "v_mul_f32 v161, v161, v214\n"
    "v_mul_f32 v162, v162, v214\n"
    "v_mul_f32 v163, v163, v214\n"
    "v_cvt_pk_bf16_f32 v56, v156, v157\n"
    "v_cvt_pk_bf16_f32 v57, v158, v159\n"
    "v_cvt_pk_bf16_f32 v58, v160, v161\n"
    "v_cvt_pk_bf16_f32 v59, v162, v163\n"
    "s_branch .Lgqa_HL01_rs\n"
    ".Lgqa_HL01_slow:\n"
    "s_sub_u32 s82, s58, 0x0\n"
    "s_and_b32 s82, s82, 0x3ffff\n"
    "v_mov_b32 v214, 0\n"
    "s_xor_b32 s83, s82, 0x0\n"
    "s_nop 1\n"
    "s_call_b64 s[70:71], .Lgqa_SQK1\n"
    "v_max3_f32 v214, v214, v60, v61\n"
    "v_max3_f32 v214, v214, v62, v63\n"
    "s_xor_b32 s83, s82, 0x1000\n"
    "s_nop 1\n"
    "s_call_b64 s[70:71], .Lgqa_SQK1\n"
    "v_max3_f32 v214, v214, v60, v61\n"
    "v_max3_f32 v214, v214, v62, v63\n"
    "s_xor_b32 s83, s82, 0x2000\n"
    "s_nop 1\n"
    "s_call_b64 s[70:71], .Lgqa_SQK1\n"
    "v_max3_f32 v214, v214, v60, v61\n"
    "v_max3_f32 v214, v214, v62, v63\n"
    "s_xor_b32 s83, s82, 0x3000\n"
    "s_nop 1\n"
    "s_call_b64 s[70:71], .Lgqa_SQK1\n"
    "v_max3_f32 v214, v214, v60, v61\n"
    "v_max3_f32 v214, v214, v62, v63\n"
    "v_mov_b32 v156, v214\n"
    "s_nop 1\n"
    "v_permlane16_swap_b32 v214, v156\n"
    "v_max_f32 v214, v214, v156\n"
    "v_mov_b32 v156, v214\n"
    "s_nop 1\n"
    "v_permlane32_swap_b32 v214, v156\n"
    "v_max_f32 v214, v214, v156\n"
    "v_min_f32 v157, 0x71800000, v175\n"
    "v_log_f32 v157, v157\n"
    "s_nop 0\n"
    "v_max_f32 v214, v214, v157\n"
    "v_sub_f32 v112, v112, v214\n"
    "v_sub_f32 v113, v113, v214\n"
    "v_sub_f32 v114, v114, v214\n"
    "v_sub_f32 v115, v115, v214\n"
    "v_sub_f32 v48, v48, v214\n"
    "v_sub_f32 v49, v49, v214\n"
    "v_sub_f32 v50, v50, v214\n"
    "v_sub_f32 v51, v51, v214\n"
    "v_sub_f32 v136, v136, v214\n"
    "v_sub_f32 v137, v137, v214\n"
    "v_sub_f32 v138, v138, v214\n"
    "v_sub_f32 v139, v139, v214\n"
    "v_exp_f32 v214, -v214\n"
    "v_mov_b32 v166, 0\n"
    "s_xor_b32 s83, s82, 0x0\n"
    "s_nop 1\n"
    "s_call_b64 s[70:71], .Lgqa_SQK1\n"
    "v_exp_f32 v60, v60\n"
    "v_exp_f32 v61, v61\n"
    "v_exp_f32 v62, v62\n"
    "v_exp_f32 v63, v63\n"
    "v_add_f32 v166, v166, v60\n"
    "v_add_f32 v166, v166, v61\n"
    "v_add_f32 v166, v166, v62\n"
    "v_add_f32 v166, v166, v63\n"
    "v_cvt_pk_bf16_f32 v52, v60, v61\n"
    "v_cvt_pk_bf16_f32 v53, v62, v63\n"
    "s_xor_b32 s83, s82, 0x1000\n"
    "s_nop 1\n"
    "s_call_b64 s[70:71], .Lgqa_SQK1\n"
    "v_exp_f32 v60, v60\n"
    "v_exp_f32 v61, v61\n"
    "v_exp_f32 v62, v62\n"
    "v_exp_f32 v63, v63\n"
    "v_add_f32 v166, v166, v60\n"
    "v_add_f32 v166, v166, v61\n"
    "v_add_f32 v166, v166, v62\n"
    "v_add_f32 v166, v166, v63\n"
    "v_cvt_pk_bf16_f32 v54, v60, v61\n"
    "v_cvt_pk_bf16_f32 v55, v62, v63\n"
    "s_xor_b32 s83, s82, 0x2000\n"
    "s_nop 1\n"
    "s_call_b64 s[70:71], .Lgqa_SQK1\n"
    "v_exp_f32 v60, v60\n"
    "v_exp_f32 v61, v61\n"
    "v_exp_f32 v62, v62\n"
    "v_exp_f32 v63, v63\n"
    "v_add_f32 v166, v166, v60\n"
    "v_add_f32 v166, v166, v61\n"
    "v_add_f32 v166, v166, v62\n"
    "v_add_f32 v166, v166, v63\n"
    "v_cvt_pk_bf16_f32 v56, v60, v61\n"
    "v_cvt_pk_bf16_f32 v57, v62, v63\n"
    "s_xor_b32 s83, s82, 0x3000\n"
    "s_nop 1\n"
    "s_call_b64 s[70:71], .Lgqa_SQK1\n"
    "v_exp_f32 v60, v60\n"
    "v_exp_f32 v61, v61\n"
    "v_exp_f32 v62, v62\n"
    "v_exp_f32 v63, v63\n"
    "v_add_f32 v166, v166, v60\n"
    "v_add_f32 v166, v166, v61\n"
    "v_add_f32 v166, v166, v62\n"
    "v_add_f32 v166, v166, v63\n"
    "v_cvt_pk_bf16_f32 v58, v60, v61\n"
    "v_cvt_pk_bf16_f32 v59, v62, v63\n"
    "v_mov_b32 v156, v166\n"
    "s_nop 1\n"
    "v_permlane16_swap_b32 v166, v156\n"
    "v_add_f32 v166, v166, v156\n"
    "v_mov_b32 v156, v166\n"
    "s_nop 1\n"
    "v_permlane32_swap_b32 v166, v156\n"
    "v_add_f32 v166, v166, v156\n"
    "v_cmp_lt_f32 vcc, 0x71800000, v175\n"
    "v_mul_f32 v168, v168, v214\n"
    "s_nop 0\n"
    "v_cndmask_b32 v168, v168, v166, vcc\n"
    ".Lgqa_HL01_rs:\n"
    "s_call_b64 s[70:71], .Lgqa_RS1\n"
    ".Lgqa_HL01_ret:\n"
    "s_setpc_b64 s[68:69]\n"
    ".Lgqa_HL11:\n"
    "v_add_f32 v168, v168, v169\n"
    "v_mov_b32 v169, 0\n"
    "v_cmp_lt_f32 vcc, s64, v168\n"
    "s_nop 1\n"
    "s_cbranch_vccz .Lgqa_HL11_ret\n"
    "v_mov_b32 v175, v168\n"
    "v_cmp_lt_f32 vcc, 0x71800000, v175\n"
    "s_nop 1\n"
    "s_cbranch_vccnz .Lgqa_HL11_slow\n"
    "v_frexp_exp_i32_f32 v175, v175\n"
    "v_max_i32 v175, 0, v175\n"
    "v_cvt_f32_i32 v214, v175\n"
    "v_sub_f32 v52, v52, v214\n"
    "v_sub_f32 v53, v53, v214\n"
    "v_sub_f32 v54, v54, v214\n"
    "v_sub_f32 v55, v55, v214\n"
    "v_sub_f32 v48, v48, v214\n"
    "v_sub_f32 v49, v49, v214\n"
    "v_sub_f32 v50, v50, v214\n"
    "v_sub_f32 v51, v51, v214\n"
    "v_sub_f32 v136, v136, v214\n"
    "v_sub_f32 v137, v137, v214\n"
    "v_sub_f32 v138, v138, v214\n"
    "v_sub_f32 v139, v139, v214\n"
    "v_sub_u32 v175, 0, v175\n"
    "v_ldexp_f32 v214, 1.0, v175\n"
    "v_mul_f32 v168, v168, v214\n"
    "v_lshlrev_b32 v156, 16, v112\n"
    "v_and_b32 v157, 0xffff0000, v112\n"
    "v_lshlrev_b32 v158, 16, v113\n"
    "v_and_b32 v159, 0xffff0000, v113\n"
    "v_lshlrev_b32 v160, 16, v114\n"
    "v_and_b32 v161, 0xffff0000, v114\n"
    "v_lshlrev_b32 v162, 16, v115\n"
    "v_and_b32 v163, 0xffff0000, v115\n"
    "v_mul_f32 v156, v156, v214\n"
    "v_mul_f32 v157, v157, v214\n"
    "v_mul_f32 v158, v158, v214\n"
    "v_mul_f32 v159, v159, v214\n"
    "v_mul_f32 v160, v160, v214\n"
    "v_mul_f32 v161, v161, v214\n"
    "v_mul_f32 v162, v162, v214\n"
    "v_mul_f32 v163, v163, v214\n"
    "v_cvt_pk_bf16_f32 v112, v156, v157\n"
    "v_cvt_pk_bf16_f32 v113, v158, v159\n"
    "v_cvt_pk_bf16_f32 v114, v160, v161\n"
    "v_cvt_pk_bf16_f32 v115, v162, v163\n"
    "v_lshlrev_b32 v156, 16, v116\n"
    "v_and_b32 v157, 0xffff0000, v116\n"
    "v_lshlrev_b32 v158, 16, v117\n"
    "v_and_b32 v159, 0xffff0000, v117\n"
    "v_lshlrev_b32 v160, 16, v118\n"
    "v_and_b32 v161, 0xffff0000, v118\n"
    "v_lshlrev_b32 v162, 16, v119\n"
    "v_and_b32 v163, 0xffff0000, v119\n"
    "v_mul_f32 v156, v156, v214\n"
    "v_mul_f32 v157, v157, v214\n"
    "v_mul_f32 v158, v158, v214\n"
    "v_mul_f32 v159, v159, v214\n"
    "v_mul_f32 v160, v160, v214\n"
    "v_mul_f32 v161, v161, v214\n"
    "v_mul_f32 v162, v162, v214\n"
    "v_mul_f32 v163, v163, v214\n"
    "v_cvt_pk_bf16_f32 v116, v156, v157\n"
    "v_cvt_pk_bf16_f32 v117, v158, v159\n"
    "v_cvt_pk_bf16_f32 v118, v160, v161\n"
    "v_cvt_pk_bf16_f32 v119, v162, v163\n"
    "s_branch .Lgqa_HL11_rs\n"
    ".Lgqa_HL11_slow:\n"
    "s_sub_u32 s82, s58, 0x8000\n"
    "s_and_b32 s82, s82, 0x3ffff\n"
    "v_mov_b32 v214, 0\n"
    "s_xor_b32 s83, s82, 0x4000\n"
    "s_nop 1\n"
    "s_call_b64 s[70:71], .Lgqa_SQK1\n"
    "v_max3_f32 v214, v214, v60, v61\n"
    "v_max3_f32 v214, v214, v62, v63\n"
    "s_xor_b32 s83, s82, 0x5000\n"
    "s_nop 1\n"
    "s_call_b64 s[70:71], .Lgqa_SQK1\n"
    "v_max3_f32 v214, v214, v60, v61\n"
    "v_max3_f32 v214, v214, v62, v63\n"
    "s_xor_b32 s83, s82, 0x6000\n"
    "s_nop 1\n"
    "s_call_b64 s[70:71], .Lgqa_SQK1\n"
    "v_max3_f32 v214, v214, v60, v61\n"
    "v_max3_f32 v214, v214, v62, v63\n"
    "s_xor_b32 s83, s82, 0x7000\n"
    "s_nop 1\n"
    "s_call_b64 s[70:71], .Lgqa_SQK1\n"
    "v_max3_f32 v214, v214, v60, v61\n"
    "v_max3_f32 v214, v214, v62, v63\n"
    "v_mov_b32 v156, v214\n"
    "s_nop 1\n"
    "v_permlane16_swap_b32 v214, v156\n"
    "v_max_f32 v214, v214, v156\n"
    "v_mov_b32 v156, v214\n"
    "s_nop 1\n"
    "v_permlane32_swap_b32 v214, v156\n"
    "v_max_f32 v214, v214, v156\n"
    "v_min_f32 v157, 0x71800000, v175\n"
    "v_log_f32 v157, v157\n"
    "s_nop 0\n"
    "v_max_f32 v214, v214, v157\n"
    "v_sub_f32 v52, v52, v214\n"
    "v_sub_f32 v53, v53, v214\n"
    "v_sub_f32 v54, v54, v214\n"
    "v_sub_f32 v55, v55, v214\n"
    "v_sub_f32 v48, v48, v214\n"
    "v_sub_f32 v49, v49, v214\n"
    "v_sub_f32 v50, v50, v214\n"
    "v_sub_f32 v51, v51, v214\n"
    "v_sub_f32 v136, v136, v214\n"
    "v_sub_f32 v137, v137, v214\n"
    "v_sub_f32 v138, v138, v214\n"
    "v_sub_f32 v139, v139, v214\n"
    "v_exp_f32 v214, -v214\n"
    "v_mov_b32 v166, 0\n"
    "s_xor_b32 s83, s82, 0x4000\n"
    "s_nop 1\n"
    "s_call_b64 s[70:71], .Lgqa_SQK1\n"
    "v_exp_f32 v60, v60\n"
    "v_exp_f32 v61, v61\n"
    "v_exp_f32 v62, v62\n"
    "v_exp_f32 v63, v63\n"
    "v_add_f32 v166, v166, v60\n"
    "v_add_f32 v166, v166, v61\n"
    "v_add_f32 v166, v166, v62\n"
    "v_add_f32 v166, v166, v63\n"
    "v_cvt_pk_bf16_f32 v112, v60, v61\n"
    "v_cvt_pk_bf16_f32 v113, v62, v63\n"
    "s_xor_b32 s83, s82, 0x5000\n"
    "s_nop 1\n"
    "s_call_b64 s[70:71], .Lgqa_SQK1\n"
    "v_exp_f32 v60, v60\n"
    "v_exp_f32 v61, v61\n"
    "v_exp_f32 v62, v62\n"
    "v_exp_f32 v63, v63\n"
    "v_add_f32 v166, v166, v60\n"
    "v_add_f32 v166, v166, v61\n"
    "v_add_f32 v166, v166, v62\n"
    "v_add_f32 v166, v166, v63\n"
    "v_cvt_pk_bf16_f32 v114, v60, v61\n"
    "v_cvt_pk_bf16_f32 v115, v62, v63\n"
    "s_xor_b32 s83, s82, 0x6000\n"
    "s_nop 1\n"
    "s_call_b64 s[70:71], .Lgqa_SQK1\n"
    "v_exp_f32 v60, v60\n"
    "v_exp_f32 v61, v61\n"
    "v_exp_f32 v62, v62\n"
    "v_exp_f32 v63, v63\n"
    "v_add_f32 v166, v166, v60\n"
    "v_add_f32 v166, v166, v61\n"
    "v_add_f32 v166, v166, v62\n"
    "v_add_f32 v166, v166, v63\n"
    "v_cvt_pk_bf16_f32 v116, v60, v61\n"
    "v_cvt_pk_bf16_f32 v117, v62, v63\n"
    "s_xor_b32 s83, s82, 0x7000\n"
    "s_nop 1\n"
    "s_call_b64 s[70:71], .Lgqa_SQK1\n"
    "v_exp_f32 v60, v60\n"
    "v_exp_f32 v61, v61\n"
    "v_exp_f32 v62, v62\n"
    "v_exp_f32 v63, v63\n"
    "v_add_f32 v166, v166, v60\n"
    "v_add_f32 v166, v166, v61\n"
    "v_add_f32 v166, v166, v62\n"
    "v_add_f32 v166, v166, v63\n"
    "v_cvt_pk_bf16_f32 v118, v60, v61\n"
    "v_cvt_pk_bf16_f32 v119, v62, v63\n"
    "v_mov_b32 v156, v166\n"
    "s_nop 1\n"
    "v_permlane16_swap_b32 v166, v156\n"
    "v_add_f32 v166, v166, v156\n"
    "v_mov_b32 v156, v166\n"
    "s_nop 1\n"
    "v_permlane32_swap_b32 v166, v156\n"
    "v_add_f32 v166, v166, v156\n"
    "v_cmp_lt_f32 vcc, 0x71800000, v175\n"
    "v_mul_f32 v168, v168, v214\n"
    "s_nop 0\n"
    "v_cndmask_b32 v168, v168, v166, vcc\n"
    ".Lgqa_HL11_rs:\n"
    "s_call_b64 s[70:71], .Lgqa_RS1\n"
    ".Lgqa_HL11_ret:\n"
    "s_setpc_b64 s[68:69]\n"
    ".Lgqa_SQK1:\n"
    "buffer_load_dwordx4 v[156:159], v215, s[40:43], s83 offen offset:0\n"
    "buffer_load_dwordx4 v[160:163], v215, s[40:43], s83 offen offset:64\n"
    "s_waitcnt vmcnt(0)\n"
    "v_mfma_f32_16x16x32_bf16 v[60:63], v[156:159], a[176:179], v[136:139]\n"
    "v_mfma_f32_16x16x32_bf16 v[60:63], v[160:163], a[180:183], v[60:63]\n"
    "s_nop 15\n"
    "buffer_load_dwordx4 v[156:159], v215, s[40:43], s83 offen offset:128\n"
    "buffer_load_dwordx4 v[160:163], v215, s[40:43], s83 offen offset:192\n"
    "s_waitcnt vmcnt(0)\n"
    "v_mfma_f32_16x16x32_bf16 v[60:63], v[156:159], a[184:187], v[60:63]\n"
    "v_mfma_f32_16x16x32_bf16 v[60:63], v[160:163], a[188:191], v[60:63]\n"
    "s_nop 15\n"
    "s_setpc_b64 s[70:71]\n"
    ".Lgqa_HL02:\n"
    "v_cmp_lt_f32 vcc, s64, v172\n"
    "s_nop 1\n"
    "s_cbranch_vccz .Lgqa_HL02_ret\n"
    "v_mov_b32 v175, v172\n"
    "v_mov_b32 v156, v175\n"
    "s_nop 1\n"
    "v_permlane16_swap_b32 v175, v156\n"
    "v_max_f32 v175, v175, v156\n"
    "v_mov_b32 v156, v175\n"
    "s_nop 1\n"
    "v_permlane32_swap_b32 v175, v156\n"
    "v_max_f32 v175, v175, v156\n"
    "v_cmp_lt_f32 vcc, 0x71800000, v175\n"
    "s_nop 1\n"
    "s_cbranch_vccnz .Lgqa_HL02_slow\n"
    "v_frexp_exp_i32_f32 v175, v175\n"
    "v_max_i32 v175, 0, v175\n"
    "v_cvt_f32_i32 v214, v175\n"
    "v_sub_f32 v96, v96, v214\n"
    "v_sub_f32 v97, v97, v214\n"
    "v_sub_f32 v98, v98, v214\n"
    "v_sub_f32 v99, v99, v214\n"
    "v_sub_f32 v20, v20, v214\n"
    "v_sub_f32 v21, v21, v214\n"
    "v_sub_f32 v22, v22, v214\n"
    "v_sub_f32 v23, v23, v214\n"
    "v_sub_f32 v140, v140, v214\n"
    "v_sub_f32 v141, v141, v214\n"
    "v_sub_f32 v142, v142, v214\n"
    "v_sub_f32 v143, v143, v214\n"
    "v_sub_u32 v175, 0, v175\n"
    "v_ldexp_f32 v214, 1.0, v175\n"
    "v_mul_f32 v172, v172, v214\n"
    "v_lshlrev_b32 v156, 16, v16\n"
    "v_and_b32 v157, 0xffff0000, v16\n"
    "v_lshlrev_b32 v158, 16, v17\n"
    "v_and_b32 v159, 0xffff0000, v17\n"
    "v_lshlrev_b32 v160, 16, v18\n"
    "v_and_b32 v161, 0xffff0000, v18\n"
    "v_lshlrev_b32 v162, 16, v19\n"
    "v_and_b32 v163, 0xffff0000, v19\n"
    "v_mul_f32 v156, v156, v214\n"
    "v_mul_f32 v157, v157, v214\n"
    "v_mul_f32 v158, v158, v214\n"
    "v_mul_f32 v159, v159, v214\n"
    "v_mul_f32 v160, v160, v214\n"
    "v_mul_f32 v161, v161, v214\n"
    "v_mul_f32 v162, v162, v214\n"
    "v_mul_f32 v163, v163, v214\n"
    "v_cvt_pk_bf16_f32 v16, v156, v157\n"
    "v_cvt_pk_bf16_f32 v17, v158, v159\n"
    "v_cvt_pk_bf16_f32 v18, v160, v161\n"
    "v_cvt_pk_bf16_f32 v19, v162, v163\n"
    "v_lshlrev_b32 v156, 16, v64\n"
    "v_and_b32 v157, 0xffff0000, v64\n"
    "v_lshlrev_b32 v158, 16, v65\n"
    "v_and_b32 v159, 0xffff0000, v65\n"
    "v_lshlrev_b32 v160, 16, v66\n"
    "v_and_b32 v161, 0xffff0000, v66\n"
    "v_lshlrev_b32 v162, 16, v67\n"
    "v_and_b32 v163, 0xffff0000, v67\n"
    "v_mul_f32 v156, v156, v214\n"
    "v_mul_f32 v157, v157, v214\n"
    "v_mul_f32 v158, v158, v214\n"
    "v_mul_f32 v159, v159, v214\n"
    "v_mul_f32 v160, v160, v214\n"
    "v_mul_f32 v161, v161, v214\n"
    "v_mul_f32 v162, v162, v214\n"
    "v_mul_f32 v163, v163, v214\n"
    "v_cvt_pk_bf16_f32 v64, v156, v157\n"
    "v_cvt_pk_bf16_f32 v65, v158, v159\n"
    "v_cvt_pk_bf16_f32 v66, v160, v161\n"
    "v_cvt_pk_bf16_f32 v67, v162, v163\n"
    "s_branch .Lgqa_HL02_rs\n"
    ".Lgqa_HL02_slow:\n"
    "s_sub_u32 s82, s58, 0x0\n"
    "s_and_b32 s82, s82, 0x3ffff\n"
    "v_mov_b32 v214, 0\n"
    "s_xor_b32 s83, s82, 0x0\n"
    "s_nop 1\n"
    "s_call_b64 s[70:71], .Lgqa_SQK2\n"
    "v_max3_f32 v214, v214, v68, v69\n"
    "v_max3_f32 v214, v214, v70, v71\n"
    "s_xor_b32 s83, s82, 0x1000\n"
    "s_nop 1\n"
    "s_call_b64 s[70:71], .Lgqa_SQK2\n"
    "v_max3_f32 v214, v214, v68, v69\n"
    "v_max3_f32 v214, v214, v70, v71\n"
    "s_xor_b32 s83, s82, 0x2000\n"
    "s_nop 1\n"
    "s_call_b64 s[70:71], .Lgqa_SQK2\n"
    "v_max3_f32 v214, v214, v68, v69\n"
    "v_max3_f32 v214, v214, v70, v71\n"
    "s_xor_b32 s83, s82, 0x3000\n"
    "s_nop 1\n"
    "s_call_b64 s[70:71], .Lgqa_SQK2\n"
    "v_max3_f32 v214, v214, v68, v69\n"
    "v_max3_f32 v214, v214, v70, v71\n"
    "v_mov_b32 v156, v214\n"
    "s_nop 1\n"
    "v_permlane16_swap_b32 v214, v156\n"
    "v_max_f32 v214, v214, v156\n"
    "v_mov_b32 v156, v214\n"
    "s_nop 1\n"
    "v_permlane32_swap_b32 v214, v156\n"
    "v_max_f32 v214, v214, v156\n"
    "v_min_f32 v157, 0x71800000, v175\n"
    "v_log_f32 v157, v157\n"
    "s_nop 0\n"
    "v_max_f32 v214, v214, v157\n"
    "v_sub_f32 v96, v96, v214\n"
    "v_sub_f32 v97, v97, v214\n"
    "v_sub_f32 v98, v98, v214\n"
    "v_sub_f32 v99, v99, v214\n"
    "v_sub_f32 v20, v20, v214\n"
    "v_sub_f32 v21, v21, v214\n"
    "v_sub_f32 v22, v22, v214\n"
    "v_sub_f32 v23, v23, v214\n"
    "v_sub_f32 v140, v140, v214\n"
    "v_sub_f32 v141, v141, v214\n"
    "v_sub_f32 v142, v142, v214\n"
    "v_sub_f32 v143, v143, v214\n"
    "v_exp_f32 v214, -v214\n"
    "v_mov_b32 v166, 0\n"
    "s_xor_b32 s83, s82, 0x0\n"
    "s_nop 1\n"
    "s_call_b64 s[70:71], .Lgqa_SQK2\n"
    "v_exp_f32 v68, v68\n"
    "v_exp_f32 v69, v69\n"
    "v_exp_f32 v70, v70\n"
    "v_exp_f32 v71, v71\n"
    "v_add_f32 v166, v166, v68\n"
    "v_add_f32 v166, v166, v69\n"
    "v_add_f32 v166, v166, v70\n"
    "v_add_f32 v166, v166, v71\n"
    "v_cvt_pk_bf16_f32 v16, v68, v69\n"
    "v_cvt_pk_bf16_f32 v17, v70, v71\n"
    "s_xor_b32 s83, s82, 0x1000\n"
    "s_nop 1\n"
    "s_call_b64 s[70:71], .Lgqa_SQK2\n"
    "v_exp_f32 v68, v68\n"
    "v_exp_f32 v69, v69\n"
    "v_exp_f32 v70, v70\n"
    "v_exp_f32 v71, v71\n"
    "v_add_f32 v166, v166, v68\n"
    "v_add_f32 v166, v166, v69\n"
    "v_add_f32 v166, v166, v70\n"
    "v_add_f32 v166, v166, v71\n"
    "v_cvt_pk_bf16_f32 v18, v68, v69\n"
    "v_cvt_pk_bf16_f32 v19, v70, v71\n"
    "s_xor_b32 s83, s82, 0x2000\n"
    "s_nop 1\n"
    "s_call_b64 s[70:71], .Lgqa_SQK2\n"
    "v_exp_f32 v68, v68\n"
    "v_exp_f32 v69, v69\n"
    "v_exp_f32 v70, v70\n"
    "v_exp_f32 v71, v71\n"
    "v_add_f32 v166, v166, v68\n"
    "v_add_f32 v166, v166, v69\n"
    "v_add_f32 v166, v166, v70\n"
    "v_add_f32 v166, v166, v71\n"
    "v_cvt_pk_bf16_f32 v64, v68, v69\n"
    "v_cvt_pk_bf16_f32 v65, v70, v71\n"
    "s_xor_b32 s83, s82, 0x3000\n"
    "s_nop 1\n"
    "s_call_b64 s[70:71], .Lgqa_SQK2\n"
    "v_exp_f32 v68, v68\n"
    "v_exp_f32 v69, v69\n"
    "v_exp_f32 v70, v70\n"
    "v_exp_f32 v71, v71\n"
    "v_add_f32 v166, v166, v68\n"
    "v_add_f32 v166, v166, v69\n"
    "v_add_f32 v166, v166, v70\n"
    "v_add_f32 v166, v166, v71\n"
    "v_cvt_pk_bf16_f32 v66, v68, v69\n"
    "v_cvt_pk_bf16_f32 v67, v70, v71\n"
    "v_cmp_lt_f32 vcc, 0x71800000, v175\n"
    "v_mul_f32 v172, v172, v214\n"
    "s_nop 0\n"
    "v_cndmask_b32 v172, v172, v166, vcc\n"
    ".Lgqa_HL02_rs:\n"
    "s_call_b64 s[70:71], .Lgqa_RS2\n"
    ".Lgqa_HL02_ret:\n"
    "s_setpc_b64 s[68:69]\n"
    ".Lgqa_HL12:\n"
    "v_cmp_lt_f32 vcc, s64, v172\n"
    "s_nop 1\n"
    "s_cbranch_vccz .Lgqa_HL12_ret\n"
    "v_mov_b32 v175, v172\n"
    "v_mov_b32 v156, v175\n"
    "s_nop 1\n"
    "v_permlane16_swap_b32 v175, v156\n"
    "v_max_f32 v175, v175, v156\n"
    "v_mov_b32 v156, v175\n"
    "s_nop 1\n"
    "v_permlane32_swap_b32 v175, v156\n"
    "v_max_f32 v175, v175, v156\n"
    "v_cmp_lt_f32 vcc, 0x71800000, v175\n"
    "s_nop 1\n"
    "s_cbranch_vccnz .Lgqa_HL12_slow\n"
    "v_frexp_exp_i32_f32 v175, v175\n"
    "v_max_i32 v175, 0, v175\n"
    "v_cvt_f32_i32 v214, v175\n"
    "v_sub_f32 v16, v16, v214\n"
    "v_sub_f32 v17, v17, v214\n"
    "v_sub_f32 v18, v18, v214\n"
    "v_sub_f32 v19, v19, v214\n"
    "v_sub_f32 v20, v20, v214\n"
    "v_sub_f32 v21, v21, v214\n"
    "v_sub_f32 v22, v22, v214\n"
    "v_sub_f32 v23, v23, v214\n"
    "v_sub_f32 v140, v140, v214\n"
    "v_sub_f32 v141, v141, v214\n"
    "v_sub_f32 v142, v142, v214\n"
    "v_sub_f32 v143, v143, v214\n"
    "v_sub_u32 v175, 0, v175\n"
    "v_ldexp_f32 v214, 1.0, v175\n"
    "v_mul_f32 v172, v172, v214\n"
    "v_lshlrev_b32 v156, 16, v96\n"
    "v_and_b32 v157, 0xffff0000, v96\n"
    "v_lshlrev_b32 v158, 16, v97\n"
    "v_and_b32 v159, 0xffff0000, v97\n"
    "v_lshlrev_b32 v160, 16, v98\n"
    "v_and_b32 v161, 0xffff0000, v98\n"
    "v_lshlrev_b32 v162, 16, v99\n"
    "v_and_b32 v163, 0xffff0000, v99\n"
    "v_mul_f32 v156, v156, v214\n"
    "v_mul_f32 v157, v157, v214\n"
    "v_mul_f32 v158, v158, v214\n"
    "v_mul_f32 v159, v159, v214\n"
    "v_mul_f32 v160, v160, v214\n"
    "v_mul_f32 v161, v161, v214\n"
    "v_mul_f32 v162, v162, v214\n"
    "v_mul_f32 v163, v163, v214\n"
    "v_cvt_pk_bf16_f32 v96, v156, v157\n"
    "v_cvt_pk_bf16_f32 v97, v158, v159\n"
    "v_cvt_pk_bf16_f32 v98, v160, v161\n"
    "v_cvt_pk_bf16_f32 v99, v162, v163\n"
    "v_lshlrev_b32 v156, 16, v120\n"
    "v_and_b32 v157, 0xffff0000, v120\n"
    "v_lshlrev_b32 v158, 16, v121\n"
    "v_and_b32 v159, 0xffff0000, v121\n"
    "v_lshlrev_b32 v160, 16, v122\n"
    "v_and_b32 v161, 0xffff0000, v122\n"
    "v_lshlrev_b32 v162, 16, v123\n"
    "v_and_b32 v163, 0xffff0000, v123\n"
    "v_mul_f32 v156, v156, v214\n"
    "v_mul_f32 v157, v157, v214\n"
    "v_mul_f32 v158, v158, v214\n"
    "v_mul_f32 v159, v159, v214\n"
    "v_mul_f32 v160, v160, v214\n"
    "v_mul_f32 v161, v161, v214\n"
    "v_mul_f32 v162, v162, v214\n"
    "v_mul_f32 v163, v163, v214\n"
    "v_cvt_pk_bf16_f32 v120, v156, v157\n"
    "v_cvt_pk_bf16_f32 v121, v158, v159\n"
    "v_cvt_pk_bf16_f32 v122, v160, v161\n"
    "v_cvt_pk_bf16_f32 v123, v162, v163\n"
    "s_branch .Lgqa_HL12_rs\n"
    ".Lgqa_HL12_slow:\n"
    "s_sub_u32 s82, s58, 0x8000\n"
    "s_and_b32 s82, s82, 0x3ffff\n"
    "v_mov_b32 v214, 0\n"
    "s_xor_b32 s83, s82, 0x4000\n"
    "s_nop 1\n"
    "s_call_b64 s[70:71], .Lgqa_SQK2\n"
    "v_max3_f32 v214, v214, v68, v69\n"
    "v_max3_f32 v214, v214, v70, v71\n"
    "s_xor_b32 s83, s82, 0x5000\n"
    "s_nop 1\n"
    "s_call_b64 s[70:71], .Lgqa_SQK2\n"
    "v_max3_f32 v214, v214, v68, v69\n"
    "v_max3_f32 v214, v214, v70, v71\n"
    "s_xor_b32 s83, s82, 0x6000\n"
    "s_nop 1\n"
    "s_call_b64 s[70:71], .Lgqa_SQK2\n"
    "v_max3_f32 v214, v214, v68, v69\n"
    "v_max3_f32 v214, v214, v70, v71\n"
    "s_xor_b32 s83, s82, 0x7000\n"
    "s_nop 1\n"
    "s_call_b64 s[70:71], .Lgqa_SQK2\n"
    "v_max3_f32 v214, v214, v68, v69\n"
    "v_max3_f32 v214, v214, v70, v71\n"
    "v_mov_b32 v156, v214\n"
    "s_nop 1\n"
    "v_permlane16_swap_b32 v214, v156\n"
    "v_max_f32 v214, v214, v156\n"
    "v_mov_b32 v156, v214\n"
    "s_nop 1\n"
    "v_permlane32_swap_b32 v214, v156\n"
    "v_max_f32 v214, v214, v156\n"
    "v_min_f32 v157, 0x71800000, v175\n"
    "v_log_f32 v157, v157\n"
    "s_nop 0\n"
    "v_max_f32 v214, v214, v157\n"
    "v_sub_f32 v16, v16, v214\n"
    "v_sub_f32 v17, v17, v214\n"
    "v_sub_f32 v18, v18, v214\n"
    "v_sub_f32 v19, v19, v214\n"
    "v_sub_f32 v20, v20, v214\n"
    "v_sub_f32 v21, v21, v214\n"
    "v_sub_f32 v22, v22, v214\n"
    "v_sub_f32 v23, v23, v214\n"
    "v_sub_f32 v140, v140, v214\n"
    "v_sub_f32 v141, v141, v214\n"
    "v_sub_f32 v142, v142, v214\n"
    "v_sub_f32 v143, v143, v214\n"
    "v_exp_f32 v214, -v214\n"
    "v_mov_b32 v166, 0\n"
    "s_xor_b32 s83, s82, 0x4000\n"
    "s_nop 1\n"
    "s_call_b64 s[70:71], .Lgqa_SQK2\n"
    "v_exp_f32 v68, v68\n"
    "v_exp_f32 v69, v69\n"
    "v_exp_f32 v70, v70\n"
    "v_exp_f32 v71, v71\n"
    "v_add_f32 v166, v166, v68\n"
    "v_add_f32 v166, v166, v69\n"
    "v_add_f32 v166, v166, v70\n"
    "v_add_f32 v166, v166, v71\n"
    "v_cvt_pk_bf16_f32 v96, v68, v69\n"
    "v_cvt_pk_bf16_f32 v97, v70, v71\n"
    "s_xor_b32 s83, s82, 0x5000\n"
    "s_nop 1\n"
    "s_call_b64 s[70:71], .Lgqa_SQK2\n"
    "v_exp_f32 v68, v68\n"
    "v_exp_f32 v69, v69\n"
    "v_exp_f32 v70, v70\n"
    "v_exp_f32 v71, v71\n"
    "v_add_f32 v166, v166, v68\n"
    "v_add_f32 v166, v166, v69\n"
    "v_add_f32 v166, v166, v70\n"
    "v_add_f32 v166, v166, v71\n"
    "v_cvt_pk_bf16_f32 v98, v68, v69\n"
    "v_cvt_pk_bf16_f32 v99, v70, v71\n"
    "s_xor_b32 s83, s82, 0x6000\n"
    "s_nop 1\n"
    "s_call_b64 s[70:71], .Lgqa_SQK2\n"
    "v_exp_f32 v68, v68\n"
    "v_exp_f32 v69, v69\n"
    "v_exp_f32 v70, v70\n"
    "v_exp_f32 v71, v71\n"
    "v_add_f32 v166, v166, v68\n"
    "v_add_f32 v166, v166, v69\n"
    "v_add_f32 v166, v166, v70\n"
    "v_add_f32 v166, v166, v71\n"
    "v_cvt_pk_bf16_f32 v120, v68, v69\n"
    "v_cvt_pk_bf16_f32 v121, v70, v71\n"
    "s_xor_b32 s83, s82, 0x7000\n"
    "s_nop 1\n"
    "s_call_b64 s[70:71], .Lgqa_SQK2\n"
    "v_exp_f32 v68, v68\n"
    "v_exp_f32 v69, v69\n"
    "v_exp_f32 v70, v70\n"
    "v_exp_f32 v71, v71\n"
    "v_add_f32 v166, v166, v68\n"
    "v_add_f32 v166, v166, v69\n"
    "v_add_f32 v166, v166, v70\n"
    "v_add_f32 v166, v166, v71\n"
    "v_cvt_pk_bf16_f32 v122, v68, v69\n"
    "v_cvt_pk_bf16_f32 v123, v70, v71\n"
    "v_cmp_lt_f32 vcc, 0x71800000, v175\n"
    "v_mul_f32 v172, v172, v214\n"
    "s_nop 0\n"
    "v_cndmask_b32 v172, v172, v166, vcc\n"
    ".Lgqa_HL12_rs:\n"
    "s_call_b64 s[70:71], .Lgqa_RS2\n"
    ".Lgqa_HL12_ret:\n"
    "s_setpc_b64 s[68:69]\n"
    ".Lgqa_SQK2:\n"
    "buffer_load_dwordx4 v[156:159], v215, s[40:43], s83 offen offset:0\n"
    "buffer_load_dwordx4 v[160:163], v215, s[40:43], s83 offen offset:64\n"
    "s_waitcnt vmcnt(0)\n"
    "v_mfma_f32_16x16x32_bf16 v[68:71], v[156:159], a[192:195], v[140:143]\n"
    "v_mfma_f32_16x16x32_bf16 v[68:71], v[160:163], a[196:199], v[68:71]\n"
    "s_nop 15\n"
    "buffer_load_dwordx4 v[156:159], v215, s[40:43], s83 offen offset:128\n"
    "buffer_load_dwordx4 v[160:163], v215, s[40:43], s83 offen offset:192\n"
    "s_waitcnt vmcnt(0)\n"
    "v_mfma_f32_16x16x32_bf16 v[68:71], v[156:159], a[200:203], v[68:71]\n"
    "v_mfma_f32_16x16x32_bf16 v[68:71], v[160:163], a[204:207], v[68:71]\n"
    "s_nop 15\n"
    "s_setpc_b64 s[70:71]\n"
    ".Lgqa_HL03:\n"
    "v_cmp_lt_f32 vcc, s64, v173\n"
    "s_nop 1\n"
    "s_cbranch_vccz .Lgqa_HL03_ret\n"
    "v_mov_b32 v175, v173\n"
    "v_mov_b32 v156, v175\n"
    "s_nop 1\n"
    "v_permlane16_swap_b32 v175, v156\n"
    "v_max_f32 v175, v175, v156\n"
    "v_mov_b32 v156, v175\n"
    "s_nop 1\n"
    "v_permlane32_swap_b32 v175, v156\n"
    "v_max_f32 v175, v175, v156\n"
    "v_cmp_lt_f32 vcc, 0x71800000, v175\n"
    "s_nop 1\n"
    "s_cbranch_vccnz .Lgqa_HL03_slow\n"
    "v_frexp_exp_i32_f32 v175, v175\n"
    "v_max_i32 v175, 0, v175\n"
    "v_cvt_f32_i32 v214, v175\n"
    "v_sub_f32 v100, v100, v214\n"
    "v_sub_f32 v101, v101, v214\n"
    "v_sub_f32 v102, v102, v214\n"
    "v_sub_f32 v103, v103, v214\n"
    "v_sub_f32 v28, v28, v214\n"
    "v_sub_f32 v29, v29, v214\n"
    "v_sub_f32 v30, v30, v214\n"
    "v_sub_f32 v31, v31, v214\n"
    "v_sub_f32 v144, v144, v214\n"
    "v_sub_f32 v145, v145, v214\n"
    "v_sub_f32 v146, v146, v214\n"
    "v_sub_f32 v147, v147, v214\n"
    "v_sub_u32 v175, 0, v175\n"
    "v_ldexp_f32 v214, 1.0, v175\n"
    "v_mul_f32 v173, v173, v214\n"
    "v_lshlrev_b32 v156, 16, v24\n"
    "v_and_b32 v157, 0xffff0000, v24\n"
    "v_lshlrev_b32 v158, 16, v25\n"
    "v_and_b32 v159, 0xffff0000, v25\n"
    "v_lshlrev_b32 v160, 16, v26\n"
    "v_and_b32 v161, 0xffff0000, v26\n"
    "v_lshlrev_b32 v162, 16, v27\n"
    "v_and_b32 v163, 0xffff0000, v27\n"
    "v_mul_f32 v156, v156, v214\n"
    "v_mul_f32 v157, v157, v214\n"
    "v_mul_f32 v158, v158, v214\n"
    "v_mul_f32 v159, v159, v214\n"
    "v_mul_f32 v160, v160, v214\n"
    "v_mul_f32 v161, v161, v214\n"
    "v_mul_f32 v162, v162, v214\n"
    "v_mul_f32 v163, v163, v214\n"
    "v_cvt_pk_bf16_f32 v24, v156, v157\n"
    "v_cvt_pk_bf16_f32 v25, v158, v159\n"
    "v_cvt_pk_bf16_f32 v26, v160, v161\n"
    "v_cvt_pk_bf16_f32 v27, v162, v163\n"
    "v_lshlrev_b32 v156, 16, v72\n"
    "v_and_b32 v157, 0xffff0000, v72\n"
    "v_lshlrev_b32 v158, 16, v73\n"
    "v_and_b32 v159, 0xffff0000, v73\n"
    "v_lshlrev_b32 v160, 16, v74\n"
    "v_and_b32 v161, 0xffff0000, v74\n"
    "v_lshlrev_b32 v162, 16, v75\n"
    "v_and_b32 v163, 0xffff0000, v75\n"
    "v_mul_f32 v156, v156, v214\n"
    "v_mul_f32 v157, v157, v214\n"
    "v_mul_f32 v158, v158, v214\n"
    "v_mul_f32 v159, v159, v214\n"
    "v_mul_f32 v160, v160, v214\n"
    "v_mul_f32 v161, v161, v214\n"
    "v_mul_f32 v162, v162, v214\n"
    "v_mul_f32 v163, v163, v214\n"
    "v_cvt_pk_bf16_f32 v72, v156, v157\n"
    "v_cvt_pk_bf16_f32 v73, v158, v159\n"
    "v_cvt_pk_bf16_f32 v74, v160, v161\n"
    "v_cvt_pk_bf16_f32 v75, v162, v163\n"
    "s_branch .Lgqa_HL03_rs\n"
    ".Lgqa_HL03_slow:\n"
    "s_sub_u32 s82, s58, 0x0\n"
    "s_and_b32 s82, s82, 0x3ffff\n"
    "v_mov_b32 v214, 0\n"
    "s_xor_b32 s83, s82, 0x0\n"
    "s_nop 1\n"
    "s_call_b64 s[70:71], .Lgqa_SQK3\n"
    "v_max3_f32 v214, v214, v76, v77\n"
    "v_max3_f32 v214, v214, v78, v79\n"
    "s_xor_b32 s83, s82, 0x1000\n"
    "s_nop 1\n"
    "s_call_b64 s[70:71], .Lgqa_SQK3\n"
    "v_max3_f32 v214, v214, v76, v77\n"
    "v_max3_f32 v214, v214, v78, v79\n"
    "s_xor_b32 s83, s82, 0x2000\n"
    "s_nop 1\n"
    "s_call_b64 s[70:71], .Lgqa_SQK3\n"
    "v_max3_f32 v214, v214, v76, v77\n"
    "v_max3_f32 v214, v214, v78, v79\n"
    "s_xor_b32 s83, s82, 0x3000\n"
    "s_nop 1\n"
    "s_call_b64 s[70:71], .Lgqa_SQK3\n"
    "v_max3_f32 v214, v214, v76, v77\n"
    "v_max3_f32 v214, v214, v78, v79\n"
    "v_mov_b32 v156, v214\n"
    "s_nop 1\n"
    "v_permlane16_swap_b32 v214, v156\n"
    "v_max_f32 v214, v214, v156\n"
    "v_mov_b32 v156, v214\n"
    "s_nop 1\n"
    "v_permlane32_swap_b32 v214, v156\n"
    "v_max_f32 v214, v214, v156\n"
    "v_min_f32 v157, 0x71800000, v175\n"
    "v_log_f32 v157, v157\n"
    "s_nop 0\n"
    "v_max_f32 v214, v214, v157\n"
    "v_sub_f32 v100, v100, v214\n"
    "v_sub_f32 v101, v101, v214\n"
    "v_sub_f32 v102, v102, v214\n"
    "v_sub_f32 v103, v103, v214\n"
    "v_sub_f32 v28, v28, v214\n"
    "v_sub_f32 v29, v29, v214\n"
    "v_sub_f32 v30, v30, v214\n"
    "v_sub_f32 v31, v31, v214\n"
    "v_sub_f32 v144, v144, v214\n"
    "v_sub_f32 v145, v145, v214\n"
    "v_sub_f32 v146, v146, v214\n"
    "v_sub_f32 v147, v147, v214\n"
    "v_exp_f32 v214, -v214\n"
    "v_mov_b32 v166, 0\n"
    "s_xor_b32 s83, s82, 0x0\n"
    "s_nop 1\n"
    "s_call_b64 s[70:71], .Lgqa_SQK3\n"
    "v_exp_f32 v76, v76\n"
    "v_exp_f32 v77, v77\n"
    "v_exp_f32 v78, v78\n"
    "v_exp_f32 v79, v79\n"
    "v_add_f32 v166, v166, v76\n"
    "v_add_f32 v166, v166, v77\n"
    "v_add_f32 v166, v166, v78\n"
    "v_add_f32 v166, v166, v79\n"
    "v_cvt_pk_bf16_f32 v24, v76, v77\n"
    "v_cvt_pk_bf16_f32 v25, v78, v79\n"
    "s_xor_b32 s83, s82, 0x1000\n"
    "s_nop 1\n"
    "s_call_b64 s[70:71], .Lgqa_SQK3\n"
    "v_exp_f32 v76, v76\n"
    "v_exp_f32 v77, v77\n"
    "v_exp_f32 v78, v78\n"
    "v_exp_f32 v79, v79\n"
    "v_add_f32 v166, v166, v76\n"
    "v_add_f32 v166, v166, v77\n"
    "v_add_f32 v166, v166, v78\n"
    "v_add_f32 v166, v166, v79\n"
    "v_cvt_pk_bf16_f32 v26, v76, v77\n"
    "v_cvt_pk_bf16_f32 v27, v78, v79\n"
    "s_xor_b32 s83, s82, 0x2000\n"
    "s_nop 1\n"
    "s_call_b64 s[70:71], .Lgqa_SQK3\n"
    "v_exp_f32 v76, v76\n"
    "v_exp_f32 v77, v77\n"
    "v_exp_f32 v78, v78\n"
    "v_exp_f32 v79, v79\n"
    "v_add_f32 v166, v166, v76\n"
    "v_add_f32 v166, v166, v77\n"
    "v_add_f32 v166, v166, v78\n"
    "v_add_f32 v166, v166, v79\n"
    "v_cvt_pk_bf16_f32 v72, v76, v77\n"
    "v_cvt_pk_bf16_f32 v73, v78, v79\n"
    "s_xor_b32 s83, s82, 0x3000\n"
    "s_nop 1\n"
    "s_call_b64 s[70:71], .Lgqa_SQK3\n"
    "v_exp_f32 v76, v76\n"
    "v_exp_f32 v77, v77\n"
    "v_exp_f32 v78, v78\n"
    "v_exp_f32 v79, v79\n"
    "v_add_f32 v166, v166, v76\n"
    "v_add_f32 v166, v166, v77\n"
    "v_add_f32 v166, v166, v78\n"
    "v_add_f32 v166, v166, v79\n"
    "v_cvt_pk_bf16_f32 v74, v76, v77\n"
    "v_cvt_pk_bf16_f32 v75, v78, v79\n"
    "v_cmp_lt_f32 vcc, 0x71800000, v175\n"
    "v_mul_f32 v173, v173, v214\n"
    "s_nop 0\n"
    "v_cndmask_b32 v173, v173, v166, vcc\n"
    ".Lgqa_HL03_rs:\n"
    "s_call_b64 s[70:71], .Lgqa_RS3\n"
    ".Lgqa_HL03_ret:\n"
    "s_setpc_b64 s[68:69]\n"
    ".Lgqa_HL13:\n"
    "v_cmp_lt_f32 vcc, s64, v173\n"
    "s_nop 1\n"
    "s_cbranch_vccz .Lgqa_HL13_ret\n"
    "v_mov_b32 v175, v173\n"
    "v_mov_b32 v156, v175\n"
    "s_nop 1\n"
    "v_permlane16_swap_b32 v175, v156\n"
    "v_max_f32 v175, v175, v156\n"
    "v_mov_b32 v156, v175\n"
    "s_nop 1\n"
    "v_permlane32_swap_b32 v175, v156\n"
    "v_max_f32 v175, v175, v156\n"
    "v_cmp_lt_f32 vcc, 0x71800000, v175\n"
    "s_nop 1\n"
    "s_cbranch_vccnz .Lgqa_HL13_slow\n"
    "v_frexp_exp_i32_f32 v175, v175\n"
    "v_max_i32 v175, 0, v175\n"
    "v_cvt_f32_i32 v214, v175\n"
    "v_sub_f32 v24, v24, v214\n"
    "v_sub_f32 v25, v25, v214\n"
    "v_sub_f32 v26, v26, v214\n"
    "v_sub_f32 v27, v27, v214\n"
    "v_sub_f32 v28, v28, v214\n"
    "v_sub_f32 v29, v29, v214\n"
    "v_sub_f32 v30, v30, v214\n"
    "v_sub_f32 v31, v31, v214\n"
    "v_sub_f32 v144, v144, v214\n"
    "v_sub_f32 v145, v145, v214\n"
    "v_sub_f32 v146, v146, v214\n"
    "v_sub_f32 v147, v147, v214\n"
    "v_sub_u32 v175, 0, v175\n"
    "v_ldexp_f32 v214, 1.0, v175\n"
    "v_mul_f32 v173, v173, v214\n"
    "v_lshlrev_b32 v156, 16, v100\n"
    "v_and_b32 v157, 0xffff0000, v100\n"
    "v_lshlrev_b32 v158, 16, v101\n"
    "v_and_b32 v159, 0xffff0000, v101\n"
    "v_lshlrev_b32 v160, 16, v102\n"
    "v_and_b32 v161, 0xffff0000, v102\n"
    "v_lshlrev_b32 v162, 16, v103\n"
    "v_and_b32 v163, 0xffff0000, v103\n"
    "v_mul_f32 v156, v156, v214\n"
    "v_mul_f32 v157, v157, v214\n"
    "v_mul_f32 v158, v158, v214\n"
    "v_mul_f32 v159, v159, v214\n"
    "v_mul_f32 v160, v160, v214\n"
    "v_mul_f32 v161, v161, v214\n"
    "v_mul_f32 v162, v162, v214\n"
    "v_mul_f32 v163, v163, v214\n"
    "v_cvt_pk_bf16_f32 v100, v156, v157\n"
    "v_cvt_pk_bf16_f32 v101, v158, v159\n"
    "v_cvt_pk_bf16_f32 v102, v160, v161\n"
    "v_cvt_pk_bf16_f32 v103, v162, v163\n"
    "v_lshlrev_b32 v156, 16, v124\n"
    "v_and_b32 v157, 0xffff0000, v124\n"
    "v_lshlrev_b32 v158, 16, v125\n"
    "v_and_b32 v159, 0xffff0000, v125\n"
    "v_lshlrev_b32 v160, 16, v126\n"
    "v_and_b32 v161, 0xffff0000, v126\n"
    "v_lshlrev_b32 v162, 16, v127\n"
    "v_and_b32 v163, 0xffff0000, v127\n"
    "v_mul_f32 v156, v156, v214\n"
    "v_mul_f32 v157, v157, v214\n"
    "v_mul_f32 v158, v158, v214\n"
    "v_mul_f32 v159, v159, v214\n"
    "v_mul_f32 v160, v160, v214\n"
    "v_mul_f32 v161, v161, v214\n"
    "v_mul_f32 v162, v162, v214\n"
    "v_mul_f32 v163, v163, v214\n"
    "v_cvt_pk_bf16_f32 v124, v156, v157\n"
    "v_cvt_pk_bf16_f32 v125, v158, v159\n"
    "v_cvt_pk_bf16_f32 v126, v160, v161\n"
    "v_cvt_pk_bf16_f32 v127, v162, v163\n"
    "s_branch .Lgqa_HL13_rs\n"
    ".Lgqa_HL13_slow:\n"
    "s_sub_u32 s82, s58, 0x8000\n"
    "s_and_b32 s82, s82, 0x3ffff\n"
    "v_mov_b32 v214, 0\n"
    "s_xor_b32 s83, s82, 0x4000\n"
    "s_nop 1\n"
    "s_call_b64 s[70:71], .Lgqa_SQK3\n"
    "v_max3_f32 v214, v214, v76, v77\n"
    "v_max3_f32 v214, v214, v78, v79\n"
    "s_xor_b32 s83, s82, 0x5000\n"
    "s_nop 1\n"
    "s_call_b64 s[70:71], .Lgqa_SQK3\n"
    "v_max3_f32 v214, v214, v76, v77\n"
    "v_max3_f32 v214, v214, v78, v79\n"
    "s_xor_b32 s83, s82, 0x6000\n"
    "s_nop 1\n"
    "s_call_b64 s[70:71], .Lgqa_SQK3\n"
    "v_max3_f32 v214, v214, v76, v77\n"
    "v_max3_f32 v214, v214, v78, v79\n"
    "s_xor_b32 s83, s82, 0x7000\n"
    "s_nop 1\n"
    "s_call_b64 s[70:71], .Lgqa_SQK3\n"
    "v_max3_f32 v214, v214, v76, v77\n"
    "v_max3_f32 v214, v214, v78, v79\n"
    "v_mov_b32 v156, v214\n"
    "s_nop 1\n"
    "v_permlane16_swap_b32 v214, v156\n"
    "v_max_f32 v214, v214, v156\n"
    "v_mov_b32 v156, v214\n"
    "s_nop 1\n"
    "v_permlane32_swap_b32 v214, v156\n"
    "v_max_f32 v214, v214, v156\n"
    "v_min_f32 v157, 0x71800000, v175\n"
    "v_log_f32 v157, v157\n"
    "s_nop 0\n"
    "v_max_f32 v214, v214, v157\n"
    "v_sub_f32 v24, v24, v214\n"
    "v_sub_f32 v25, v25, v214\n"
    "v_sub_f32 v26, v26, v214\n"
    "v_sub_f32 v27, v27, v214\n"
    "v_sub_f32 v28, v28, v214\n"
    "v_sub_f32 v29, v29, v214\n"
    "v_sub_f32 v30, v30, v214\n"
    "v_sub_f32 v31, v31, v214\n"
    "v_sub_f32 v144, v144, v214\n"
    "v_sub_f32 v145, v145, v214\n"
    "v_sub_f32 v146, v146, v214\n"
    "v_sub_f32 v147, v147, v214\n"
    "v_exp_f32 v214, -v214\n"
    "v_mov_b32 v166, 0\n"
    "s_xor_b32 s83, s82, 0x4000\n"
    "s_nop 1\n"
    "s_call_b64 s[70:71], .Lgqa_SQK3\n"
    "v_exp_f32 v76, v76\n"
    "v_exp_f32 v77, v77\n"
    "v_exp_f32 v78, v78\n"
    "v_exp_f32 v79, v79\n"
    "v_add_f32 v166, v166, v76\n"
    "v_add_f32 v166, v166, v77\n"
    "v_add_f32 v166, v166, v78\n"
    "v_add_f32 v166, v166, v79\n"
    "v_cvt_pk_bf16_f32 v100, v76, v77\n"
    "v_cvt_pk_bf16_f32 v101, v78, v79\n"
    "s_xor_b32 s83, s82, 0x5000\n"
    "s_nop 1\n"
    "s_call_b64 s[70:71], .Lgqa_SQK3\n"
    "v_exp_f32 v76, v76\n"
    "v_exp_f32 v77, v77\n"
    "v_exp_f32 v78, v78\n"
    "v_exp_f32 v79, v79\n"
    "v_add_f32 v166, v166, v76\n"
    "v_add_f32 v166, v166, v77\n"
    "v_add_f32 v166, v166, v78\n"
    "v_add_f32 v166, v166, v79\n"
    "v_cvt_pk_bf16_f32 v102, v76, v77\n"
    "v_cvt_pk_bf16_f32 v103, v78, v79\n"
    "s_xor_b32 s83, s82, 0x6000\n"
    "s_nop 1\n"
    "s_call_b64 s[70:71], .Lgqa_SQK3\n"
    "v_exp_f32 v76, v76\n"
    "v_exp_f32 v77, v77\n"
    "v_exp_f32 v78, v78\n"
    "v_exp_f32 v79, v79\n"
    "v_add_f32 v166, v166, v76\n"
    "v_add_f32 v166, v166, v77\n"
    "v_add_f32 v166, v166, v78\n"
    "v_add_f32 v166, v166, v79\n"
    "v_cvt_pk_bf16_f32 v124, v76, v77\n"
    "v_cvt_pk_bf16_f32 v125, v78, v79\n"
    "s_xor_b32 s83, s82, 0x7000\n"
    "s_nop 1\n"
    "s_call_b64 s[70:71], .Lgqa_SQK3\n"
    "v_exp_f32 v76, v76\n"
    "v_exp_f32 v77, v77\n"
    "v_exp_f32 v78, v78\n"
    "v_exp_f32 v79, v79\n"
    "v_add_f32 v166, v166, v76\n"
    "v_add_f32 v166, v166, v77\n"
    "v_add_f32 v166, v166, v78\n"
    "v_add_f32 v166, v166, v79\n"
    "v_cvt_pk_bf16_f32 v126, v76, v77\n"
    "v_cvt_pk_bf16_f32 v127, v78, v79\n"
    "v_cmp_lt_f32 vcc, 0x71800000, v175\n"
    "v_mul_f32 v173, v173, v214\n"
    "s_nop 0\n"
    "v_cndmask_b32 v173, v173, v166, vcc\n"
    ".Lgqa_HL13_rs:\n"
    "s_call_b64 s[70:71], .Lgqa_RS3\n"
    ".Lgqa_HL13_ret:\n"
    "s_setpc_b64 s[68:69]\n"
    ".Lgqa_SQK3:\n"
    "buffer_load_dwordx4 v[156:159], v215, s[40:43], s83 offen offset:0\n"
    "buffer_load_dwordx4 v[160:163], v215, s[40:43], s83 offen offset:64\n"
    "s_waitcnt vmcnt(0)\n"
    "v_mfma_f32_16x16x32_bf16 v[76:79], v[156:159], a[208:211], v[144:147]\n"
    "v_mfma_f32_16x16x32_bf16 v[76:79], v[160:163], a[212:215], v[76:79]\n"
    "s_nop 15\n"
    "buffer_load_dwordx4 v[156:159], v215, s[40:43], s83 offen offset:128\n"
    "buffer_load_dwordx4 v[160:163], v215, s[40:43], s83 offen offset:192\n"
    "s_waitcnt vmcnt(0)\n"
    "v_mfma_f32_16x16x32_bf16 v[76:79], v[156:159], a[216:219], v[76:79]\n"
    "v_mfma_f32_16x16x32_bf16 v[76:79], v[160:163], a[220:223], v[76:79]\n"
    "s_nop 15\n"
    "s_setpc_b64 s[70:71]\n"
    ".Lgqa_HL04:\n"
    "v_cmp_lt_f32 vcc, s64, v174\n"
    "s_nop 1\n"
    "s_cbranch_vccz .Lgqa_HL04_ret\n"
    "v_mov_b32 v175, v174\n"
    "v_mov_b32 v156, v175\n"
    "s_nop 1\n"
    "v_permlane16_swap_b32 v175, v156\n"
    "v_max_f32 v175, v175, v156\n"
    "v_mov_b32 v156, v175\n"
    "s_nop 1\n"
    "v_permlane32_swap_b32 v175, v156\n"
    "v_max_f32 v175, v175, v156\n"
    "v_cmp_lt_f32 vcc, 0x71800000, v175\n"
    "s_nop 1\n"
    "s_cbranch_vccnz .Lgqa_HL04_slow\n"
    "v_frexp_exp_i32_f32 v175, v175\n"
    "v_max_i32 v175, 0, v175\n"
    "v_cvt_f32_i32 v214, v175\n"
    "v_sub_f32 v104, v104, v214\n"
    "v_sub_f32 v105, v105, v214\n"
    "v_sub_f32 v106, v106, v214\n"
    "v_sub_f32 v107, v107, v214\n"
    "v_sub_f32 v36, v36, v214\n"
    "v_sub_f32 v37, v37, v214\n"
    "v_sub_f32 v38, v38, v214\n"
    "v_sub_f32 v39, v39, v214\n"
    "v_sub_f32 v148, v148, v214\n"
    "v_sub_f32 v149, v149, v214\n"
    "v_sub_f32 v150, v150, v214\n"
    "v_sub_f32 v151, v151, v214\n"
    "v_sub_u32 v175, 0, v175\n"
    "v_ldexp_f32 v214, 1.0, v175\n"
    "v_mul_f32 v174, v174, v214\n"
    "v_lshlrev_b32 v156, 16, v32\n"
    "v_and_b32 v157, 0xffff0000, v32\n"
    "v_lshlrev_b32 v158, 16, v33\n"
    "v_and_b32 v159, 0xffff0000, v33\n"
    "v_lshlrev_b32 v160, 16, v34\n"
    "v_and_b32 v161, 0xffff0000, v34\n"
    "v_lshlrev_b32 v162, 16, v35\n"
    "v_and_b32 v163, 0xffff0000, v35\n"
    "v_mul_f32 v156, v156, v214\n"
    "v_mul_f32 v157, v157, v214\n"
    "v_mul_f32 v158, v158, v214\n"
    "v_mul_f32 v159, v159, v214\n"
    "v_mul_f32 v160, v160, v214\n"
    "v_mul_f32 v161, v161, v214\n"
    "v_mul_f32 v162, v162, v214\n"
    "v_mul_f32 v163, v163, v214\n"
    "v_cvt_pk_bf16_f32 v32, v156, v157\n"
    "v_cvt_pk_bf16_f32 v33, v158, v159\n"
    "v_cvt_pk_bf16_f32 v34, v160, v161\n"
    "v_cvt_pk_bf16_f32 v35, v162, v163\n"
    "v_lshlrev_b32 v156, 16, v80\n"
    "v_and_b32 v157, 0xffff0000, v80\n"
    "v_lshlrev_b32 v158, 16, v81\n"
    "v_and_b32 v159, 0xffff0000, v81\n"
    "v_lshlrev_b32 v160, 16, v82\n"
    "v_and_b32 v161, 0xffff0000, v82\n"
    "v_lshlrev_b32 v162, 16, v83\n"
    "v_and_b32 v163, 0xffff0000, v83\n"
    "v_mul_f32 v156, v156, v214\n"
    "v_mul_f32 v157, v157, v214\n"
    "v_mul_f32 v158, v158, v214\n"
    "v_mul_f32 v159, v159, v214\n"
    "v_mul_f32 v160, v160, v214\n"
    "v_mul_f32 v161, v161, v214\n"
    "v_mul_f32 v162, v162, v214\n"
    "v_mul_f32 v163, v163, v214\n"
    "v_cvt_pk_bf16_f32 v80, v156, v157\n"
    "v_cvt_pk_bf16_f32 v81, v158, v159\n"
    "v_cvt_pk_bf16_f32 v82, v160, v161\n"
    "v_cvt_pk_bf16_f32 v83, v162, v163\n"
    "s_branch .Lgqa_HL04_rs\n"
    ".Lgqa_HL04_slow:\n"
    "s_sub_u32 s82, s58, 0x0\n"
    "s_and_b32 s82, s82, 0x3ffff\n"
    "v_mov_b32 v214, 0\n"
    "s_xor_b32 s83, s82, 0x0\n"
    "s_nop 1\n"
    "s_call_b64 s[70:71], .Lgqa_SQK4\n"
    "v_max3_f32 v214, v214, v84, v85\n"
    "v_max3_f32 v214, v214, v86, v87\n"
    "s_xor_b32 s83, s82, 0x1000\n"
    "s_nop 1\n"
    "s_call_b64 s[70:71], .Lgqa_SQK4\n"
    "v_max3_f32 v214, v214, v84, v85\n"
    "v_max3_f32 v214, v214, v86, v87\n"
    "s_xor_b32 s83, s82, 0x2000\n"
    "s_nop 1\n"
    "s_call_b64 s[70:71], .Lgqa_SQK4\n"
    "v_max3_f32 v214, v214, v84, v85\n"
    "v_max3_f32 v214, v214, v86, v87\n"
    "s_xor_b32 s83, s82, 0x3000\n"
    "s_nop 1\n"
    "s_call_b64 s[70:71], .Lgqa_SQK4\n"
    "v_max3_f32 v214, v214, v84, v85\n"
    "v_max3_f32 v214, v214, v86, v87\n"
    "v_mov_b32 v156, v214\n"
    "s_nop 1\n"
    "v_permlane16_swap_b32 v214, v156\n"
    "v_max_f32 v214, v214, v156\n"
    "v_mov_b32 v156, v214\n"
    "s_nop 1\n"
    "v_permlane32_swap_b32 v214, v156\n"
    "v_max_f32 v214, v214, v156\n"
    "v_min_f32 v157, 0x71800000, v175\n"
    "v_log_f32 v157, v157\n"
    "s_nop 0\n"
    "v_max_f32 v214, v214, v157\n"
    "v_sub_f32 v104, v104, v214\n"
    "v_sub_f32 v105, v105, v214\n"
    "v_sub_f32 v106, v106, v214\n"
    "v_sub_f32 v107, v107, v214\n"
    "v_sub_f32 v36, v36, v214\n"
    "v_sub_f32 v37, v37, v214\n"
    "v_sub_f32 v38, v38, v214\n"
    "v_sub_f32 v39, v39, v214\n"
    "v_sub_f32 v148, v148, v214\n"
    "v_sub_f32 v149, v149, v214\n"
    "v_sub_f32 v150, v150, v214\n"
    "v_sub_f32 v151, v151, v214\n"
    "v_exp_f32 v214, -v214\n"
    "v_mov_b32 v166, 0\n"
    "s_xor_b32 s83, s82, 0x0\n"
    "s_nop 1\n"
    "s_call_b64 s[70:71], .Lgqa_SQK4\n"
    "v_exp_f32 v84, v84\n"
    "v_exp_f32 v85, v85\n"
    "v_exp_f32 v86, v86\n"
    "v_exp_f32 v87, v87\n"
    "v_add_f32 v166, v166, v84\n"
    "v_add_f32 v166, v166, v85\n"
    "v_add_f32 v166, v166, v86\n"
    "v_add_f32 v166, v166, v87\n"
    "v_cvt_pk_bf16_f32 v32, v84, v85\n"
    "v_cvt_pk_bf16_f32 v33, v86, v87\n"
    "s_xor_b32 s83, s82, 0x1000\n"
    "s_nop 1\n"
    "s_call_b64 s[70:71], .Lgqa_SQK4\n"
    "v_exp_f32 v84, v84\n"
    "v_exp_f32 v85, v85\n"
    "v_exp_f32 v86, v86\n"
    "v_exp_f32 v87, v87\n"
    "v_add_f32 v166, v166, v84\n"
    "v_add_f32 v166, v166, v85\n"
    "v_add_f32 v166, v166, v86\n"
    "v_add_f32 v166, v166, v87\n"
    "v_cvt_pk_bf16_f32 v34, v84, v85\n"
    "v_cvt_pk_bf16_f32 v35, v86, v87\n"
    "s_xor_b32 s83, s82, 0x2000\n"
    "s_nop 1\n"
    "s_call_b64 s[70:71], .Lgqa_SQK4\n"
    "v_exp_f32 v84, v84\n"
    "v_exp_f32 v85, v85\n"
    "v_exp_f32 v86, v86\n"
    "v_exp_f32 v87, v87\n"
    "v_add_f32 v166, v166, v84\n"
    "v_add_f32 v166, v166, v85\n"
    "v_add_f32 v166, v166, v86\n"
    "v_add_f32 v166, v166, v87\n"
    "v_cvt_pk_bf16_f32 v80, v84, v85\n"
    "v_cvt_pk_bf16_f32 v81, v86, v87\n"
    "s_xor_b32 s83, s82, 0x3000\n"
    "s_nop 1\n"
    "s_call_b64 s[70:71], .Lgqa_SQK4\n"
    "v_exp_f32 v84, v84\n"
    "v_exp_f32 v85, v85\n"
    "v_exp_f32 v86, v86\n"
    "v_exp_f32 v87, v87\n"
    "v_add_f32 v166, v166, v84\n"
    "v_add_f32 v166, v166, v85\n"
    "v_add_f32 v166, v166, v86\n"
    "v_add_f32 v166, v166, v87\n"
    "v_cvt_pk_bf16_f32 v82, v84, v85\n"
    "v_cvt_pk_bf16_f32 v83, v86, v87\n"
    "v_cmp_lt_f32 vcc, 0x71800000, v175\n"
    "v_mul_f32 v174, v174, v214\n"
    "s_nop 0\n"
    "v_cndmask_b32 v174, v174, v166, vcc\n"
    ".Lgqa_HL04_rs:\n"
    "s_call_b64 s[70:71], .Lgqa_RS4\n"
    ".Lgqa_HL04_ret:\n"
    "s_setpc_b64 s[68:69]\n"
    ".Lgqa_HL14:\n"
    "v_cmp_lt_f32 vcc, s64, v174\n"
    "s_nop 1\n"
    "s_cbranch_vccz .Lgqa_HL14_ret\n"
    "v_mov_b32 v175, v174\n"
    "v_mov_b32 v156, v175\n"
    "s_nop 1\n"
    "v_permlane16_swap_b32 v175, v156\n"
    "v_max_f32 v175, v175, v156\n"
    "v_mov_b32 v156, v175\n"
    "s_nop 1\n"
    "v_permlane32_swap_b32 v175, v156\n"
    "v_max_f32 v175, v175, v156\n"
    "v_cmp_lt_f32 vcc, 0x71800000, v175\n"
    "s_nop 1\n"
    "s_cbranch_vccnz .Lgqa_HL14_slow\n"
    "v_frexp_exp_i32_f32 v175, v175\n"
    "v_max_i32 v175, 0, v175\n"
    "v_cvt_f32_i32 v214, v175\n"
    "v_sub_f32 v32, v32, v214\n"
    "v_sub_f32 v33, v33, v214\n"
    "v_sub_f32 v34, v34, v214\n"
    "v_sub_f32 v35, v35, v214\n"
    "v_sub_f32 v36, v36, v214\n"
    "v_sub_f32 v37, v37, v214\n"
    "v_sub_f32 v38, v38, v214\n"
    "v_sub_f32 v39, v39, v214\n"
    "v_sub_f32 v148, v148, v214\n"
    "v_sub_f32 v149, v149, v214\n"
    "v_sub_f32 v150, v150, v214\n"
    "v_sub_f32 v151, v151, v214\n"
    "v_sub_u32 v175, 0, v175\n"
    "v_ldexp_f32 v214, 1.0, v175\n"
    "v_mul_f32 v174, v174, v214\n"
    "v_lshlrev_b32 v156, 16, v104\n"
    "v_and_b32 v157, 0xffff0000, v104\n"
    "v_lshlrev_b32 v158, 16, v105\n"
    "v_and_b32 v159, 0xffff0000, v105\n"
    "v_lshlrev_b32 v160, 16, v106\n"
    "v_and_b32 v161, 0xffff0000, v106\n"
    "v_lshlrev_b32 v162, 16, v107\n"
    "v_and_b32 v163, 0xffff0000, v107\n"
    "v_mul_f32 v156, v156, v214\n"
    "v_mul_f32 v157, v157, v214\n"
    "v_mul_f32 v158, v158, v214\n"
    "v_mul_f32 v159, v159, v214\n"
    "v_mul_f32 v160, v160, v214\n"
    "v_mul_f32 v161, v161, v214\n"
    "v_mul_f32 v162, v162, v214\n"
    "v_mul_f32 v163, v163, v214\n"
    "v_cvt_pk_bf16_f32 v104, v156, v157\n"
    "v_cvt_pk_bf16_f32 v105, v158, v159\n"
    "v_cvt_pk_bf16_f32 v106, v160, v161\n"
    "v_cvt_pk_bf16_f32 v107, v162, v163\n"
    "v_lshlrev_b32 v156, 16, v128\n"
    "v_and_b32 v157, 0xffff0000, v128\n"
    "v_lshlrev_b32 v158, 16, v129\n"
    "v_and_b32 v159, 0xffff0000, v129\n"
    "v_lshlrev_b32 v160, 16, v130\n"
    "v_and_b32 v161, 0xffff0000, v130\n"
    "v_lshlrev_b32 v162, 16, v131\n"
    "v_and_b32 v163, 0xffff0000, v131\n"
    "v_mul_f32 v156, v156, v214\n"
    "v_mul_f32 v157, v157, v214\n"
    "v_mul_f32 v158, v158, v214\n"
    "v_mul_f32 v159, v159, v214\n"
    "v_mul_f32 v160, v160, v214\n"
    "v_mul_f32 v161, v161, v214\n"
    "v_mul_f32 v162, v162, v214\n"
    "v_mul_f32 v163, v163, v214\n"
    "v_cvt_pk_bf16_f32 v128, v156, v157\n"
    "v_cvt_pk_bf16_f32 v129, v158, v159\n"
    "v_cvt_pk_bf16_f32 v130, v160, v161\n"
    "v_cvt_pk_bf16_f32 v131, v162, v163\n"
    "s_branch .Lgqa_HL14_rs\n"
    ".Lgqa_HL14_slow:\n"
    "s_sub_u32 s82, s58, 0x8000\n"
    "s_and_b32 s82, s82, 0x3ffff\n"
    "v_mov_b32 v214, 0\n"
    "s_xor_b32 s83, s82, 0x4000\n"
    "s_nop 1\n"
    "s_call_b64 s[70:71], .Lgqa_SQK4\n"
    "v_max3_f32 v214, v214, v84, v85\n"
    "v_max3_f32 v214, v214, v86, v87\n"
    "s_xor_b32 s83, s82, 0x5000\n"
    "s_nop 1\n"
    "s_call_b64 s[70:71], .Lgqa_SQK4\n"
    "v_max3_f32 v214, v214, v84, v85\n"
    "v_max3_f32 v214, v214, v86, v87\n"
    "s_xor_b32 s83, s82, 0x6000\n"
    "s_nop 1\n"
    "s_call_b64 s[70:71], .Lgqa_SQK4\n"
    "v_max3_f32 v214, v214, v84, v85\n"
    "v_max3_f32 v214, v214, v86, v87\n"
    "s_xor_b32 s83, s82, 0x7000\n"
    "s_nop 1\n"
    "s_call_b64 s[70:71], .Lgqa_SQK4\n"
    "v_max3_f32 v214, v214, v84, v85\n"
    "v_max3_f32 v214, v214, v86, v87\n"
    "v_mov_b32 v156, v214\n"
    "s_nop 1\n"
    "v_permlane16_swap_b32 v214, v156\n"
    "v_max_f32 v214, v214, v156\n"
    "v_mov_b32 v156, v214\n"
    "s_nop 1\n"
    "v_permlane32_swap_b32 v214, v156\n"
    "v_max_f32 v214, v214, v156\n"
    "v_min_f32 v157, 0x71800000, v175\n"
    "v_log_f32 v157, v157\n"
    "s_nop 0\n"
    "v_max_f32 v214, v214, v157\n"
    "v_sub_f32 v32, v32, v214\n"
    "v_sub_f32 v33, v33, v214\n"
    "v_sub_f32 v34, v34, v214\n"
    "v_sub_f32 v35, v35, v214\n"
    "v_sub_f32 v36, v36, v214\n"
    "v_sub_f32 v37, v37, v214\n"
    "v_sub_f32 v38, v38, v214\n"
    "v_sub_f32 v39, v39, v214\n"
    "v_sub_f32 v148, v148, v214\n"
    "v_sub_f32 v149, v149, v214\n"
    "v_sub_f32 v150, v150, v214\n"
    "v_sub_f32 v151, v151, v214\n"
    "v_exp_f32 v214, -v214\n"
    "v_mov_b32 v166, 0\n"
    "s_xor_b32 s83, s82, 0x4000\n"
    "s_nop 1\n"
    "s_call_b64 s[70:71], .Lgqa_SQK4\n"
    "v_exp_f32 v84, v84\n"
    "v_exp_f32 v85, v85\n"
    "v_exp_f32 v86, v86\n"
    "v_exp_f32 v87, v87\n"
    "v_add_f32 v166, v166, v84\n"
    "v_add_f32 v166, v166, v85\n"
    "v_add_f32 v166, v166, v86\n"
    "v_add_f32 v166, v166, v87\n"
    "v_cvt_pk_bf16_f32 v104, v84, v85\n"
    "v_cvt_pk_bf16_f32 v105, v86, v87\n"
    "s_xor_b32 s83, s82, 0x5000\n"
    "s_nop 1\n"
    "s_call_b64 s[70:71], .Lgqa_SQK4\n"
    "v_exp_f32 v84, v84\n"
    "v_exp_f32 v85, v85\n"
    "v_exp_f32 v86, v86\n"
    "v_exp_f32 v87, v87\n"
    "v_add_f32 v166, v166, v84\n"
    "v_add_f32 v166, v166, v85\n"
    "v_add_f32 v166, v166, v86\n"
    "v_add_f32 v166, v166, v87\n"
    "v_cvt_pk_bf16_f32 v106, v84, v85\n"
    "v_cvt_pk_bf16_f32 v107, v86, v87\n"
    "s_xor_b32 s83, s82, 0x6000\n"
    "s_nop 1\n"
    "s_call_b64 s[70:71], .Lgqa_SQK4\n"
    "v_exp_f32 v84, v84\n"
    "v_exp_f32 v85, v85\n"
    "v_exp_f32 v86, v86\n"
    "v_exp_f32 v87, v87\n"
    "v_add_f32 v166, v166, v84\n"
    "v_add_f32 v166, v166, v85\n"
    "v_add_f32 v166, v166, v86\n"
    "v_add_f32 v166, v166, v87\n"
    "v_cvt_pk_bf16_f32 v128, v84, v85\n"
    "v_cvt_pk_bf16_f32 v129, v86, v87\n"
    "s_xor_b32 s83, s82, 0x7000\n"
    "s_nop 1\n"
    "s_call_b64 s[70:71], .Lgqa_SQK4\n"
    "v_exp_f32 v84, v84\n"
    "v_exp_f32 v85, v85\n"
    "v_exp_f32 v86, v86\n"
    "v_exp_f32 v87, v87\n"
    "v_add_f32 v166, v166, v84\n"
    "v_add_f32 v166, v166, v85\n"
    "v_add_f32 v166, v166, v86\n"
    "v_add_f32 v166, v166, v87\n"
    "v_cvt_pk_bf16_f32 v130, v84, v85\n"
    "v_cvt_pk_bf16_f32 v131, v86, v87\n"
    "v_cmp_lt_f32 vcc, 0x71800000, v175\n"
    "v_mul_f32 v174, v174, v214\n"
    "s_nop 0\n"
    "v_cndmask_b32 v174, v174, v166, vcc\n"
    ".Lgqa_HL14_rs:\n"
    "s_call_b64 s[70:71], .Lgqa_RS4\n"
    ".Lgqa_HL14_ret:\n"
    "s_setpc_b64 s[68:69]\n"
    ".Lgqa_SQK4:\n"
    "buffer_load_dwordx4 v[156:159], v215, s[40:43], s83 offen offset:0\n"
    "buffer_load_dwordx4 v[160:163], v215, s[40:43], s83 offen offset:64\n"
    "s_waitcnt vmcnt(0)\n"
    "v_mfma_f32_16x16x32_bf16 v[84:87], v[156:159], a[224:227], v[148:151]\n"
    "v_mfma_f32_16x16x32_bf16 v[84:87], v[160:163], a[228:231], v[84:87]\n"
    "s_nop 15\n"
    "buffer_load_dwordx4 v[156:159], v215, s[40:43], s83 offen offset:128\n"
    "buffer_load_dwordx4 v[160:163], v215, s[40:43], s83 offen offset:192\n"
    "s_waitcnt vmcnt(0)\n"
    "v_mfma_f32_16x16x32_bf16 v[84:87], v[156:159], a[232:235], v[84:87]\n"
    "v_mfma_f32_16x16x32_bf16 v[84:87], v[160:163], a[236:239], v[84:87]\n"
    "s_nop 15\n"
    "s_setpc_b64 s[70:71]\n"
    ".Lgqa_RS0:\n"
    "v_mul_f32 v220, v220, v214\n"
    "v_mul_f32 v221, v221, v214\n"
    "v_mul_f32 v222, v222, v214\n"
    "v_mul_f32 v223, v223, v214\n"
    "v_mul_f32 v224, v224, v214\n"
    "v_mul_f32 v225, v225, v214\n"
    "v_mul_f32 v226, v226, v214\n"
    "v_mul_f32 v227, v227, v214\n"
    "v_mul_f32 v228, v228, v214\n"
    "v_mul_f32 v229, v229, v214\n"
    "v_mul_f32 v230, v230, v214\n"
    "v_mul_f32 v231, v231, v214\n"
    "v_mul_f32 v232, v232, v214\n"
    "v_mul_f32 v233, v233, v214\n"
    "v_mul_f32 v234, v234, v214\n"
    "v_mul_f32 v235, v235, v214\n"
    "v_mul_f32 v236, v236, v214\n"
    "v_mul_f32 v237, v237, v214\n"
    "v_mul_f32 v238, v238, v214\n"
    "v_mul_f32 v239, v239, v214\n"
    "v_mul_f32 v240, v240, v214\n"
    "v_mul_f32 v241, v241, v214\n"
    "v_mul_f32 v242, v242, v214\n"
    "v_mul_f32 v243, v243, v214\n"
    "v_mul_f32 v244, v244, v214\n"
    "v_mul_f32 v245, v245, v214\n"
    "v_mul_f32 v246, v246, v214\n"
    "v_mul_f32 v247, v247, v214\n"
    "v_mul_f32 v248, v248, v214\n"
    "v_mul_f32 v249, v249, v214\n"
    "v_mul_f32 v250, v250, v214\n"
    "v_mul_f32 v251, v251, v214\n"
    "s_setpc_b64 s[70:71]\n"
    ".Lgqa_RS1:\n"
    "v_mul_f32 v176, v176, v214\n"
    "v_mul_f32 v177, v177, v214\n"
    "v_mul_f32 v178, v178, v214\n"
    "v_mul_f32 v179, v179, v214\n"
    "v_mul_f32 v180, v180, v214\n"
    "v_mul_f32 v181, v181, v214\n"
    "v_mul_f32 v182, v182, v214\n"
    "v_mul_f32 v183, v183, v214\n"
    "v_mul_f32 v184, v184, v214\n"
    "v_mul_f32 v185, v185, v214\n"
    "v_mul_f32 v186, v186, v214\n"
    "v_mul_f32 v187, v187, v214\n"
    "v_mul_f32 v188, v188, v214\n"
    "v_mul_f32 v189, v189, v214\n"
    "v_mul_f32 v190, v190, v214\n"
    "v_mul_f32 v191, v191, v214\n"
    "v_mul_f32 v192, v192, v214\n"
    "v_mul_f32 v193, v193, v214\n"
    "v_mul_f32 v194, v194, v214\n"
    "v_mul_f32 v195, v195, v214\n"
    "v_mul_f32 v196, v196, v214\n"
    "v_mul_f32 v197, v197, v214\n"
    "v_mul_f32 v198, v198, v214\n"
    "v_mul_f32 v199, v199, v214\n"
    "v_mul_f32 v200, v200, v214\n"
    "v_mul_f32 v201, v201, v214\n"
    "v_mul_f32 v202, v202, v214\n"
    "v_mul_f32 v203, v203, v214\n"
    "v_mul_f32 v204, v204, v214\n"
    "v_mul_f32 v205, v205, v214\n"
    "v_mul_f32 v206, v206, v214\n"
    "v_mul_f32 v207, v207, v214\n"
    "s_setpc_b64 s[70:71]\n"
    ".Lgqa_RS2:\n"
    "v_accvgpr_read_b32 v156, a32\n"
    "v_accvgpr_read_b32 v157, a33\n"
    "v_accvgpr_read_b32 v158, a34\n"
    "v_accvgpr_read_b32 v159, a35\n"
    "v_accvgpr_read_b32 v160, a36\n"
    "v_accvgpr_read_b32 v161, a37\n"
    "v_accvgpr_read_b32 v162, a38\n"
    "v_accvgpr_read_b32 v163, a39\n"
    "v_mul_f32 v156, v156, v214\n"
    "v_mul_f32 v157, v157, v214\n"
    "v_mul_f32 v158, v158, v214\n"
    "v_mul_f32 v159, v159, v214\n"
    "v_mul_f32 v160, v160, v214\n"
    "v_mul_f32 v161, v161, v214\n"
    "v_mul_f32 v162, v162, v214\n"
    "v_mul_f32 v163, v163, v214\n"
    "v_accvgpr_write_b32 a32, v156\n"
    "v_accvgpr_write_b32 a33, v157\n"
    "v_accvgpr_write_b32 a34, v158\n"
    "v_accvgpr_write_b32 a35, v159\n"
    "v_accvgpr_write_b32 a36, v160\n"
    "v_accvgpr_write_b32 a37, v161\n"
    "v_accvgpr_write_b32 a38, v162\n"
    "v_accvgpr_write_b32 a39, v163\n"
    "v_accvgpr_read_b32 v156, a40\n"
    "v_accvgpr_read_b32 v157, a41\n"
    "v_accvgpr_read_b32 v158, a42\n"
    "v_accvgpr_read_b32 v159, a43\n"
    "v_accvgpr_read_b32 v160, a44\n"
    "v_accvgpr_read_b32 v161, a45\n"
    "v_accvgpr_read_b32 v162, a46\n"
    "v_accvgpr_read_b32 v163, a47\n"
    "v_mul_f32 v156, v156, v214\n"
    "v_mul_f32 v157, v157, v214\n"
    "v_mul_f32 v158, v158, v214\n"
    "v_mul_f32 v159, v159, v214\n"
    "v_mul_f32 v160, v160, v214\n"
    "v_mul_f32 v161, v161, v214\n"
    "v_mul_f32 v162, v162, v214\n"
    "v_mul_f32 v163, v163, v214\n"
    "v_accvgpr_write_b32 a40, v156\n"
    "v_accvgpr_write_b32 a41, v157\n"
    "v_accvgpr_write_b32 a42, v158\n"
    "v_accvgpr_write_b32 a43, v159\n"
    "v_accvgpr_write_b32 a44, v160\n"
    "v_accvgpr_write_b32 a45, v161\n"
    "v_accvgpr_write_b32 a46, v162\n"
    "v_accvgpr_write_b32 a47, v163\n"
    "v_accvgpr_read_b32 v156, a48\n"
    "v_accvgpr_read_b32 v157, a49\n"
    "v_accvgpr_read_b32 v158, a50\n"
    "v_accvgpr_read_b32 v159, a51\n"
    "v_accvgpr_read_b32 v160, a52\n"
    "v_accvgpr_read_b32 v161, a53\n"
    "v_accvgpr_read_b32 v162, a54\n"
    "v_accvgpr_read_b32 v163, a55\n"
    "v_mul_f32 v156, v156, v214\n"
    "v_mul_f32 v157, v157, v214\n"
    "v_mul_f32 v158, v158, v214\n"
    "v_mul_f32 v159, v159, v214\n"
    "v_mul_f32 v160, v160, v214\n"
    "v_mul_f32 v161, v161, v214\n"
    "v_mul_f32 v162, v162, v214\n"
    "v_mul_f32 v163, v163, v214\n"
    "v_accvgpr_write_b32 a48, v156\n"
    "v_accvgpr_write_b32 a49, v157\n"
    "v_accvgpr_write_b32 a50, v158\n"
    "v_accvgpr_write_b32 a51, v159\n"
    "v_accvgpr_write_b32 a52, v160\n"
    "v_accvgpr_write_b32 a53, v161\n"
    "v_accvgpr_write_b32 a54, v162\n"
    "v_accvgpr_write_b32 a55, v163\n"
    "v_accvgpr_read_b32 v156, a56\n"
    "v_accvgpr_read_b32 v157, a57\n"
    "v_accvgpr_read_b32 v158, a58\n"
    "v_accvgpr_read_b32 v159, a59\n"
    "v_accvgpr_read_b32 v160, a60\n"
    "v_accvgpr_read_b32 v161, a61\n"
    "v_accvgpr_read_b32 v162, a62\n"
    "v_accvgpr_read_b32 v163, a63\n"
    "v_mul_f32 v156, v156, v214\n"
    "v_mul_f32 v157, v157, v214\n"
    "v_mul_f32 v158, v158, v214\n"
    "v_mul_f32 v159, v159, v214\n"
    "v_mul_f32 v160, v160, v214\n"
    "v_mul_f32 v161, v161, v214\n"
    "v_mul_f32 v162, v162, v214\n"
    "v_mul_f32 v163, v163, v214\n"
    "v_accvgpr_write_b32 a56, v156\n"
    "v_accvgpr_write_b32 a57, v157\n"
    "v_accvgpr_write_b32 a58, v158\n"
    "v_accvgpr_write_b32 a59, v159\n"
    "v_accvgpr_write_b32 a60, v160\n"
    "v_accvgpr_write_b32 a61, v161\n"
    "v_accvgpr_write_b32 a62, v162\n"
    "v_accvgpr_write_b32 a63, v163\n"
    "s_setpc_b64 s[70:71]\n"
    ".Lgqa_RS3:\n"
    "v_accvgpr_read_b32 v156, a64\n"
    "v_accvgpr_read_b32 v157, a65\n"
    "v_accvgpr_read_b32 v158, a66\n"
    "v_accvgpr_read_b32 v159, a67\n"
    "v_accvgpr_read_b32 v160, a68\n"
    "v_accvgpr_read_b32 v161, a69\n"
    "v_accvgpr_read_b32 v162, a70\n"
    "v_accvgpr_read_b32 v163, a71\n"
    "v_mul_f32 v156, v156, v214\n"
    "v_mul_f32 v157, v157, v214\n"
    "v_mul_f32 v158, v158, v214\n"
    "v_mul_f32 v159, v159, v214\n"
    "v_mul_f32 v160, v160, v214\n"
    "v_mul_f32 v161, v161, v214\n"
    "v_mul_f32 v162, v162, v214\n"
    "v_mul_f32 v163, v163, v214\n"
    "v_accvgpr_write_b32 a64, v156\n"
    "v_accvgpr_write_b32 a65, v157\n"
    "v_accvgpr_write_b32 a66, v158\n"
    "v_accvgpr_write_b32 a67, v159\n"
    "v_accvgpr_write_b32 a68, v160\n"
    "v_accvgpr_write_b32 a69, v161\n"
    "v_accvgpr_write_b32 a70, v162\n"
    "v_accvgpr_write_b32 a71, v163\n"
    "v_accvgpr_read_b32 v156, a72\n"
    "v_accvgpr_read_b32 v157, a73\n"
    "v_accvgpr_read_b32 v158, a74\n"
    "v_accvgpr_read_b32 v159, a75\n"
    "v_accvgpr_read_b32 v160, a76\n"
    "v_accvgpr_read_b32 v161, a77\n"
    "v_accvgpr_read_b32 v162, a78\n"
    "v_accvgpr_read_b32 v163, a79\n"
    "v_mul_f32 v156, v156, v214\n"
    "v_mul_f32 v157, v157, v214\n"
    "v_mul_f32 v158, v158, v214\n"
    "v_mul_f32 v159, v159, v214\n"
    "v_mul_f32 v160, v160, v214\n"
    "v_mul_f32 v161, v161, v214\n"
    "v_mul_f32 v162, v162, v214\n"
    "v_mul_f32 v163, v163, v214\n"
    "v_accvgpr_write_b32 a72, v156\n"
    "v_accvgpr_write_b32 a73, v157\n"
    "v_accvgpr_write_b32 a74, v158\n"
    "v_accvgpr_write_b32 a75, v159\n"
    "v_accvgpr_write_b32 a76, v160\n"
    "v_accvgpr_write_b32 a77, v161\n"
    "v_accvgpr_write_b32 a78, v162\n"
    "v_accvgpr_write_b32 a79, v163\n"
    "v_accvgpr_read_b32 v156, a80\n"
    "v_accvgpr_read_b32 v157, a81\n"
    "v_accvgpr_read_b32 v158, a82\n"
    "v_accvgpr_read_b32 v159, a83\n"
    "v_accvgpr_read_b32 v160, a84\n"
    "v_accvgpr_read_b32 v161, a85\n"
    "v_accvgpr_read_b32 v162, a86\n"
    "v_accvgpr_read_b32 v163, a87\n"
    "v_mul_f32 v156, v156, v214\n"
    "v_mul_f32 v157, v157, v214\n"
    "v_mul_f32 v158, v158, v214\n"
    "v_mul_f32 v159, v159, v214\n"
    "v_mul_f32 v160, v160, v214\n"
    "v_mul_f32 v161, v161, v214\n"
    "v_mul_f32 v162, v162, v214\n"
    "v_mul_f32 v163, v163, v214\n"
    "v_accvgpr_write_b32 a80, v156\n"
    "v_accvgpr_write_b32 a81, v157\n"
    "v_accvgpr_write_b32 a82, v158\n"
    "v_accvgpr_write_b32 a83, v159\n"
    "v_accvgpr_write_b32 a84, v160\n"
    "v_accvgpr_write_b32 a85, v161\n"
    "v_accvgpr_write_b32 a86, v162\n"
    "v_accvgpr_write_b32 a87, v163\n"
    "v_accvgpr_read_b32 v156, a88\n"
    "v_accvgpr_read_b32 v157, a89\n"
    "v_accvgpr_read_b32 v158, a90\n"
    "v_accvgpr_read_b32 v159, a91\n"
    "v_accvgpr_read_b32 v160, a92\n"
    "v_accvgpr_read_b32 v161, a93\n"
    "v_accvgpr_read_b32 v162, a94\n"
    "v_accvgpr_read_b32 v163, a95\n"
    "v_mul_f32 v156, v156, v214\n"
    "v_mul_f32 v157, v157, v214\n"
    "v_mul_f32 v158, v158, v214\n"
    "v_mul_f32 v159, v159, v214\n"
    "v_mul_f32 v160, v160, v214\n"
    "v_mul_f32 v161, v161, v214\n"
    "v_mul_f32 v162, v162, v214\n"
    "v_mul_f32 v163, v163, v214\n"
    "v_accvgpr_write_b32 a88, v156\n"
    "v_accvgpr_write_b32 a89, v157\n"
    "v_accvgpr_write_b32 a90, v158\n"
    "v_accvgpr_write_b32 a91, v159\n"
    "v_accvgpr_write_b32 a92, v160\n"
    "v_accvgpr_write_b32 a93, v161\n"
    "v_accvgpr_write_b32 a94, v162\n"
    "v_accvgpr_write_b32 a95, v163\n"
    "s_setpc_b64 s[70:71]\n"
    ".Lgqa_RS4:\n"
    "v_accvgpr_read_b32 v156, a96\n"
    "v_accvgpr_read_b32 v157, a97\n"
    "v_accvgpr_read_b32 v158, a98\n"
    "v_accvgpr_read_b32 v159, a99\n"
    "v_accvgpr_read_b32 v160, a100\n"
    "v_accvgpr_read_b32 v161, a101\n"
    "v_accvgpr_read_b32 v162, a102\n"
    "v_accvgpr_read_b32 v163, a103\n"
    "v_mul_f32 v156, v156, v214\n"
    "v_mul_f32 v157, v157, v214\n"
    "v_mul_f32 v158, v158, v214\n"
    "v_mul_f32 v159, v159, v214\n"
    "v_mul_f32 v160, v160, v214\n"
    "v_mul_f32 v161, v161, v214\n"
    "v_mul_f32 v162, v162, v214\n"
    "v_mul_f32 v163, v163, v214\n"
    "v_accvgpr_write_b32 a96, v156\n"
    "v_accvgpr_write_b32 a97, v157\n"
    "v_accvgpr_write_b32 a98, v158\n"
    "v_accvgpr_write_b32 a99, v159\n"
    "v_accvgpr_write_b32 a100, v160\n"
    "v_accvgpr_write_b32 a101, v161\n"
    "v_accvgpr_write_b32 a102, v162\n"
    "v_accvgpr_write_b32 a103, v163\n"
    "v_accvgpr_read_b32 v156, a104\n"
    "v_accvgpr_read_b32 v157, a105\n"
    "v_accvgpr_read_b32 v158, a106\n"
    "v_accvgpr_read_b32 v159, a107\n"
    "v_accvgpr_read_b32 v160, a108\n"
    "v_accvgpr_read_b32 v161, a109\n"
    "v_accvgpr_read_b32 v162, a110\n"
    "v_accvgpr_read_b32 v163, a111\n"
    "v_mul_f32 v156, v156, v214\n"
    "v_mul_f32 v157, v157, v214\n"
    "v_mul_f32 v158, v158, v214\n"
    "v_mul_f32 v159, v159, v214\n"
    "v_mul_f32 v160, v160, v214\n"
    "v_mul_f32 v161, v161, v214\n"
    "v_mul_f32 v162, v162, v214\n"
    "v_mul_f32 v163, v163, v214\n"
    "v_accvgpr_write_b32 a104, v156\n"
    "v_accvgpr_write_b32 a105, v157\n"
    "v_accvgpr_write_b32 a106, v158\n"
    "v_accvgpr_write_b32 a107, v159\n"
    "v_accvgpr_write_b32 a108, v160\n"
    "v_accvgpr_write_b32 a109, v161\n"
    "v_accvgpr_write_b32 a110, v162\n"
    "v_accvgpr_write_b32 a111, v163\n"
    "v_accvgpr_read_b32 v156, a112\n"
    "v_accvgpr_read_b32 v157, a113\n"
    "v_accvgpr_read_b32 v158, a114\n"
    "v_accvgpr_read_b32 v159, a115\n"
    "v_accvgpr_read_b32 v160, a116\n"
    "v_accvgpr_read_b32 v161, a117\n"
    "v_accvgpr_read_b32 v162, a118\n"
    "v_accvgpr_read_b32 v163, a119\n"
    "v_mul_f32 v156, v156, v214\n"
    "v_mul_f32 v157, v157, v214\n"
    "v_mul_f32 v158, v158, v214\n"
    "v_mul_f32 v159, v159, v214\n"
    "v_mul_f32 v160, v160, v214\n"
    "v_mul_f32 v161, v161, v214\n"
    "v_mul_f32 v162, v162, v214\n"
    "v_mul_f32 v163, v163, v214\n"
    "v_accvgpr_write_b32 a112, v156\n"
    "v_accvgpr_write_b32 a113, v157\n"
    "v_accvgpr_write_b32 a114, v158\n"
    "v_accvgpr_write_b32 a115, v159\n"
    "v_accvgpr_write_b32 a116, v160\n"
    "v_accvgpr_write_b32 a117, v161\n"
    "v_accvgpr_write_b32 a118, v162\n"
    "v_accvgpr_write_b32 a119, v163\n"
    "v_accvgpr_read_b32 v156, a120\n"
    "v_accvgpr_read_b32 v157, a121\n"
    "v_accvgpr_read_b32 v158, a122\n"
    "v_accvgpr_read_b32 v159, a123\n"
    "v_accvgpr_read_b32 v160, a124\n"
    "v_accvgpr_read_b32 v161, a125\n"
    "v_accvgpr_read_b32 v162, a126\n"
    "v_accvgpr_read_b32 v163, a127\n"
    "v_mul_f32 v156, v156, v214\n"
    "v_mul_f32 v157, v157, v214\n"
    "v_mul_f32 v158, v158, v214\n"
    "v_mul_f32 v159, v159, v214\n"
    "v_mul_f32 v160, v160, v214\n"
    "v_mul_f32 v161, v161, v214\n"
    "v_mul_f32 v162, v162, v214\n"
    "v_mul_f32 v163, v163, v214\n"
    "v_accvgpr_write_b32 a120, v156\n"
    "v_accvgpr_write_b32 a121, v157\n"
    "v_accvgpr_write_b32 a122, v158\n"
    "v_accvgpr_write_b32 a123, v159\n"
    "v_accvgpr_write_b32 a124, v160\n"
    "v_accvgpr_write_b32 a125, v161\n"
    "v_accvgpr_write_b32 a126, v162\n"
    "v_accvgpr_write_b32 a127, v163\n"
    "s_setpc_b64 s[70:71]\n"
    ".Lgqa_RS5:\n"
    "v_accvgpr_read_b32 v156, a128\n"
    "v_accvgpr_read_b32 v157, a129\n"
    "v_accvgpr_read_b32 v158, a130\n"
    "v_accvgpr_read_b32 v159, a131\n"
    "v_accvgpr_read_b32 v160, a132\n"
    "v_accvgpr_read_b32 v161, a133\n"
    "v_accvgpr_read_b32 v162, a134\n"
    "v_accvgpr_read_b32 v163, a135\n"
    "v_mul_f32 v156, v156, v214\n"
    "v_mul_f32 v157, v157, v214\n"
    "v_mul_f32 v158, v158, v214\n"
    "v_mul_f32 v159, v159, v214\n"
    "v_mul_f32 v160, v160, v214\n"
    "v_mul_f32 v161, v161, v214\n"
    "v_mul_f32 v162, v162, v214\n"
    "v_mul_f32 v163, v163, v214\n"
    "v_accvgpr_write_b32 a128, v156\n"
    "v_accvgpr_write_b32 a129, v157\n"
    "v_accvgpr_write_b32 a130, v158\n"
    "v_accvgpr_write_b32 a131, v159\n"
    "v_accvgpr_write_b32 a132, v160\n"
    "v_accvgpr_write_b32 a133, v161\n"
    "v_accvgpr_write_b32 a134, v162\n"
    "v_accvgpr_write_b32 a135, v163\n"
    "v_accvgpr_read_b32 v156, a136\n"
    "v_accvgpr_read_b32 v157, a137\n"
    "v_accvgpr_read_b32 v158, a138\n"
    "v_accvgpr_read_b32 v159, a139\n"
    "v_accvgpr_read_b32 v160, a140\n"
    "v_accvgpr_read_b32 v161, a141\n"
    "v_accvgpr_read_b32 v162, a142\n"
    "v_accvgpr_read_b32 v163, a143\n"
    "v_mul_f32 v156, v156, v214\n"
    "v_mul_f32 v157, v157, v214\n"
    "v_mul_f32 v158, v158, v214\n"
    "v_mul_f32 v159, v159, v214\n"
    "v_mul_f32 v160, v160, v214\n"
    "v_mul_f32 v161, v161, v214\n"
    "v_mul_f32 v162, v162, v214\n"
    "v_mul_f32 v163, v163, v214\n"
    "v_accvgpr_write_b32 a136, v156\n"
    "v_accvgpr_write_b32 a137, v157\n"
    "v_accvgpr_write_b32 a138, v158\n"
    "v_accvgpr_write_b32 a139, v159\n"
    "v_accvgpr_write_b32 a140, v160\n"
    "v_accvgpr_write_b32 a141, v161\n"
    "v_accvgpr_write_b32 a142, v162\n"
    "v_accvgpr_write_b32 a143, v163\n"
    "v_accvgpr_read_b32 v156, a144\n"
    "v_accvgpr_read_b32 v157, a145\n"
    "v_accvgpr_read_b32 v158, a146\n"
    "v_accvgpr_read_b32 v159, a147\n"
    "v_accvgpr_read_b32 v160, a148\n"
    "v_accvgpr_read_b32 v161, a149\n"
    "v_accvgpr_read_b32 v162, a150\n"
    "v_accvgpr_read_b32 v163, a151\n"
    "v_mul_f32 v156, v156, v214\n"
    "v_mul_f32 v157, v157, v214\n"
    "v_mul_f32 v158, v158, v214\n"
    "v_mul_f32 v159, v159, v214\n"
    "v_mul_f32 v160, v160, v214\n"
    "v_mul_f32 v161, v161, v214\n"
    "v_mul_f32 v162, v162, v214\n"
    "v_mul_f32 v163, v163, v214\n"
    "v_accvgpr_write_b32 a144, v156\n"
    "v_accvgpr_write_b32 a145, v157\n"
    "v_accvgpr_write_b32 a146, v158\n"
    "v_accvgpr_write_b32 a147, v159\n"
    "v_accvgpr_write_b32 a148, v160\n"
    "v_accvgpr_write_b32 a149, v161\n"
    "v_accvgpr_write_b32 a150, v162\n"
    "v_accvgpr_write_b32 a151, v163\n"
    "v_accvgpr_read_b32 v156, a152\n"
    "v_accvgpr_read_b32 v157, a153\n"
    "v_accvgpr_read_b32 v158, a154\n"
    "v_accvgpr_read_b32 v159, a155\n"
    "v_accvgpr_read_b32 v160, a156\n"
    "v_accvgpr_read_b32 v161, a157\n"
    "v_accvgpr_read_b32 v162, a158\n"
    "v_accvgpr_read_b32 v163, a159\n"
    "v_mul_f32 v156, v156, v214\n"
    "v_mul_f32 v157, v157, v214\n"
    "v_mul_f32 v158, v158, v214\n"
    "v_mul_f32 v159, v159, v214\n"
    "v_mul_f32 v160, v160, v214\n"
    "v_mul_f32 v161, v161, v214\n"
    "v_mul_f32 v162, v162, v214\n"
    "v_mul_f32 v163, v163, v214\n"
    "v_accvgpr_write_b32 a152, v156\n"
    "v_accvgpr_write_b32 a153, v157\n"
    "v_accvgpr_write_b32 a154, v158\n"
    "v_accvgpr_write_b32 a155, v159\n"
    "v_accvgpr_write_b32 a156, v160\n"
    "v_accvgpr_write_b32 a157, v161\n"
    "v_accvgpr_write_b32 a158, v162\n"
    "v_accvgpr_write_b32 a159, v163\n"
    "s_setpc_b64 s[70:71]\n"
    ".Lgqa_HMH:\n"
    "v_cmp_lt_f32 vcc, s64, v216\n"
    "s_nop 1\n"
    "s_cbranch_vccz .Lgqa_HMH_ret\n"
    "v_mov_b32 v175, v216\n"
    "v_max3_f32 v214, 0, v40, v41\n"
    "v_max3_f32 v214, v214, v42, v43\n"
    "v_max3_f32 v214, v214, v44, v45\n"
    "v_max3_f32 v214, v214, v46, v47\n"
    "v_mov_b32 v156, v214\n"
    "s_nop 1\n"
    "v_permlane16_swap_b32 v214, v156\n"
    "v_max_f32 v214, v214, v156\n"
    "v_mov_b32 v156, v214\n"
    "s_nop 1\n"
    "v_permlane32_swap_b32 v214, v156\n"
    "v_max_f32 v214, v214, v156\n"
    "v_min_f32 v157, 0x71800000, v175\n"
    "v_log_f32 v157, v157\n"
    "s_nop 0\n"
    "v_max_f32 v214, v214, v157\n"
    "v_sub_f32 v40, v40, v214\n"
    "v_sub_f32 v41, v41, v214\n"
    "v_sub_f32 v42, v42, v214\n"
    "v_sub_f32 v43, v43, v214\n"
    "v_sub_f32 v44, v44, v214\n"
    "v_sub_f32 v45, v45, v214\n"
    "v_sub_f32 v46, v46, v214\n"
    "v_sub_f32 v47, v47, v214\n"
    "v_sub_f32 v152, v152, v214\n"
    "v_sub_f32 v153, v153, v214\n"
    "v_sub_f32 v154, v154, v214\n"
    "v_sub_f32 v155, v155, v214\n"
    "v_exp_f32 v214, -v214\n"
    "v_exp_f32 v156, v40\n"
    "v_exp_f32 v157, v41\n"
    "s_nop 0\n"
    "v_add_f32 v166, v156, v157\n"
    "v_cvt_pk_bf16_f32 v108, v156, v157\n"
    "v_exp_f32 v158, v42\n"
    "s_nop 0\n"
    "v_add_f32 v166, v166, v158\n"
    "v_exp_f32 v159, v43\n"
    "s_nop 0\n"
    "v_add_f32 v166, v166, v159\n"
    "v_cvt_pk_bf16_f32 v109, v158, v159\n"
    "v_exp_f32 v160, v44\n"
    "s_nop 0\n"
    "v_add_f32 v166, v166, v160\n"
    "v_exp_f32 v161, v45\n"
    "s_nop 0\n"
    "v_add_f32 v166, v166, v161\n"
    "v_cvt_pk_bf16_f32 v110, v160, v161\n"
    "v_exp_f32 v162, v46\n"
    "s_nop 0\n"
    "v_add_f32 v166, v166, v162\n"
    "v_exp_f32 v163, v47\n"
    "s_nop 0\n"
    "v_add_f32 v166, v166, v163\n"
    "v_cvt_pk_bf16_f32 v111, v162, v163\n"
    "v_mov_b32 v156, v166\n"
    "s_nop 1\n"
    "v_permlane16_swap_b32 v166, v156\n"
    "v_add_f32 v166, v166, v156\n"
    "v_mov_b32 v156, v166\n"
    "s_nop 1\n"
    "v_permlane32_swap_b32 v166, v156\n"
    "v_add_f32 v166, v166, v156\n"
    "v_cmp_lt_f32 vcc, 0x71800000, v175\n"
    "v_mul_f32 v216, v216, v214\n"
    "s_nop 0\n"
    "v_cndmask_b32 v216, v216, v166, vcc\n"
    "s_call_b64 s[70:71], .Lgqa_RS5\n"
    ".Lgqa_HMH_ret:\n"
    "s_setpc_b64 s[68:69]\n"
    ".Lgqa_HH0:\n"
    "v_max3_f32 v214, 0, v228, v229\n"
    "v_max3_f32 v214, v214, v230, v231\n"
    "v_max3_f32 v214, v214, v232, v233\n"
    "v_max3_f32 v214, v214, v234, v235\n"
    "v_max3_f32 v214, v214, v4, v5\n"
    "v_max3_f32 v214, v214, v6, v7\n"
    "v_max3_f32 v214, v214, v0, v1\n"
    "v_max3_f32 v214, v214, v2, v3\n"
    "v_max3_f32 v214, v214, v8, v9\n"
    "v_max3_f32 v214, v214, v10, v11\n"
    "v_max3_f32 v214, v214, v12, v13\n"
    "v_max3_f32 v214, v214, v14, v15\n"
    "v_mov_b32 v156, v214\n"
    "s_nop 1\n"
    "v_permlane16_swap_b32 v214, v156\n"
    "v_max_f32 v214, v214, v156\n"
    "v_mov_b32 v156, v214\n"
    "s_nop 1\n"
    "v_permlane32_swap_b32 v214, v156\n"
    "v_max_f32 v214, v214, v156\n"
    "v_sub_f32 v220, v220, v214\n"
    "v_sub_f32 v221, v221, v214\n"
    "v_sub_f32 v222, v222, v214\n"
    "v_sub_f32 v223, v223, v214\n"
    "v_sub_f32 v224, v224, v214\n"
    "v_sub_f32 v225, v225, v214\n"
    "v_sub_f32 v226, v226, v214\n"
    "v_sub_f32 v227, v227, v214\n"
    "v_sub_f32 v228, v228, v214\n"
    "v_sub_f32 v229, v229, v214\n"
    "v_sub_f32 v230, v230, v214\n"
    "v_sub_f32 v231, v231, v214\n"
    "v_sub_f32 v232, v232, v214\n"
    "v_sub_f32 v233, v233, v214\n"
    "v_sub_f32 v234, v234, v214\n"
    "v_sub_f32 v235, v235, v214\n"
    "v_sub_f32 v4, v4, v214\n"
    "v_sub_f32 v5, v5, v214\n"
    "v_sub_f32 v6, v6, v214\n"
    "v_sub_f32 v7, v7, v214\n"
    "v_sub_f32 v0, v0, v214\n"
    "v_sub_f32 v1, v1, v214\n"
    "v_sub_f32 v2, v2, v214\n"
    "v_sub_f32 v3, v3, v214\n"
    "v_sub_f32 v8, v8, v214\n"
    "v_sub_f32 v9, v9, v214\n"
    "v_sub_f32 v10, v10, v214\n"
    "v_sub_f32 v11, v11, v214\n"
    "v_sub_f32 v12, v12, v214\n"
    "v_sub_f32 v13, v13, v214\n"
    "v_sub_f32 v14, v14, v214\n"
    "v_sub_f32 v15, v15, v214\n"
    "v_sub_f32 v132, v132, v214\n"
    "v_sub_f32 v133, v133, v214\n"
    "v_sub_f32 v134, v134, v214\n"
    "v_sub_f32 v135, v135, v214\n"
    "v_exp_f32 v156, v220\n"
    "v_exp_f32 v157, v221\n"
    "s_nop 0\n"
    "v_add_f32 v164, v156, v157\n"
    "v_cvt_pk_bf16_f32 v88, v156, v157\n"
    "v_exp_f32 v158, v222\n"
    "s_nop 0\n"
    "v_add_f32 v164, v164, v158\n"
    "v_exp_f32 v159, v223\n"
    "s_nop 0\n"
    "v_add_f32 v164, v164, v159\n"
    "v_cvt_pk_bf16_f32 v89, v158, v159\n"
    "v_exp_f32 v160, v224\n"
    "s_nop 0\n"
    "v_add_f32 v164, v164, v160\n"
    "v_exp_f32 v161, v225\n"
    "s_nop 0\n"
    "v_add_f32 v164, v164, v161\n"
    "v_cvt_pk_bf16_f32 v90, v160, v161\n"
    "v_exp_f32 v162, v226\n"
    "s_nop 0\n"
    "v_add_f32 v164, v164, v162\n"
    "v_exp_f32 v163, v227\n"
    "s_nop 0\n"
    "v_add_f32 v164, v164, v163\n"
    "v_cvt_pk_bf16_f32 v91, v162, v163\n"
    "v_exp_f32 v156, v228\n"
    "v_exp_f32 v157, v229\n"
    "v_add_f32 v164, v164, v156\n"
    "v_add_f32 v164, v164, v157\n"
    "v_cvt_pk_bf16_f32 v92, v156, v157\n"
    "v_exp_f32 v158, v230\n"
    "s_nop 0\n"
    "v_add_f32 v164, v164, v158\n"
    "v_exp_f32 v159, v231\n"
    "s_nop 0\n"
    "v_add_f32 v164, v164, v159\n"
    "v_cvt_pk_bf16_f32 v93, v158, v159\n"
    "v_exp_f32 v160, v232\n"
    "s_nop 0\n"
    "v_add_f32 v164, v164, v160\n"
    "v_exp_f32 v161, v233\n"
    "s_nop 0\n"
    "v_add_f32 v164, v164, v161\n"
    "v_cvt_pk_bf16_f32 v94, v160, v161\n"
    "v_exp_f32 v162, v234\n"
    "s_nop 0\n"
    "v_add_f32 v164, v164, v162\n"
    "v_exp_f32 v163, v235\n"
    "s_nop 0\n"
    "v_add_f32 v164, v164, v163\n"
    "v_cvt_pk_bf16_f32 v95, v162, v163\n"
    "v_mov_b32 v156, v164\n"
    "s_nop 1\n"
    "v_permlane16_swap_b32 v164, v156\n"
    "v_add_f32 v164, v164, v156\n"
    "v_mov_b32 v156, v164\n"
    "s_nop 1\n"
    "v_permlane32_swap_b32 v164, v156\n"
    "v_add_f32 v164, v164, v156\n"
    "s_setpc_b64 s[68:69]\n"
    ".Lgqa_HH1:\n"
    "v_max3_f32 v214, 0, v244, v245\n"
    "v_max3_f32 v214, v214, v246, v247\n"
    "v_max3_f32 v214, v214, v248, v249\n"
    "v_max3_f32 v214, v214, v250, v251\n"
    "v_max3_f32 v214, v214, v52, v53\n"
    "v_max3_f32 v214, v214, v54, v55\n"
    "v_max3_f32 v214, v214, v48, v49\n"
    "v_max3_f32 v214, v214, v50, v51\n"
    "v_max3_f32 v214, v214, v56, v57\n"
    "v_max3_f32 v214, v214, v58, v59\n"
    "v_max3_f32 v214, v214, v60, v61\n"
    "v_max3_f32 v214, v214, v62, v63\n"
    "v_mov_b32 v156, v214\n"
    "s_nop 1\n"
    "v_permlane16_swap_b32 v214, v156\n"
    "v_max_f32 v214, v214, v156\n"
    "v_mov_b32 v156, v214\n"
    "s_nop 1\n"
    "v_permlane32_swap_b32 v214, v156\n"
    "v_max_f32 v214, v214, v156\n"
    "v_sub_f32 v236, v236, v214\n"
    "v_sub_f32 v237, v237, v214\n"
    "v_sub_f32 v238, v238, v214\n"
    "v_sub_f32 v239, v239, v214\n"
    "v_sub_f32 v240, v240, v214\n"
    "v_sub_f32 v241, v241, v214\n"
    "v_sub_f32 v242, v242, v214\n"
    "v_sub_f32 v243, v243, v214\n"
    "v_sub_f32 v244, v244, v214\n"
    "v_sub_f32 v245, v245, v214\n"
    "v_sub_f32 v246, v246, v214\n"
    "v_sub_f32 v247, v247, v214\n"
    "v_sub_f32 v248, v248, v214\n"
    "v_sub_f32 v249, v249, v214\n"
    "v_sub_f32 v250, v250, v214\n"
    "v_sub_f32 v251, v251, v214\n"
    "v_sub_f32 v52, v52, v214\n"
    "v_sub_f32 v53, v53, v214\n"
    "v_sub_f32 v54, v54, v214\n"
    "v_sub_f32 v55, v55, v214\n"
    "v_sub_f32 v48, v48, v214\n"
    "v_sub_f32 v49, v49, v214\n"
    "v_sub_f32 v50, v50, v214\n"
    "v_sub_f32 v51, v51, v214\n"
    "v_sub_f32 v56, v56, v214\n"
    "v_sub_f32 v57, v57, v214\n"
    "v_sub_f32 v58, v58, v214\n"
    "v_sub_f32 v59, v59, v214\n"
    "v_sub_f32 v60, v60, v214\n"
    "v_sub_f32 v61, v61, v214\n"
    "v_sub_f32 v62, v62, v214\n"
    "v_sub_f32 v63, v63, v214\n"
    "v_sub_f32 v136, v136, v214\n"
    "v_sub_f32 v137, v137, v214\n"
    "v_sub_f32 v138, v138, v214\n"
    "v_sub_f32 v139, v139, v214\n"
    "v_exp_f32 v156, v236\n"
    "v_exp_f32 v157, v237\n"
    "s_nop 0\n"
    "v_add_f32 v168, v156, v157\n"
    "v_cvt_pk_bf16_f32 v112, v156, v157\n"
    "v_exp_f32 v158, v238\n"
    "s_nop 0\n"
    "v_add_f32 v168, v168, v158\n"
    "v_exp_f32 v159, v239\n"
    "s_nop 0\n"
    "v_add_f32 v168, v168, v159\n"
    "v_cvt_pk_bf16_f32 v113, v158, v159\n"
    "v_exp_f32 v160, v240\n"
    "s_nop 0\n"
    "v_add_f32 v168, v168, v160\n"
    "v_exp_f32 v161, v241\n"
    "s_nop 0\n"
    "v_add_f32 v168, v168, v161\n"
    "v_cvt_pk_bf16_f32 v114, v160, v161\n"
    "v_exp_f32 v162, v242\n"
    "s_nop 0\n"
    "v_add_f32 v168, v168, v162\n"
    "v_exp_f32 v163, v243\n"
    "s_nop 0\n"
    "v_add_f32 v168, v168, v163\n"
    "v_cvt_pk_bf16_f32 v115, v162, v163\n"
    "v_exp_f32 v156, v244\n"
    "v_exp_f32 v157, v245\n"
    "v_add_f32 v168, v168, v156\n"
    "v_add_f32 v168, v168, v157\n"
    "v_cvt_pk_bf16_f32 v116, v156, v157\n"
    "v_exp_f32 v158, v246\n"
    "s_nop 0\n"
    "v_add_f32 v168, v168, v158\n"
    "v_exp_f32 v159, v247\n"
    "s_nop 0\n"
    "v_add_f32 v168, v168, v159\n"
    "v_cvt_pk_bf16_f32 v117, v158, v159\n"
    "v_exp_f32 v160, v248\n"
    "s_nop 0\n"
    "v_add_f32 v168, v168, v160\n"
    "v_exp_f32 v161, v249\n"
    "s_nop 0\n"
    "v_add_f32 v168, v168, v161\n"
    "v_cvt_pk_bf16_f32 v118, v160, v161\n"
    "v_exp_f32 v162, v250\n"
    "s_nop 0\n"
    "v_add_f32 v168, v168, v162\n"
    "v_exp_f32 v163, v251\n"
    "s_nop 0\n"
    "v_add_f32 v168, v168, v163\n"
    "v_cvt_pk_bf16_f32 v119, v162, v163\n"
    "v_mov_b32 v156, v168\n"
    "s_nop 1\n"
    "v_permlane16_swap_b32 v168, v156\n"
    "v_add_f32 v168, v168, v156\n"
    "v_mov_b32 v156, v168\n"
    "s_nop 1\n"
    "v_permlane32_swap_b32 v168, v156\n"
    "v_add_f32 v168, v168, v156\n"
    "s_setpc_b64 s[68:69]\n"
    ".Lgqa_HH2:\n"
    "v_max3_f32 v214, 0, v228, v229\n"
    "v_max3_f32 v214, v214, v230, v231\n"
    "v_max3_f32 v214, v214, v232, v233\n"
    "v_max3_f32 v214, v214, v234, v235\n"
    "v_max3_f32 v214, v214, v16, v17\n"
    "v_max3_f32 v214, v214, v18, v19\n"
    "v_max3_f32 v214, v214, v20, v21\n"
    "v_max3_f32 v214, v214, v22, v23\n"
    "v_max3_f32 v214, v214, v64, v65\n"
    "v_max3_f32 v214, v214, v66, v67\n"
    "v_max3_f32 v214, v214, v68, v69\n"
    "v_max3_f32 v214, v214, v70, v71\n"
    "v_mov_b32 v156, v214\n"
    "s_nop 1\n"
    "v_permlane16_swap_b32 v214, v156\n"
    "v_max_f32 v214, v214, v156\n"
    "v_mov_b32 v156, v214\n"
    "s_nop 1\n"
    "v_permlane32_swap_b32 v214, v156\n"
    "v_max_f32 v214, v214, v156\n"
    "v_sub_f32 v220, v220, v214\n"
    "v_sub_f32 v221, v221, v214\n"
    "v_sub_f32 v222, v222, v214\n"
    "v_sub_f32 v223, v223, v214\n"
    "v_sub_f32 v224, v224, v214\n"
    "v_sub_f32 v225, v225, v214\n"
    "v_sub_f32 v226, v226, v214\n"
    "v_sub_f32 v227, v227, v214\n"
    "v_sub_f32 v228, v228, v214\n"
    "v_sub_f32 v229, v229, v214\n"
    "v_sub_f32 v230, v230, v214\n"
    "v_sub_f32 v231, v231, v214\n"
    "v_sub_f32 v232, v232, v214\n"
    "v_sub_f32 v233, v233, v214\n"
    "v_sub_f32 v234, v234, v214\n"
    "v_sub_f32 v235, v235, v214\n"
    "v_sub_f32 v16, v16, v214\n"
    "v_sub_f32 v17, v17, v214\n"
    "v_sub_f32 v18, v18, v214\n"
    "v_sub_f32 v19, v19, v214\n"
    "v_sub_f32 v20, v20, v214\n"
    "v_sub_f32 v21, v21, v214\n"
    "v_sub_f32 v22, v22, v214\n"
    "v_sub_f32 v23, v23, v214\n"
    "v_sub_f32 v64, v64, v214\n"
    "v_sub_f32 v65, v65, v214\n"
    "v_sub_f32 v66, v66, v214\n"
    "v_sub_f32 v67, v67, v214\n"
    "v_sub_f32 v68, v68, v214\n"
    "v_sub_f32 v69, v69, v214\n"
    "v_sub_f32 v70, v70, v214\n"
    "v_sub_f32 v71, v71, v214\n"
    "v_sub_f32 v140, v140, v214\n"
    "v_sub_f32 v141, v141, v214\n"
    "v_sub_f32 v142, v142, v214\n"
    "v_sub_f32 v143, v143, v214\n"
    "v_exp_f32 v156, v220\n"
    "v_exp_f32 v157, v221\n"
    "s_nop 0\n"
    "v_add_f32 v172, v156, v157\n"
    "v_cvt_pk_bf16_f32 v96, v156, v157\n"
    "v_exp_f32 v158, v222\n"
    "s_nop 0\n"
    "v_add_f32 v172, v172, v158\n"
    "v_exp_f32 v159, v223\n"
    "s_nop 0\n"
    "v_add_f32 v172, v172, v159\n"
    "v_cvt_pk_bf16_f32 v97, v158, v159\n"
    "v_exp_f32 v160, v224\n"
    "s_nop 0\n"
    "v_add_f32 v172, v172, v160\n"
    "v_exp_f32 v161, v225\n"
    "s_nop 0\n"
    "v_add_f32 v172, v172, v161\n"
    "v_cvt_pk_bf16_f32 v98, v160, v161\n"
    "v_exp_f32 v162, v226\n"
    "s_nop 0\n"
    "v_add_f32 v172, v172, v162\n"
    "v_exp_f32 v163, v227\n"
    "s_nop 0\n"
    "v_add_f32 v172, v172, v163\n"
    "v_cvt_pk_bf16_f32 v99, v162, v163\n"
    "v_exp_f32 v156, v228\n"
    "v_exp_f32 v157, v229\n"
    "v_add_f32 v172, v172, v156\n"
    "v_add_f32 v172, v172, v157\n"
    "v_cvt_pk_bf16_f32 v120, v156, v157\n"
    "v_exp_f32 v158, v230\n"
    "s_nop 0\n"
    "v_add_f32 v172, v172, v158\n"
    "v_exp_f32 v159, v231\n"
    "s_nop 0\n"
    "v_add_f32 v172, v172, v159\n"
    "v_cvt_pk_bf16_f32 v121, v158, v159\n"
    "v_exp_f32 v160, v232\n"
    "s_nop 0\n"
    "v_add_f32 v172, v172, v160\n"
    "v_exp_f32 v161, v233\n"
    "s_nop 0\n"
    "v_add_f32 v172, v172, v161\n"
    "v_cvt_pk_bf16_f32 v122, v160, v161\n"
    "v_exp_f32 v162, v234\n"
    "s_nop 0\n"
    "v_add_f32 v172, v172, v162\n"
    "v_exp_f32 v163, v235\n"
    "s_nop 0\n"
    "v_add_f32 v172, v172, v163\n"
    "v_cvt_pk_bf16_f32 v123, v162, v163\n"
    "s_setpc_b64 s[68:69]\n"
    ".Lgqa_HH3:\n"
    "v_max3_f32 v214, 0, v244, v245\n"
    "v_max3_f32 v214, v214, v246, v247\n"
    "v_max3_f32 v214, v214, v248, v249\n"
    "v_max3_f32 v214, v214, v250, v251\n"
    "v_max3_f32 v214, v214, v24, v25\n"
    "v_max3_f32 v214, v214, v26, v27\n"
    "v_max3_f32 v214, v214, v28, v29\n"
    "v_max3_f32 v214, v214, v30, v31\n"
    "v_max3_f32 v214, v214, v72, v73\n"
    "v_max3_f32 v214, v214, v74, v75\n"
    "v_max3_f32 v214, v214, v76, v77\n"
    "v_max3_f32 v214, v214, v78, v79\n"
    "v_mov_b32 v156, v214\n"
    "s_nop 1\n"
    "v_permlane16_swap_b32 v214, v156\n"
    "v_max_f32 v214, v214, v156\n"
    "v_mov_b32 v156, v214\n"
    "s_nop 1\n"
    "v_permlane32_swap_b32 v214, v156\n"
    "v_max_f32 v214, v214, v156\n"
    "v_sub_f32 v236, v236, v214\n"
    "v_sub_f32 v237, v237, v214\n"
    "v_sub_f32 v238, v238, v214\n"
    "v_sub_f32 v239, v239, v214\n"
    "v_sub_f32 v240, v240, v214\n"
    "v_sub_f32 v241, v241, v214\n"
    "v_sub_f32 v242, v242, v214\n"
    "v_sub_f32 v243, v243, v214\n"
    "v_sub_f32 v244, v244, v214\n"
    "v_sub_f32 v245, v245, v214\n"
    "v_sub_f32 v246, v246, v214\n"
    "v_sub_f32 v247, v247, v214\n"
    "v_sub_f32 v248, v248, v214\n"
    "v_sub_f32 v249, v249, v214\n"
    "v_sub_f32 v250, v250, v214\n"
    "v_sub_f32 v251, v251, v214\n"
    "v_sub_f32 v24, v24, v214\n"
    "v_sub_f32 v25, v25, v214\n"
    "v_sub_f32 v26, v26, v214\n"
    "v_sub_f32 v27, v27, v214\n"
    "v_sub_f32 v28, v28, v214\n"
    "v_sub_f32 v29, v29, v214\n"
    "v_sub_f32 v30, v30, v214\n"
    "v_sub_f32 v31, v31, v214\n"
    "v_sub_f32 v72, v72, v214\n"
    "v_sub_f32 v73, v73, v214\n"
    "v_sub_f32 v74, v74, v214\n"
    "v_sub_f32 v75, v75, v214\n"
    "v_sub_f32 v76, v76, v214\n"
    "v_sub_f32 v77, v77, v214\n"
    "v_sub_f32 v78, v78, v214\n"
    "v_sub_f32 v79, v79, v214\n"
    "v_sub_f32 v144, v144, v214\n"
    "v_sub_f32 v145, v145, v214\n"
    "v_sub_f32 v146, v146, v214\n"
    "v_sub_f32 v147, v147, v214\n"
    "v_exp_f32 v156, v236\n"
    "v_exp_f32 v157, v237\n"
    "s_nop 0\n"
    "v_add_f32 v173, v156, v157\n"
    "v_cvt_pk_bf16_f32 v100, v156, v157\n"
    "v_exp_f32 v158, v238\n"
    "s_nop 0\n"
    "v_add_f32 v173, v173, v158\n"
    "v_exp_f32 v159, v239\n"
    "s_nop 0\n"
    "v_add_f32 v173, v173, v159\n"
    "v_cvt_pk_bf16_f32 v101, v158, v159\n"
    "v_exp_f32 v160, v240\n"
    "s_nop 0\n"
    "v_add_f32 v173, v173, v160\n"
    "v_exp_f32 v161, v241\n"
    "s_nop 0\n"
    "v_add_f32 v173, v173, v161\n"
    "v_cvt_pk_bf16_f32 v102, v160, v161\n"
    "v_exp_f32 v162, v242\n"
    "s_nop 0\n"
    "v_add_f32 v173, v173, v162\n"
    "v_exp_f32 v163, v243\n"
    "s_nop 0\n"
    "v_add_f32 v173, v173, v163\n"
    "v_cvt_pk_bf16_f32 v103, v162, v163\n"
    "v_exp_f32 v156, v244\n"
    "v_exp_f32 v157, v245\n"
    "v_add_f32 v173, v173, v156\n"
    "v_add_f32 v173, v173, v157\n"
    "v_cvt_pk_bf16_f32 v124, v156, v157\n"
    "v_exp_f32 v158, v246\n"
    "s_nop 0\n"
    "v_add_f32 v173, v173, v158\n"
    "v_exp_f32 v159, v247\n"
    "s_nop 0\n"
    "v_add_f32 v173, v173, v159\n"
    "v_cvt_pk_bf16_f32 v125, v158, v159\n"
    "v_exp_f32 v160, v248\n"
    "s_nop 0\n"
    "v_add_f32 v173, v173, v160\n"
    "v_exp_f32 v161, v249\n"
    "s_nop 0\n"
    "v_add_f32 v173, v173, v161\n"
    "v_cvt_pk_bf16_f32 v126, v160, v161\n"
    "v_exp_f32 v162, v250\n"
    "s_nop 0\n"
    "v_add_f32 v173, v173, v162\n"
    "v_exp_f32 v163, v251\n"
    "s_nop 0\n"
    "v_add_f32 v173, v173, v163\n"
    "v_cvt_pk_bf16_f32 v127, v162, v163\n"
    "s_setpc_b64 s[68:69]\n"
    ".Lgqa_HT0_30:\n"
    "v_cmp_lt_f32 vcc, s64, v164\n"
    "s_nop 1\n"
    "s_cbranch_vccz .Lgqa_HT0_30_ret\n"
    "v_mov_b32 v175, v164\n"
    "v_max3_f32 v214, 0, v88, v89\n"
    "v_max3_f32 v214, v214, v90, v91\n"
    "v_max3_f32 v214, v214, v0, v1\n"
    "v_max3_f32 v214, v214, v2, v3\n"
    "v_max3_f32 v214, v214, v92, v93\n"
    "v_max3_f32 v214, v214, v94, v95\n"
    "v_max3_f32 v214, v214, v12, v13\n"
    "v_max3_f32 v214, v214, v14, v15\n"
    "v_mov_b32 v156, v214\n"
    "s_nop 1\n"
    "v_permlane16_swap_b32 v214, v156\n"
    "v_max_f32 v214, v214, v156\n"
    "v_mov_b32 v156, v214\n"
    "s_nop 1\n"
    "v_permlane32_swap_b32 v214, v156\n"
    "v_max_f32 v214, v214, v156\n"
    "v_min_f32 v157, 0x71800000, v175\n"
    "v_log_f32 v157, v157\n"
    "s_nop 0\n"
    "v_max_f32 v214, v214, v157\n"
    "v_sub_f32 v88, v88, v214\n"
    "v_sub_f32 v89, v89, v214\n"
    "v_sub_f32 v90, v90, v214\n"
    "v_sub_f32 v91, v91, v214\n"
    "v_sub_f32 v0, v0, v214\n"
    "v_sub_f32 v1, v1, v214\n"
    "v_sub_f32 v2, v2, v214\n"
    "v_sub_f32 v3, v3, v214\n"
    "v_sub_f32 v92, v92, v214\n"
    "v_sub_f32 v93, v93, v214\n"
    "v_sub_f32 v94, v94, v214\n"
    "v_sub_f32 v95, v95, v214\n"
    "v_sub_f32 v12, v12, v214\n"
    "v_sub_f32 v13, v13, v214\n"
    "v_sub_f32 v14, v14, v214\n"
    "v_sub_f32 v15, v15, v214\n"
    "v_exp_f32 v214, -v214\n"
    "v_exp_f32 v156, v88\n"
    "v_exp_f32 v157, v89\n"
    "s_nop 0\n"
    "v_add_f32 v166, v156, v157\n"
    "v_cvt_pk_bf16_f32 v132, v156, v157\n"
    "v_exp_f32 v158, v90\n"
    "s_nop 0\n"
    "v_add_f32 v166, v166, v158\n"
    "v_exp_f32 v159, v91\n"
    "s_nop 0\n"
    "v_add_f32 v166, v166, v159\n"
    "v_cvt_pk_bf16_f32 v133, v158, v159\n"
    "v_exp_f32 v160, v0\n"
    "s_nop 0\n"
    "v_add_f32 v166, v166, v160\n"
    "v_exp_f32 v161, v1\n"
    "s_nop 0\n"
    "v_add_f32 v166, v166, v161\n"
    "v_cvt_pk_bf16_f32 v134, v160, v161\n"
    "v_exp_f32 v162, v2\n"
    "s_nop 0\n"
    "v_add_f32 v166, v166, v162\n"
    "v_exp_f32 v163, v3\n"
    "s_nop 0\n"
    "v_add_f32 v166, v166, v163\n"
    "v_cvt_pk_bf16_f32 v135, v162, v163\n"
    "v_exp_f32 v156, v92\n"
    "v_exp_f32 v157, v93\n"
    "v_add_f32 v166, v166, v156\n"
    "v_add_f32 v166, v166, v157\n"
    "v_cvt_pk_bf16_f32 v136, v156, v157\n"
    "v_exp_f32 v158, v94\n"
    "s_nop 0\n"
    "v_add_f32 v166, v166, v158\n"
    "v_exp_f32 v159, v95\n"
    "s_nop 0\n"
    "v_add_f32 v166, v166, v159\n"
    "v_cvt_pk_bf16_f32 v137, v158, v159\n"
    "v_exp_f32 v160, v12\n"
    "s_nop 0\n"
    "v_add_f32 v166, v166, v160\n"
    "v_exp_f32 v161, v13\n"
    "s_nop 0\n"
    "v_add_f32 v166, v166, v161\n"
    "v_cvt_pk_bf16_f32 v138, v160, v161\n"
    "v_exp_f32 v162, v14\n"
    "s_nop 0\n"
    "v_add_f32 v166, v166, v162\n"
    "v_exp_f32 v163, v15\n"
    "s_nop 0\n"
    "v_add_f32 v166, v166, v163\n"
    "v_cvt_pk_bf16_f32 v139, v162, v163\n"
    "v_lshlrev_b32 v156, 16, v4\n"
    "v_and_b32 v157, 0xffff0000, v4\n"
    "v_lshlrev_b32 v158, 16, v5\n"
    "v_and_b32 v159, 0xffff0000, v5\n"
    "v_lshlrev_b32 v160, 16, v6\n"
    "v_and_b32 v161, 0xffff0000, v6\n"
    "v_lshlrev_b32 v162, 16, v7\n"
    "v_and_b32 v163, 0xffff0000, v7\n"
    "v_mul_f32 v156, v156, v214\n"
    "v_mul_f32 v157, v157, v214\n"
    "v_mul_f32 v158, v158, v214\n"
    "v_mul_f32 v159, v159, v214\n"
    "v_mul_f32 v160, v160, v214\n"
    "v_mul_f32 v161, v161, v214\n"
    "v_mul_f32 v162, v162, v214\n"
    "v_mul_f32 v163, v163, v214\n"
    "v_cvt_pk_bf16_f32 v4, v156, v157\n"
    "v_cvt_pk_bf16_f32 v5, v158, v159\n"
    "v_cvt_pk_bf16_f32 v6, v160, v161\n"
    "v_cvt_pk_bf16_f32 v7, v162, v163\n"
    "v_lshlrev_b32 v156, 16, v8\n"
    "v_and_b32 v157, 0xffff0000, v8\n"
    "v_lshlrev_b32 v158, 16, v9\n"
    "v_and_b32 v159, 0xffff0000, v9\n"
    "v_lshlrev_b32 v160, 16, v10\n"
    "v_and_b32 v161, 0xffff0000, v10\n"
    "v_lshlrev_b32 v162, 16, v11\n"
    "v_and_b32 v163, 0xffff0000, v11\n"
    "v_mul_f32 v156, v156, v214\n"
    "v_mul_f32 v157, v157, v214\n"
    "v_mul_f32 v158, v158, v214\n"
    "v_mul_f32 v159, v159, v214\n"
    "v_mul_f32 v160, v160, v214\n"
    "v_mul_f32 v161, v161, v214\n"
    "v_mul_f32 v162, v162, v214\n"
    "v_mul_f32 v163, v163, v214\n"
    "v_cvt_pk_bf16_f32 v8, v156, v157\n"
    "v_cvt_pk_bf16_f32 v9, v158, v159\n"
    "v_cvt_pk_bf16_f32 v10, v160, v161\n"
    "v_cvt_pk_bf16_f32 v11, v162, v163\n"
    "v_mov_b32 v156, v166\n"
    "s_nop 1\n"
    "v_permlane16_swap_b32 v166, v156\n"
    "v_add_f32 v166, v166, v156\n"
    "v_mov_b32 v156, v166\n"
    "s_nop 1\n"
    "v_permlane32_swap_b32 v166, v156\n"
    "v_add_f32 v166, v166, v156\n"
    "v_cmp_lt_f32 vcc, 0x71800000, v175\n"
    "v_mul_f32 v164, v164, v214\n"
    "s_nop 0\n"
    "v_cndmask_b32 v164, v164, v166, vcc\n"
    "s_call_b64 s[70:71], .Lgqa_RS0\n"
    ".Lgqa_HT0_30_ret:\n"
    "s_setpc_b64 s[68:69]\n"
    ".Lgqa_HT1_30:\n"
    "v_cmp_lt_f32 vcc, s64, v168\n"
    "s_nop 1\n"
    "s_cbranch_vccz .Lgqa_HT1_30_ret\n"
    "v_mov_b32 v175, v168\n"
    "v_max3_f32 v214, 0, v112, v113\n"
    "v_max3_f32 v214, v214, v114, v115\n"
    "v_max3_f32 v214, v214, v48, v49\n"
    "v_max3_f32 v214, v214, v50, v51\n"
    "v_max3_f32 v214, v214, v116, v117\n"
    "v_max3_f32 v214, v214, v118, v119\n"
    "v_max3_f32 v214, v214, v60, v61\n"
    "v_max3_f32 v214, v214, v62, v63\n"
    "v_mov_b32 v156, v214\n"
    "s_nop 1\n"
    "v_permlane16_swap_b32 v214, v156\n"
    "v_max_f32 v214, v214, v156\n"
    "v_mov_b32 v156, v214\n"
    "s_nop 1\n"
    "v_permlane32_swap_b32 v214, v156\n"
    "v_max_f32 v214, v214, v156\n"
    "v_min_f32 v157, 0x71800000, v175\n"
    "v_log_f32 v157, v157\n"
    "s_nop 0\n"
    "v_max_f32 v214, v214, v157\n"
    "v_sub_f32 v112, v112, v214\n"
    "v_sub_f32 v113, v113, v214\n"
    "v_sub_f32 v114, v114, v214\n"
    "v_sub_f32 v115, v115, v214\n"
    "v_sub_f32 v48, v48, v214\n"
    "v_sub_f32 v49, v49, v214\n"
    "v_sub_f32 v50, v50, v214\n"
    "v_sub_f32 v51, v51, v214\n"
    "v_sub_f32 v116, v116, v214\n"
    "v_sub_f32 v117, v117, v214\n"
    "v_sub_f32 v118, v118, v214\n"
    "v_sub_f32 v119, v119, v214\n"
    "v_sub_f32 v60, v60, v214\n"
    "v_sub_f32 v61, v61, v214\n"
    "v_sub_f32 v62, v62, v214\n"
    "v_sub_f32 v63, v63, v214\n"
    "v_exp_f32 v214, -v214\n"
    "v_exp_f32 v156, v112\n"
    "v_exp_f32 v157, v113\n"
    "s_nop 0\n"
    "v_add_f32 v166, v156, v157\n"
    "v_cvt_pk_bf16_f32 v88, v156, v157\n"
    "v_exp_f32 v158, v114\n"
    "s_nop 0\n"
    "v_add_f32 v166, v166, v158\n"
    "v_exp_f32 v159, v115\n"
    "s_nop 0\n"
    "v_add_f32 v166, v166, v159\n"
    "v_cvt_pk_bf16_f32 v89, v158, v159\n"
    "v_exp_f32 v160, v48\n"
    "s_nop 0\n"
    "v_add_f32 v166, v166, v160\n"
    "v_exp_f32 v161, v49\n"
    "s_nop 0\n"
    "v_add_f32 v166, v166, v161\n"
    "v_cvt_pk_bf16_f32 v90, v160, v161\n"
    "v_exp_f32 v162, v50\n"
    "s_nop 0\n"
    "v_add_f32 v166, v166, v162\n"
    "v_exp_f32 v163, v51\n"
    "s_nop 0\n"
    "v_add_f32 v166, v166, v163\n"
    "v_cvt_pk_bf16_f32 v91, v162, v163\n"
    "v_exp_f32 v156, v116\n"
    "v_exp_f32 v157, v117\n"
    "v_add_f32 v166, v166, v156\n"
    "v_add_f32 v166, v166, v157\n"
    "v_cvt_pk_bf16_f32 v92, v156, v157\n"
    "v_exp_f32 v158, v118\n"
    "s_nop 0\n"
    "v_add_f32 v166, v166, v158\n"
    "v_exp_f32 v159, v119\n"
    "s_nop 0\n"
    "v_add_f32 v166, v166, v159\n"
    "v_cvt_pk_bf16_f32 v93, v158, v159\n"
    "v_exp_f32 v160, v60\n"
    "s_nop 0\n"
    "v_add_f32 v166, v166, v160\n"
    "v_exp_f32 v161, v61\n"
    "s_nop 0\n"
    "v_add_f32 v166, v166, v161\n"
    "v_cvt_pk_bf16_f32 v94, v160, v161\n"
    "v_exp_f32 v162, v62\n"
    "s_nop 0\n"
    "v_add_f32 v166, v166, v162\n"
    "v_exp_f32 v163, v63\n"
    "s_nop 0\n"
    "v_add_f32 v166, v166, v163\n"
    "v_cvt_pk_bf16_f32 v95, v162, v163\n"
    "v_lshlrev_b32 v156, 16, v52\n"
    "v_and_b32 v157, 0xffff0000, v52\n"
    "v_lshlrev_b32 v158, 16, v53\n"
    "v_and_b32 v159, 0xffff0000, v53\n"
    "v_lshlrev_b32 v160, 16, v54\n"
    "v_and_b32 v161, 0xffff0000, v54\n"
    "v_lshlrev_b32 v162, 16, v55\n"
    "v_and_b32 v163, 0xffff0000, v55\n"
    "v_mul_f32 v156, v156, v214\n"
    "v_mul_f32 v157, v157, v214\n"
    "v_mul_f32 v158, v158, v214\n"
    "v_mul_f32 v159, v159, v214\n"
    "v_mul_f32 v160, v160, v214\n"
    "v_mul_f32 v161, v161, v214\n"
    "v_mul_f32 v162, v162, v214\n"
    "v_mul_f32 v163, v163, v214\n"
    "v_cvt_pk_bf16_f32 v52, v156, v157\n"
    "v_cvt_pk_bf16_f32 v53, v158, v159\n"
    "v_cvt_pk_bf16_f32 v54, v160, v161\n"
    "v_cvt_pk_bf16_f32 v55, v162, v163\n"
    "v_lshlrev_b32 v156, 16, v56\n"
    "v_and_b32 v157, 0xffff0000, v56\n"
    "v_lshlrev_b32 v158, 16, v57\n"
    "v_and_b32 v159, 0xffff0000, v57\n"
    "v_lshlrev_b32 v160, 16, v58\n"
    "v_and_b32 v161, 0xffff0000, v58\n"
    "v_lshlrev_b32 v162, 16, v59\n"
    "v_and_b32 v163, 0xffff0000, v59\n"
    "v_mul_f32 v156, v156, v214\n"
    "v_mul_f32 v157, v157, v214\n"
    "v_mul_f32 v158, v158, v214\n"
    "v_mul_f32 v159, v159, v214\n"
    "v_mul_f32 v160, v160, v214\n"
    "v_mul_f32 v161, v161, v214\n"
    "v_mul_f32 v162, v162, v214\n"
    "v_mul_f32 v163, v163, v214\n"
    "v_cvt_pk_bf16_f32 v56, v156, v157\n"
    "v_cvt_pk_bf16_f32 v57, v158, v159\n"
    "v_cvt_pk_bf16_f32 v58, v160, v161\n"
    "v_cvt_pk_bf16_f32 v59, v162, v163\n"
    "v_mov_b32 v156, v166\n"
    "s_nop 1\n"
    "v_permlane16_swap_b32 v166, v156\n"
    "v_add_f32 v166, v166, v156\n"
    "v_mov_b32 v156, v166\n"
    "s_nop 1\n"
    "v_permlane32_swap_b32 v166, v156\n"
    "v_add_f32 v166, v166, v156\n"
    "v_cmp_lt_f32 vcc, 0x71800000, v175\n"
    "v_mul_f32 v168, v168, v214\n"
    "s_nop 0\n"
    "v_cndmask_b32 v168, v168, v166, vcc\n"
    "s_call_b64 s[70:71], .Lgqa_RS1\n"
    ".Lgqa_HT1_30_ret:\n"
    "s_setpc_b64 s[68:69]\n"
    ".Lgqa_HT2_30:\n"
    "v_cmp_lt_f32 vcc, s64, v172\n"
    "s_nop 1\n"
    "s_cbranch_vccz .Lgqa_HT2_30_ret\n"
    "v_mov_b32 v175, v172\n"
    "v_mov_b32 v156, v175\n"
    "s_nop 1\n"
    "v_permlane16_swap_b32 v175, v156\n"
    "v_max_f32 v175, v175, v156\n"
    "v_mov_b32 v156, v175\n"
    "s_nop 1\n"
    "v_permlane32_swap_b32 v175, v156\n"
    "v_max_f32 v175, v175, v156\n"
    "v_max3_f32 v214, 0, v96, v97\n"
    "v_max3_f32 v214, v214, v98, v99\n"
    "v_max3_f32 v214, v214, v20, v21\n"
    "v_max3_f32 v214, v214, v22, v23\n"
    "v_max3_f32 v214, v214, v120, v121\n"
    "v_max3_f32 v214, v214, v122, v123\n"
    "v_max3_f32 v214, v214, v68, v69\n"
    "v_max3_f32 v214, v214, v70, v71\n"
    "v_mov_b32 v156, v214\n"
    "s_nop 1\n"
    "v_permlane16_swap_b32 v214, v156\n"
    "v_max_f32 v214, v214, v156\n"
    "v_mov_b32 v156, v214\n"
    "s_nop 1\n"
    "v_permlane32_swap_b32 v214, v156\n"
    "v_max_f32 v214, v214, v156\n"
    "v_min_f32 v157, 0x71800000, v175\n"
    "v_log_f32 v157, v157\n"
    "s_nop 0\n"
    "v_max_f32 v214, v214, v157\n"
    "v_sub_f32 v96, v96, v214\n"
    "v_sub_f32 v97, v97, v214\n"
    "v_sub_f32 v98, v98, v214\n"
    "v_sub_f32 v99, v99, v214\n"
    "v_sub_f32 v20, v20, v214\n"
    "v_sub_f32 v21, v21, v214\n"
    "v_sub_f32 v22, v22, v214\n"
    "v_sub_f32 v23, v23, v214\n"
    "v_sub_f32 v120, v120, v214\n"
    "v_sub_f32 v121, v121, v214\n"
    "v_sub_f32 v122, v122, v214\n"
    "v_sub_f32 v123, v123, v214\n"
    "v_sub_f32 v68, v68, v214\n"
    "v_sub_f32 v69, v69, v214\n"
    "v_sub_f32 v70, v70, v214\n"
    "v_sub_f32 v71, v71, v214\n"
    "v_exp_f32 v214, -v214\n"
    "v_exp_f32 v156, v96\n"
    "v_exp_f32 v157, v97\n"
    "s_nop 0\n"
    "v_add_f32 v166, v156, v157\n"
    "v_cvt_pk_bf16_f32 v112, v156, v157\n"
    "v_exp_f32 v158, v98\n"
    "s_nop 0\n"
    "v_add_f32 v166, v166, v158\n"
    "v_exp_f32 v159, v99\n"
    "s_nop 0\n"
    "v_add_f32 v166, v166, v159\n"
    "v_cvt_pk_bf16_f32 v113, v158, v159\n"
    "v_exp_f32 v160, v20\n"
    "s_nop 0\n"
    "v_add_f32 v166, v166, v160\n"
    "v_exp_f32 v161, v21\n"
    "s_nop 0\n"
    "v_add_f32 v166, v166, v161\n"
    "v_cvt_pk_bf16_f32 v114, v160, v161\n"
    "v_exp_f32 v162, v22\n"
    "s_nop 0\n"
    "v_add_f32 v166, v166, v162\n"
    "v_exp_f32 v163, v23\n"
    "s_nop 0\n"
    "v_add_f32 v166, v166, v163\n"
    "v_cvt_pk_bf16_f32 v115, v162, v163\n"
    "v_exp_f32 v156, v120\n"
    "v_exp_f32 v157, v121\n"
    "v_add_f32 v166, v166, v156\n"
    "v_add_f32 v166, v166, v157\n"
    "v_cvt_pk_bf16_f32 v116, v156, v157\n"
    "v_exp_f32 v158, v122\n"
    "s_nop 0\n"
    "v_add_f32 v166, v166, v158\n"
    "v_exp_f32 v159, v123\n"
    "s_nop 0\n"
    "v_add_f32 v166, v166, v159\n"
    "v_cvt_pk_bf16_f32 v117, v158, v159\n"
    "v_exp_f32 v160, v68\n"
    "s_nop 0\n"
    "v_add_f32 v166, v166, v160\n"
    "v_exp_f32 v161, v69\n"
    "s_nop 0\n"
    "v_add_f32 v166, v166, v161\n"
    "v_cvt_pk_bf16_f32 v118, v160, v161\n"
    "v_exp_f32 v162, v70\n"
    "s_nop 0\n"
    "v_add_f32 v166, v166, v162\n"
    "v_exp_f32 v163, v71\n"
    "s_nop 0\n"
    "v_add_f32 v166, v166, v163\n"
    "v_cvt_pk_bf16_f32 v119, v162, v163\n"
    "v_lshlrev_b32 v156, 16, v16\n"
    "v_and_b32 v157, 0xffff0000, v16\n"
    "v_lshlrev_b32 v158, 16, v17\n"
    "v_and_b32 v159, 0xffff0000, v17\n"
    "v_lshlrev_b32 v160, 16, v18\n"
    "v_and_b32 v161, 0xffff0000, v18\n"
    "v_lshlrev_b32 v162, 16, v19\n"
    "v_and_b32 v163, 0xffff0000, v19\n"
    "v_mul_f32 v156, v156, v214\n"
    "v_mul_f32 v157, v157, v214\n"
    "v_mul_f32 v158, v158, v214\n"
    "v_mul_f32 v159, v159, v214\n"
    "v_mul_f32 v160, v160, v214\n"
    "v_mul_f32 v161, v161, v214\n"
    "v_mul_f32 v162, v162, v214\n"
    "v_mul_f32 v163, v163, v214\n"
    "v_cvt_pk_bf16_f32 v16, v156, v157\n"
    "v_cvt_pk_bf16_f32 v17, v158, v159\n"
    "v_cvt_pk_bf16_f32 v18, v160, v161\n"
    "v_cvt_pk_bf16_f32 v19, v162, v163\n"
    "v_lshlrev_b32 v156, 16, v64\n"
    "v_and_b32 v157, 0xffff0000, v64\n"
    "v_lshlrev_b32 v158, 16, v65\n"
    "v_and_b32 v159, 0xffff0000, v65\n"
    "v_lshlrev_b32 v160, 16, v66\n"
    "v_and_b32 v161, 0xffff0000, v66\n"
    "v_lshlrev_b32 v162, 16, v67\n"
    "v_and_b32 v163, 0xffff0000, v67\n"
    "v_mul_f32 v156, v156, v214\n"
    "v_mul_f32 v157, v157, v214\n"
    "v_mul_f32 v158, v158, v214\n"
    "v_mul_f32 v159, v159, v214\n"
    "v_mul_f32 v160, v160, v214\n"
    "v_mul_f32 v161, v161, v214\n"
    "v_mul_f32 v162, v162, v214\n"
    "v_mul_f32 v163, v163, v214\n"
    "v_cvt_pk_bf16_f32 v64, v156, v157\n"
    "v_cvt_pk_bf16_f32 v65, v158, v159\n"
    "v_cvt_pk_bf16_f32 v66, v160, v161\n"
    "v_cvt_pk_bf16_f32 v67, v162, v163\n"
    "v_cmp_lt_f32 vcc, 0x71800000, v175\n"
    "v_mul_f32 v172, v172, v214\n"
    "s_nop 0\n"
    "v_cndmask_b32 v172, v172, v166, vcc\n"
    "s_call_b64 s[70:71], .Lgqa_RS2\n"
    ".Lgqa_HT2_30_ret:\n"
    "s_setpc_b64 s[68:69]\n"
    ".Lgqa_HT3_30:\n"
    "v_cmp_lt_f32 vcc, s64, v173\n"
    "s_nop 1\n"
    "s_cbranch_vccz .Lgqa_HT3_30_ret\n"
    "v_mov_b32 v175, v173\n"
    "v_mov_b32 v156, v175\n"
    "s_nop 1\n"
    "v_permlane16_swap_b32 v175, v156\n"
    "v_max_f32 v175, v175, v156\n"
    "v_mov_b32 v156, v175\n"
    "s_nop 1\n"
    "v_permlane32_swap_b32 v175, v156\n"
    "v_max_f32 v175, v175, v156\n"
    "v_max3_f32 v214, 0, v100, v101\n"
    "v_max3_f32 v214, v214, v102, v103\n"
    "v_max3_f32 v214, v214, v28, v29\n"
    "v_max3_f32 v214, v214, v30, v31\n"
    "v_max3_f32 v214, v214, v124, v125\n"
    "v_max3_f32 v214, v214, v126, v127\n"
    "v_max3_f32 v214, v214, v76, v77\n"
    "v_max3_f32 v214, v214, v78, v79\n"
    "v_mov_b32 v156, v214\n"
    "s_nop 1\n"
    "v_permlane16_swap_b32 v214, v156\n"
    "v_max_f32 v214, v214, v156\n"
    "v_mov_b32 v156, v214\n"
    "s_nop 1\n"
    "v_permlane32_swap_b32 v214, v156\n"
    "v_max_f32 v214, v214, v156\n"
    "v_min_f32 v157, 0x71800000, v175\n"
    "v_log_f32 v157, v157\n"
    "s_nop 0\n"
    "v_max_f32 v214, v214, v157\n"
    "v_sub_f32 v100, v100, v214\n"
    "v_sub_f32 v101, v101, v214\n"
    "v_sub_f32 v102, v102, v214\n"
    "v_sub_f32 v103, v103, v214\n"
    "v_sub_f32 v28, v28, v214\n"
    "v_sub_f32 v29, v29, v214\n"
    "v_sub_f32 v30, v30, v214\n"
    "v_sub_f32 v31, v31, v214\n"
    "v_sub_f32 v124, v124, v214\n"
    "v_sub_f32 v125, v125, v214\n"
    "v_sub_f32 v126, v126, v214\n"
    "v_sub_f32 v127, v127, v214\n"
    "v_sub_f32 v76, v76, v214\n"
    "v_sub_f32 v77, v77, v214\n"
    "v_sub_f32 v78, v78, v214\n"
    "v_sub_f32 v79, v79, v214\n"
    "v_exp_f32 v214, -v214\n"
    "v_exp_f32 v156, v100\n"
    "v_exp_f32 v157, v101\n"
    "s_nop 0\n"
    "v_add_f32 v166, v156, v157\n"
    "v_cvt_pk_bf16_f32 v96, v156, v157\n"
    "v_exp_f32 v158, v102\n"
    "s_nop 0\n"
    "v_add_f32 v166, v166, v158\n"
    "v_exp_f32 v159, v103\n"
    "s_nop 0\n"
    "v_add_f32 v166, v166, v159\n"
    "v_cvt_pk_bf16_f32 v97, v158, v159\n"
    "v_exp_f32 v160, v28\n"
    "s_nop 0\n"
    "v_add_f32 v166, v166, v160\n"
    "v_exp_f32 v161, v29\n"
    "s_nop 0\n"
    "v_add_f32 v166, v166, v161\n"
    "v_cvt_pk_bf16_f32 v98, v160, v161\n"
    "v_exp_f32 v162, v30\n"
    "s_nop 0\n"
    "v_add_f32 v166, v166, v162\n"
    "v_exp_f32 v163, v31\n"
    "s_nop 0\n"
    "v_add_f32 v166, v166, v163\n"
    "v_cvt_pk_bf16_f32 v99, v162, v163\n"
    "v_exp_f32 v156, v124\n"
    "v_exp_f32 v157, v125\n"
    "v_add_f32 v166, v166, v156\n"
    "v_add_f32 v166, v166, v157\n"
    "v_cvt_pk_bf16_f32 v120, v156, v157\n"
    "v_exp_f32 v158, v126\n"
    "s_nop 0\n"
    "v_add_f32 v166, v166, v158\n"
    "v_exp_f32 v159, v127\n"
    "s_nop 0\n"
    "v_add_f32 v166, v166, v159\n"
    "v_cvt_pk_bf16_f32 v121, v158, v159\n"
    "v_exp_f32 v160, v76\n"
    "s_nop 0\n"
    "v_add_f32 v166, v166, v160\n"
    "v_exp_f32 v161, v77\n"
    "s_nop 0\n"
    "v_add_f32 v166, v166, v161\n"
    "v_cvt_pk_bf16_f32 v122, v160, v161\n"
    "v_exp_f32 v162, v78\n"
    "s_nop 0\n"
    "v_add_f32 v166, v166, v162\n"
    "v_exp_f32 v163, v79\n"
    "s_nop 0\n"
    "v_add_f32 v166, v166, v163\n"
    "v_cvt_pk_bf16_f32 v123, v162, v163\n"
    "v_lshlrev_b32 v156, 16, v24\n"
    "v_and_b32 v157, 0xffff0000, v24\n"
    "v_lshlrev_b32 v158, 16, v25\n"
    "v_and_b32 v159, 0xffff0000, v25\n"
    "v_lshlrev_b32 v160, 16, v26\n"
    "v_and_b32 v161, 0xffff0000, v26\n"
    "v_lshlrev_b32 v162, 16, v27\n"
    "v_and_b32 v163, 0xffff0000, v27\n"
    "v_mul_f32 v156, v156, v214\n"
    "v_mul_f32 v157, v157, v214\n"
    "v_mul_f32 v158, v158, v214\n"
    "v_mul_f32 v159, v159, v214\n"
    "v_mul_f32 v160, v160, v214\n"
    "v_mul_f32 v161, v161, v214\n"
    "v_mul_f32 v162, v162, v214\n"
    "v_mul_f32 v163, v163, v214\n"
    "v_cvt_pk_bf16_f32 v24, v156, v157\n"
    "v_cvt_pk_bf16_f32 v25, v158, v159\n"
    "v_cvt_pk_bf16_f32 v26, v160, v161\n"
    "v_cvt_pk_bf16_f32 v27, v162, v163\n"
    "v_lshlrev_b32 v156, 16, v72\n"
    "v_and_b32 v157, 0xffff0000, v72\n"
    "v_lshlrev_b32 v158, 16, v73\n"
    "v_and_b32 v159, 0xffff0000, v73\n"
    "v_lshlrev_b32 v160, 16, v74\n"
    "v_and_b32 v161, 0xffff0000, v74\n"
    "v_lshlrev_b32 v162, 16, v75\n"
    "v_and_b32 v163, 0xffff0000, v75\n"
    "v_mul_f32 v156, v156, v214\n"
    "v_mul_f32 v157, v157, v214\n"
    "v_mul_f32 v158, v158, v214\n"
    "v_mul_f32 v159, v159, v214\n"
    "v_mul_f32 v160, v160, v214\n"
    "v_mul_f32 v161, v161, v214\n"
    "v_mul_f32 v162, v162, v214\n"
    "v_mul_f32 v163, v163, v214\n"
    "v_cvt_pk_bf16_f32 v72, v156, v157\n"
    "v_cvt_pk_bf16_f32 v73, v158, v159\n"
    "v_cvt_pk_bf16_f32 v74, v160, v161\n"
    "v_cvt_pk_bf16_f32 v75, v162, v163\n"
    "v_cmp_lt_f32 vcc, 0x71800000, v175\n"
    "v_mul_f32 v173, v173, v214\n"
    "s_nop 0\n"
    "v_cndmask_b32 v173, v173, v166, vcc\n"
    "s_call_b64 s[70:71], .Lgqa_RS3\n"
    ".Lgqa_HT3_30_ret:\n"
    "s_setpc_b64 s[68:69]\n"
    ".Lgqa_HT4_30:\n"
    "v_cmp_lt_f32 vcc, s64, v174\n"
    "s_nop 1\n"
    "s_cbranch_vccz .Lgqa_HT4_30_ret\n"
    "v_mov_b32 v175, v174\n"
    "v_mov_b32 v156, v175\n"
    "s_nop 1\n"
    "v_permlane16_swap_b32 v175, v156\n"
    "v_max_f32 v175, v175, v156\n"
    "v_mov_b32 v156, v175\n"
    "s_nop 1\n"
    "v_permlane32_swap_b32 v175, v156\n"
    "v_max_f32 v175, v175, v156\n"
    "v_max3_f32 v214, 0, v104, v105\n"
    "v_max3_f32 v214, v214, v106, v107\n"
    "v_max3_f32 v214, v214, v36, v37\n"
    "v_max3_f32 v214, v214, v38, v39\n"
    "v_max3_f32 v214, v214, v128, v129\n"
    "v_max3_f32 v214, v214, v130, v131\n"
    "v_max3_f32 v214, v214, v84, v85\n"
    "v_max3_f32 v214, v214, v86, v87\n"
    "v_max3_f32 v214, v214, v52, v53\n"
    "v_max3_f32 v214, v214, v54, v55\n"
    "v_max3_f32 v214, v214, v48, v49\n"
    "v_max3_f32 v214, v214, v50, v51\n"
    "v_max3_f32 v214, v214, v56, v57\n"
    "v_max3_f32 v214, v214, v58, v59\n"
    "v_max3_f32 v214, v214, v60, v61\n"
    "v_max3_f32 v214, v214, v62, v63\n"
    "v_max3_f32 v214, v214, v16, v17\n"
    "v_max3_f32 v214, v214, v18, v19\n"
    "v_max3_f32 v214, v214, v20, v21\n"
    "v_max3_f32 v214, v214, v22, v23\n"
    "v_mov_b32 v156, v214\n"
    "s_nop 1\n"
    "v_permlane16_swap_b32 v214, v156\n"
    "v_max_f32 v214, v214, v156\n"
    "v_mov_b32 v156, v214\n"
    "s_nop 1\n"
    "v_permlane32_swap_b32 v214, v156\n"
    "v_max_f32 v214, v214, v156\n"
    "v_min_f32 v157, 0x71800000, v175\n"
    "v_log_f32 v157, v157\n"
    "s_nop 0\n"
    "v_max_f32 v214, v214, v157\n"
    "v_sub_f32 v104, v104, v214\n"
    "v_sub_f32 v105, v105, v214\n"
    "v_sub_f32 v106, v106, v214\n"
    "v_sub_f32 v107, v107, v214\n"
    "v_sub_f32 v36, v36, v214\n"
    "v_sub_f32 v37, v37, v214\n"
    "v_sub_f32 v38, v38, v214\n"
    "v_sub_f32 v39, v39, v214\n"
    "v_sub_f32 v128, v128, v214\n"
    "v_sub_f32 v129, v129, v214\n"
    "v_sub_f32 v130, v130, v214\n"
    "v_sub_f32 v131, v131, v214\n"
    "v_sub_f32 v84, v84, v214\n"
    "v_sub_f32 v85, v85, v214\n"
    "v_sub_f32 v86, v86, v214\n"
    "v_sub_f32 v87, v87, v214\n"
    "v_sub_f32 v52, v52, v214\n"
    "v_sub_f32 v53, v53, v214\n"
    "v_sub_f32 v54, v54, v214\n"
    "v_sub_f32 v55, v55, v214\n"
    "v_sub_f32 v48, v48, v214\n"
    "v_sub_f32 v49, v49, v214\n"
    "v_sub_f32 v50, v50, v214\n"
    "v_sub_f32 v51, v51, v214\n"
    "v_sub_f32 v56, v56, v214\n"
    "v_sub_f32 v57, v57, v214\n"
    "v_sub_f32 v58, v58, v214\n"
    "v_sub_f32 v59, v59, v214\n"
    "v_sub_f32 v60, v60, v214\n"
    "v_sub_f32 v61, v61, v214\n"
    "v_sub_f32 v62, v62, v214\n"
    "v_sub_f32 v63, v63, v214\n"
    "v_sub_f32 v16, v16, v214\n"
    "v_sub_f32 v17, v17, v214\n"
    "v_sub_f32 v18, v18, v214\n"
    "v_sub_f32 v19, v19, v214\n"
    "v_sub_f32 v20, v20, v214\n"
    "v_sub_f32 v21, v21, v214\n"
    "v_sub_f32 v22, v22, v214\n"
    "v_sub_f32 v23, v23, v214\n"
    "v_exp_f32 v214, -v214\n"
    "v_exp_f32 v156, v104\n"
    "v_exp_f32 v157, v105\n"
    "s_nop 0\n"
    "v_add_f32 v166, v156, v157\n"
    "v_cvt_pk_bf16_f32 v88, v156, v157\n"
    "v_exp_f32 v158, v106\n"
    "s_nop 0\n"
    "v_add_f32 v166, v166, v158\n"
    "v_exp_f32 v159, v107\n"
    "s_nop 0\n"
    "v_add_f32 v166, v166, v159\n"
    "v_cvt_pk_bf16_f32 v89, v158, v159\n"
    "v_exp_f32 v160, v36\n"
    "s_nop 0\n"
    "v_add_f32 v166, v166, v160\n"
    "v_exp_f32 v161, v37\n"
    "s_nop 0\n"
    "v_add_f32 v166, v166, v161\n"
    "v_cvt_pk_bf16_f32 v90, v160, v161\n"
    "v_exp_f32 v162, v38\n"
    "s_nop 0\n"
    "v_add_f32 v166, v166, v162\n"
    "v_exp_f32 v163, v39\n"
    "s_nop 0\n"
    "v_add_f32 v166, v166, v163\n"
    "v_cvt_pk_bf16_f32 v91, v162, v163\n"
    "v_exp_f32 v156, v128\n"
    "v_exp_f32 v157, v129\n"
    "v_add_f32 v166, v166, v156\n"
    "v_add_f32 v166, v166, v157\n"
    "v_cvt_pk_bf16_f32 v92, v156, v157\n"
    "v_exp_f32 v158, v130\n"
    "s_nop 0\n"
    "v_add_f32 v166, v166, v158\n"
    "v_exp_f32 v159, v131\n"
    "s_nop 0\n"
    "v_add_f32 v166, v166, v159\n"
    "v_cvt_pk_bf16_f32 v93, v158, v159\n"
    "v_exp_f32 v160, v84\n"
    "s_nop 0\n"
    "v_add_f32 v166, v166, v160\n"
    "v_exp_f32 v161, v85\n"
    "s_nop 0\n"
    "v_add_f32 v166, v166, v161\n"
    "v_cvt_pk_bf16_f32 v94, v160, v161\n"
    "v_exp_f32 v162, v86\n"
    "s_nop 0\n"
    "v_add_f32 v166, v166, v162\n"
    "v_exp_f32 v163, v87\n"
    "s_nop 0\n"
    "v_add_f32 v166, v166, v163\n"
    "v_cvt_pk_bf16_f32 v95, v162, v163\n"
    "v_exp_f32 v156, v52\n"
    "v_exp_f32 v157, v53\n"
    "v_add_f32 v166, v166, v156\n"
    "v_add_f32 v166, v166, v157\n"
    "v_cvt_pk_bf16_f32 v112, v156, v157\n"
    "v_exp_f32 v158, v54\n"
    "s_nop 0\n"
    "v_add_f32 v166, v166, v158\n"
    "v_exp_f32 v159, v55\n"
    "s_nop 0\n"
    "v_add_f32 v166, v166, v159\n"
    "v_cvt_pk_bf16_f32 v113, v158, v159\n"
    "v_exp_f32 v160, v48\n"
    "s_nop 0\n"
    "v_add_f32 v166, v166, v160\n"
    "v_exp_f32 v161, v49\n"
    "s_nop 0\n"
    "v_add_f32 v166, v166, v161\n"
    "v_cvt_pk_bf16_f32 v114, v160, v161\n"
    "v_exp_f32 v162, v50\n"
    "s_nop 0\n"
    "v_add_f32 v166, v166, v162\n"
    "v_exp_f32 v163, v51\n"
    "s_nop 0\n"
    "v_add_f32 v166, v166, v163\n"
    "v_cvt_pk_bf16_f32 v115, v162, v163\n"
    "v_exp_f32 v156, v56\n"
    "v_exp_f32 v157, v57\n"
    "v_add_f32 v166, v166, v156\n"
    "v_add_f32 v166, v166, v157\n"
    "v_cvt_pk_bf16_f32 v116, v156, v157\n"
    "v_exp_f32 v158, v58\n"
    "s_nop 0\n"
    "v_add_f32 v166, v166, v158\n"
    "v_exp_f32 v159, v59\n"
    "s_nop 0\n"
    "v_add_f32 v166, v166, v159\n"
    "v_cvt_pk_bf16_f32 v117, v158, v159\n"
    "v_exp_f32 v160, v60\n"
    "s_nop 0\n"
    "v_add_f32 v166, v166, v160\n"
    "v_exp_f32 v161, v61\n"
    "s_nop 0\n"
    "v_add_f32 v166, v166, v161\n"
    "v_cvt_pk_bf16_f32 v118, v160, v161\n"
    "v_exp_f32 v162, v62\n"
    "s_nop 0\n"
    "v_add_f32 v166, v166, v162\n"
    "v_exp_f32 v163, v63\n"
    "s_nop 0\n"
    "v_add_f32 v166, v166, v163\n"
    "v_cvt_pk_bf16_f32 v119, v162, v163\n"
    "v_exp_f32 v156, v16\n"
    "v_exp_f32 v157, v17\n"
    "v_add_f32 v166, v166, v156\n"
    "v_add_f32 v166, v166, v157\n"
    "v_cvt_pk_bf16_f32 v100, v156, v157\n"
    "v_exp_f32 v158, v18\n"
    "s_nop 0\n"
    "v_add_f32 v166, v166, v158\n"
    "v_exp_f32 v159, v19\n"
    "s_nop 0\n"
    "v_add_f32 v166, v166, v159\n"
    "v_cvt_pk_bf16_f32 v101, v158, v159\n"
    "v_exp_f32 v160, v20\n"
    "s_nop 0\n"
    "v_add_f32 v166, v166, v160\n"
    "v_exp_f32 v161, v21\n"
    "s_nop 0\n"
    "v_add_f32 v166, v166, v161\n"
    "v_cvt_pk_bf16_f32 v102, v160, v161\n"
    "v_exp_f32 v162, v22\n"
    "s_nop 0\n"
    "v_add_f32 v166, v166, v162\n"
    "v_exp_f32 v163, v23\n"
    "s_nop 0\n"
    "v_add_f32 v166, v166, v163\n"
    "v_cvt_pk_bf16_f32 v103, v162, v163\n"
    "v_cmp_lt_f32 vcc, 0x71800000, v175\n"
    "v_mul_f32 v174, v174, v214\n"
    "s_nop 0\n"
    "v_cndmask_b32 v174, v174, v166, vcc\n"
    "s_call_b64 s[70:71], .Lgqa_RS4\n"
    ".Lgqa_HT4_30_ret:\n"
    "s_setpc_b64 s[68:69]\n"
    :
    : "s"(__builtin_amdgcn_kernarg_segment_ptr()), "s"(blockIdx.x), "v"(threadIdx.x), "s"((unsigned)(size_t)lds)
    : "v1", "v2", "v3", "v4", "v5", "v6", "v7", "v8", "v9", "v10", "v11", "v12", "v13", "v14", "v15", "v16", "v17", "v18", "v19", "v20", "v21", "v22", "v23", "v24", "v25", "v26", "v27", "v28", "v29", "v30", "v31", "v32", "v33", "v34", "v35", "v36", "v37", "v38", "v39", "v40", "v41", "v42", "v43", "v44", "v45", "v46", "v47", "v48", "v49", "v50", "v51", "v52", "v53", "v54", "v55", "v56", "v57", "v58", "v59", "v60", "v61", "v62", "v63", "v64", "v65", "v66", "v67", "v68", "v69", "v70", "v71", "v72", "v73", "v74", "v75", "v76", "v77", "v78", "v79", "v80", "v81", "v82", "v83", "v84", "v85", "v86", "v87", "v88", "v89", "v90", "v91", "v92", "v93", "v94", "v95", "v96", "v97", "v98", "v99", "v100", "v101", "v102", "v103", "v104", "v105", "v106", "v107", "v108", "v109", "v110", "v111", "v112", "v113", "v114", "v115", "v116", "v117", "v118", "v119", "v120", "v121", "v122", "v123", "v124", "v125", "v126", "v127", "v128", "v129", "v130", "v131", "v132", "v133", "v134", "v135", "v136", "v137", "v138", "v139", "v140", "v141", "v142", "v143", "v144", "v145", "v146", "v147", "v148", "v149", "v150", "v151", "v152", "v153", "v154", "v155", "v156", "v157", "v158", "v159", "v160", "v161", "v162", "v163", "v164", "v165", "v166", "v167", "v168", "v169", "v170", "v171", "v172", "v173", "v174", "v175", "v176", "v177", "v178", "v179", "v180", "v181", "v182", "v183", "v184", "v185", "v186", "v187", "v188", "v189", "v190", "v191", "v192", "v193", "v194", "v195", "v196", "v197", "v198", "v199", "v200", "v201", "v202", "v203", "v204", "v205", "v206", "v207", "v208", "v209", "v210", "v211", "v212", "v213", "v214", "v215", "v216", "v217", "v218", "v219", "v220", "v221", "v222", "v223", "v224", "v225", "v226", "v227", "v228", "v229", "v230", "v231", "v232", "v233", "v234", "v235", "v236", "v237", "v238", "v239", "v240", "v241", "v242", "v243", "v244", "v245", "v246", "v247", "v248", "v249", "v250", "v251", "v252", "v253", "v254", "v255", "a0", "a1", "a2", "a3", "a4", "a5", "a6", "a7", "a8", "a9", "a10", "a11", "a12", "a13", "a14", "a15", "a16", "a17", "a18", "a19", "a20", "a21", "a22", "a23", "a24", "a25", "a26", "a27", "a28", "a29", "a30", "a31", "a32", "a33", "a34", "a35", "a36", "a37", "a38", "a39", "a40", "a41", "a42", "a43", "a44", "a45", "a46", "a47", "a48", "a49", "a50", "a51", "a52", "a53", "a54", "a55", "a56", "a57", "a58", "a59", "a60", "a61", "a62", "a63", "a64", "a65", "a66", "a67", "a68", "a69", "a70", "a71", "a72", "a73", "a74", "a75", "a76", "a77", "a78", "a79", "a80", "a81", "a82", "a83", "a84", "a85", "a86", "a87", "a88", "a89", "a90", "a91", "a92", "a93", "a94", "a95", "a96", "a97", "a98", "a99", "a100", "a101", "a102", "a103", "a104", "a105", "a106", "a107", "a108", "a109", "a110", "a111", "a112", "a113", "a114", "a115", "a116", "a117", "a118", "a119", "a120", "a121", "a122", "a123", "a124", "a125", "a126", "a127", "a128", "a129", "a130", "a131", "a132", "a133", "a134", "a135", "a136", "a137", "a138", "a139", "a140", "a141", "a142", "a143", "a144", "a145", "a146", "a147", "a148", "a149", "a150", "a151", "a152", "a153", "a154", "a155", "a156", "a157", "a158", "a159", "a160", "a161", "a162", "a163", "a164", "a165", "a166", "a167", "a168", "a169", "a170", "a171", "a172", "a173", "a174", "a175", "a176", "a177", "a178", "a179", "a180", "a181", "a182", "a183", "a184", "a185", "a186", "a187", "a188", "a189", "a190", "a191", "a192", "a193", "a194", "a195", "a196", "a197", "a198", "a199", "a200", "a201", "a202", "a203", "a204", "a205", "a206", "a207", "a208", "a209", "a210", "a211", "a212", "a213", "a214", "a215", "a216", "a217", "a218", "a219", "a220", "a221", "a222", "a223", "a224", "a225", "a226", "a227", "a228", "a229", "a230", "a231", "a232", "a233", "a234", "a235", "a236", "a237", "a238", "a239", "a240", "a241", "a242", "a243", "a244", "a245", "a246", "a247", "a248", "a249", "a250", "a251", "a252", "a253", "a254", "a255", "s8", "s9", "s10", "s11", "s12", "s13", "s14", "s15", "s16", "s17", "s18", "s19", "s20", "s21", "s22", "s23", "s24", "s25", "s26", "s27", "s28", "s29", "s30", "s31", "s32", "s33", "s34", "s35", "s36", "s37", "s38", "s39", "s40", "s41", "s42", "s43", "s44", "s45", "s46", "s47", "s48", "s49", "s50", "s51", "s52", "s53", "s54", "s55", "s56", "s57", "s58", "s59", "s60", "s61", "s62", "s63", "s64", "s65", "s66", "s67", "s68", "s69", "s70", "s71", "s72", "s73", "s74", "s75", "s76", "s77", "s78", "s79", "s80", "s81", "s82", "s83", "s84", "s85", "s86", "s87", "s88", "s89", "s90", "s91", "s92", "s93", "s94", "s95", "s96", "s97", "s98", "s99", "vcc", "scc", "memory");
}

extern "C" void kernel_launch(void* const* d_in, const int* in_sizes, int n_in, void* d_out, int out_size, void* d_ws, size_t ws_size,
                              hipStream_t stream) {
  (void)in_sizes; (void)n_in; (void)out_size; (void)ws_size;
  hipLaunchKernelGGL(gqa_attn_fwd, dim3(256), dim3(256), 0, stream, (const char*)d_in[0], (const char*)d_in[1], (const char*)d_in[2], (char*)d_out,
                     (char*)d_ws);
}
```
